```python
import torch

def _softmax(x, axis=-1):
    return torch.softmax(x, dim=axis)
def _gelu(x):
    return torch.nn.functional.gelu(x, approximate="tanh")
def _silu(x):
    return torch.nn.functional.silu(x)
def _sigmoid(x):
    return torch.sigmoid(x)
def _layernorm(x, axis=-1, eps=1e-5):
    m = x.mean(dim=axis, keepdim=True)
    v = x.var(dim=axis, keepdim=True, unbiased=False)
    return (x - m) / torch.sqrt(v + eps)
def _scatter_add(tgt, idx, src, axis=0):
    return tgt.clone().index_add_(axis, idx, src)
def _segment_sum(src, seg, n):
    out = torch.zeros((n,) + src.shape[1:], dtype=src.dtype)
    return out.index_add_(0, seg, src)
def _pad(x, pw):
    flat = tuple(v for pair in reversed(pw) for v in pair)
    return torch.nn.functional.pad(x, flat)

kernel_name = 'gated_mix__ssm_conv_s4d__linear_kernel_hedgehog__prompt'

def setup_inputs():

    _w = (lambda a: (lambda t: t.to(torch.bfloat16) if t.is_floating_point() else t)(torch.as_tensor(a)))
    def _A_setup():
        Bsz, L, D, N = 1, 2048, 512, 128
        return {
            "u": _w((lambda *s: torch.randn(*s, dtype=torch.bfloat16) * 0.05)(Bsz, L, D)),
            "log_A_real": _w((lambda *s: torch.randn(*s, dtype=torch.bfloat16) * 3.2)(D, N) * 0.5),
            "A_imag": _w((lambda *s: torch.randn(*s, dtype=torch.bfloat16) * 0.05)(D, N) * 3.14),
            "C_real": _w((lambda *s: torch.randn(*s, dtype=torch.bfloat16) * 0.4)(D, N) / N ** 0.5),
            "C_imag": _w((lambda *s: torch.randn(*s, dtype=torch.bfloat16) * 0.4)(D, N) / N ** 0.5),
            "log_dt": _w((lambda *s: torch.randn(*s, dtype=torch.bfloat16) * 0.05)(D) * 0.5 - 4.0),
            "D_skip": _w((lambda *s: torch.randn(*s, dtype=torch.bfloat16) * 0.05)(D)),
        }
    _A_in = _A_setup()

    def _B_setup():
        B, S, D, H = 1, 2048, 512, 32
        Dh = D // H
        return {
            "x": _w((lambda *s: torch.randn(*s, dtype=torch.bfloat16) * 0.05)(B, S, D)),
            "wqkv": _w((lambda *s: torch.randn(*s, dtype=torch.bfloat16) * 0.8)(D, 3 * D)),
            "w_phi": _w((lambda *s: torch.randn(*s, dtype=torch.bfloat16) * 0.8)(H, Dh, Dh) * (Dh ** -0.5)),
            "wo": _w((lambda *s: torch.randn(*s, dtype=torch.bfloat16) * 0.05)(D, D)),
        }
    _B_in = _B_setup()

    return {
        "A_u": _A_in["u"],
        "A_log_A_real": _A_in["log_A_real"],
        "A_A_imag": _A_in["A_imag"],
        "A_C_real": _A_in["C_real"],
        "A_C_imag": _A_in["C_imag"],
        "A_log_dt": _A_in["log_dt"],
        "A_D_skip": _A_in["D_skip"],
        "B_x": _B_in["x"],
        "B_wqkv": _B_in["wqkv"],
        "B_w_phi": _B_in["w_phi"],
        "B_wo": _B_in["wo"],
        "gate_w": _w((lambda *s: torch.randn(*s, dtype=torch.bfloat16) * 0.05)(512) * 16.0),
    }

def reference(A_u, A_log_A_real, A_A_imag, A_C_real, A_C_imag, A_log_dt, A_D_skip, B_x, B_wqkv, B_w_phi, B_wo, gate_w):
    def _A_ref(u, log_A_real, A_imag, C_real, C_imag, log_dt, D_skip):
        L = u.shape[1]
        dt = torch.exp(log_dt)
        A = -torch.exp(log_A_real) + 1j * A_imag
        C = (C_real + 1j * C_imag) * dt[:, None]
        pos = (lambda x, dt: x.to(dt))(torch.arange(L), log_dt.dtype)
        P = torch.exp((A * dt[:, None])[:, :, None] * pos[None, None, :])
        K = 2.0 * torch.einsum("dn,dnl->dl", C, P).real
        L2 = 2 * L
        u_t = (lambda x, *d: x.permute(*d))(u, 0, 2, 1)
        Kf = (lambda x, *a, **k: torch.fft.rfft(x.float(), *a, **k))(K, L2)
        Uf = (lambda x, *a, **k: torch.fft.rfft(x.float(), *a, **k))(u_t, L2)
        y = (lambda x, *a, **k: torch.fft.irfft(x, *a, **k).to(torch.bfloat16))(Uf * Kf[None, :, :], L2)[:, :, :L]
        y = (lambda x, *d: x.permute(*d))(y, 0, 2, 1)
        return y + u * D_skip[None, None, :]

    def _B_ref(x, wqkv, w_phi, wo):
        B, S, D = x.shape
        H = 32; Dh = D // H
        qkv = (lambda x, *d: x.permute(*d))((x @ wqkv).reshape(B, S, 3, H, Dh), 2, 0, 3, 1, 4)
        q, k, v = qkv[0], qkv[1], qkv[2]
        pq = torch.einsum("bhsd,hdf->bhsf", q, w_phi)
        pk = torch.einsum("bhsd,hdf->bhsf", k, w_phi)
        phi_q = torch.concatenate([_softmax(pq, axis=-1), _softmax(-pq, axis=-1)], axis=-1)
        phi_k = torch.concatenate([_softmax(pk, axis=-1), _softmax(-pk, axis=-1)], axis=-1)
        kv = torch.einsum("bhsf,bhsd->bhfd", phi_k, v)
        z  = torch.einsum("bhsf->bhf", phi_k)
        num = torch.einsum("bhqf,bhfd->bhqd", phi_q, kv)
        den = torch.einsum("bhqf,bhf->bhq", phi_q, z)[..., None] + 1e-6
        o = num / den
        return (lambda x, *d: x.permute(*d))(o, 0, 2, 1, 3).reshape(B, S, D) @ wo

    x = A_u
    g = _sigmoid((x * gate_w).sum(-1, keepdims=True))
    a = _A_ref(x, A_log_A_real, A_A_imag, A_C_real, A_C_imag, A_log_dt, A_D_skip)
    b = _B_ref(x, B_wqkv, B_w_phi, B_wo)
    return g * a + (1 - g) * b
```

```cpp
#include <hip/hip_runtime.h>
#include <hip/hip_bf16.h>
#include <stdint.h>
#define NEMPTY 0
typedef unsigned short u16;
static __device__ __forceinline__ float b2f(u16 x) { return __uint_as_float(((unsigned)x) << 16); }
static __device__ __forceinline__ u16 f2b(float f) { __hip_bfloat16 h = __float2bfloat16(f); return *(u16*)&h; }
static __device__ __forceinline__ float rb(float f) { return b2f(f2b(f)); }
constexpr int L = 2048, D = 512, N = 128, H = 32;

__global__ void k_empty(float* p) { if (p == nullptr) p[0] = 0; }

__global__ void k_gate(const u16* u, const u16* gw, float* g) {
    int l = blockIdx.x; int t = threadIdx.x;
    float s = 0;
    for (int d = t; d < D; d += 64) s += rb(b2f(u[l * D + d]) * b2f(gw[d]));
    for (int o = 32; o; o >>= 1) s += __shfl_xor(s, o);
    if (t == 0) { float x = rb(s); g[l] = rb(1.f / (1.f + expf(-x))); }
}
__global__ void k_kgen(const u16* lar, const u16* aim, const u16* cre, const u16* cim, const u16* ldt, float* K) {
    int d = blockIdx.x;
    __shared__ float zr[N], zi[N], cr[N], ci[N];
    float dt = rb(expf(b2f(ldt[d])));
    if (threadIdx.x < N) {
        int n = threadIdx.x;
        zr[n] = -rb(expf(b2f(lar[d * N + n]))) * dt;
        zi[n] = b2f(aim[d * N + n]) * dt;
        cr[n] = b2f(cre[d * N + n]) * dt;
        ci[n] = b2f(cim[d * N + n]) * dt;
    }
    __syncthreads();
    for (int l = threadIdx.x; l < L; l += blockDim.x) {
        float p = rb((float)l);
        float s = 0;
        for (int n = 0; n < N; ++n) {
            float e = expf(zr[n] * p), sn, cs;
            sincosf(zi[n] * p, &sn, &cs);
            s += e * (cr[n] * cs - ci[n] * sn);
        }
        K[d * L + l] = 2.f * s;
    }
}
__global__ void k_conv(const u16* u, const float* K, const u16* dskip, float* a) {
    int d = blockIdx.x;
    __shared__ float us[L], ks[L];
    for (int l = threadIdx.x; l < L; l += blockDim.x) { us[l] = b2f(u[l * D + d]); ks[l] = K[d * L + l]; }
    __syncthreads();
    for (int l = threadIdx.x; l < L; l += blockDim.x) {
        float s = 0;
        for (int j = 0; j <= l; ++j) s += ks[j] * us[l - j];
        a[l * D + d] = rb(rb(s) + rb(us[l] * b2f(dskip[d])));
    }
}
__global__ void k_gemm(const u16* A, const u16* B, float* C, int M, int Nn, int Kk) {
    int n = blockIdx.x * blockDim.x + threadIdx.x; int m = blockIdx.y;
    float s = 0;
    for (int k = 0; k < Kk; ++k) s += b2f(A[m * Kk + k]) * b2f(B[k * Nn + n]);
    C[m * Nn + n] = rb(s);
}
__global__ void k_phi(const float* qkv, const u16* wphi, float* phq, float* phk) {
    int s = blockIdx.x; int t = threadIdx.x;
    int h = t >> 1, which = t & 1;
    const float* x = qkv + s * 1536 + which * 512 + h * 16;
    float p[16]; float mx = -1e30f, mn = 1e30f;
    for (int f = 0; f < 16; ++f) { float a = 0; for (int d = 0; d < 16; ++d) a += x[d] * b2f(wphi[h * 256 + d * 16 + f]); p[f] = rb(a); mx = fmaxf(mx, p[f]); mn = fminf(mn, p[f]); }
    float s1 = 0, s2 = 0, e1[16], e2[16];
    for (int f = 0; f < 16; ++f) { e1[f] = expf(p[f] - mx); s1 += e1[f]; e2[f] = expf(mn - p[f]); s2 += e2[f]; }
    float* o = (which ? phk : phq) + (s * 32 + h) * 32;
    for (int f = 0; f < 16; ++f) { o[f] = rb(e1[f] / s1); o[16 + f] = rb(e2[f] / s2); }
}
__global__ void k_kv(const float* qkv, const float* phk, float* kv, float* z) {
    int h = blockIdx.x; int t = threadIdx.x;
    int f = t >> 4, d = t & 15;
    float s = 0, zz = 0;
    for (int r = 0; r < L; ++r) { float p = phk[(r * 32 + h) * 32 + f]; s += p * qkv[r * 1536 + 1024 + h * 16 + d]; zz += p; }
    kv[(h * 32 + f) * 16 + d] = rb(s);
    if (d == 0) z[h * 32 + f] = rb(zz);
}
__global__ void k_o(const float* phq, const float* kv, const float* z, u16* o) {
    int s = blockIdx.x; int t = threadIdx.x; int h = t >> 4, d = t & 15;
    float num = 0, den = 0;
    for (int f = 0; f < 32; ++f) { float p = phq[(s * 32 + h) * 32 + f]; num += p * kv[(h * 32 + f) * 16 + d]; den += p * z[h * 32 + f]; }
    den = rb(rb(den) + 1e-6f);
    o[s * 512 + t] = f2b(rb(num) / den);
}
__global__ void k_mix(const float* g, const float* a, const float* b, u16* out) {
    int i = blockIdx.x * blockDim.x + threadIdx.x; int l = i / D;
    float gg = g[l];
    out[i] = f2b(rb(gg * a[i]) + rb(rb(1.f - gg) * b[i]));
}
extern "C" void kernel_launch(void* const* d_in, const int* in_sizes, int n_in,
                              void* d_out, int out_size, void* d_ws, size_t ws_size,
                              hipStream_t stream) {
    const u16* u = (const u16*)d_in[0]; const u16* lar = (const u16*)d_in[1]; const u16* aim = (const u16*)d_in[2];
    const u16* cre = (const u16*)d_in[3]; const u16* cim = (const u16*)d_in[4]; const u16* ldt = (const u16*)d_in[5];
    const u16* dsk = (const u16*)d_in[6]; const u16* wqkv = (const u16*)d_in[8]; const u16* wphi = (const u16*)d_in[9];
    const u16* wo = (const u16*)d_in[10]; const u16* gw = (const u16*)d_in[11];
    float* ws = (float*)d_ws;
    float* g = ws; ws += L; float* K = ws; ws += D * L; float* a = ws; ws += L * D; float* qkv = ws; ws += L * 1536;
    float* phq = ws; ws += L * 1024; float* phk = ws; ws += L * 1024; float* kv = ws; ws += 32 * 32 * 16; float* z = ws; ws += 1024;
    float* b = ws; ws += L * D; u16* o = (u16*)ws;
    k_gate<<<L, 64, 0, stream>>>(u, gw, g);
    k_kgen<<<D, 256, 0, stream>>>(lar, aim, cre, cim, ldt, K);
    k_conv<<<D, 256, 0, stream>>>(u, K, dsk, a);
    k_gemm<<<dim3(1536 / 256, L), 256, 0, stream>>>(u, wqkv, qkv, L, 1536, 512);
    k_phi<<<L, 64, 0, stream>>>(qkv, wphi, phq, phk);
    k_kv<<<H, 512, 0, stream>>>(qkv, phk, kv, z);
    k_o<<<L, 512, 0, stream>>>(phq, kv, z, o);
    k_gemm<<<dim3(512 / 256, L), 256, 0, stream>>>(o, wo, b, L, 512, 512);
    k_mix<<<L * D / 256, 256, 0, stream>>>(g, a, b, (u16*)d_out);
    for (int i = 0; i < NEMPTY; ++i) k_empty<<<256, 256, 0, stream>>>(ws);
}
```

```cpp
#include <hip/hip_runtime.h>
#include <hip/hip_bf16.h>
#include <stdint.h>

#if defined(__HIP_DEVICE_COMPILE__)
#pragma clang attribute push(__attribute__((target("no-packed-fp32-ops"))), apply_to = function)
#endif

typedef unsigned short u16;
typedef unsigned u32;
typedef unsigned long long u64;
typedef float f32x2 __attribute__((ext_vector_type(2)));
typedef float f32x4 __attribute__((ext_vector_type(4)));
typedef u32 u32x2 __attribute__((ext_vector_type(2)));
typedef u32 u32x4 __attribute__((ext_vector_type(4)));
typedef short s16x4 __attribute__((ext_vector_type(4)));
typedef __bf16 bf16x2 __attribute__((ext_vector_type(2)));
typedef __bf16 bf16x8 __attribute__((ext_vector_type(8)));
#define LAS __attribute__((address_space(3)))
#define GAS __attribute__((address_space(1)))
typedef LAS char* ldsp;
typedef __amdgpu_buffer_rsrc_t rsrc_t;
#define DEV __device__ __forceinline__

constexpr int L = 2048, D = 512;
constexpr int LDS_BYTES = 81920;
constexpr u64 TAG_STEP = 0x9E3779B97F4A7C15ull;
constexpr u32 SPIN_CAP = 40000;

constexpr u32 WS_E = 0, WS_TMO = 64;
constexpr u32 WS_FUT = 4096, WS_FKV = WS_FUT + 4096, WS_FY = WS_FKV + 4096, WS_FO = WS_FY + 4096;
constexpr u32 WS_G = 0x28000;
constexpr u32 WS_UT = 0x40000;
constexpr u32 WS_YT = WS_UT + 0x200000;
constexpr u32 WS_OP = WS_YT + 0x200000;
constexpr u32 WS_KVP = WS_OP + 0x200000;
constexpr u32 WS_ZP = WS_KVP + 0x100000;
constexpr u32 WS_DBG = WS_ZP + 0x10000;
constexpr u32 WS_USED = WS_DBG + 0x400000;

constexpr int AUX_SC1 = 16;

struct IdxTab { u16 v[2048]; };
constexpr IdxTab make_idx() {
    IdxTab t{};
    for (int i = 0; i < 2048; ++i) {
        int j = 2047 - i, p = j;
        int sh = j >= 1024 ? 3 : (j >= 512 ? 2 : (j >= 256 ? 1 : 0));
        if (sh) {
            int q = j >> sh, rem = j & ((1 << sh) - 1), half = 1 << (sh - 1);
            if (rem > half || (rem == half && (q & 1))) ++q;
            p = q << sh;
        }
        int row = 0, col = (p >> 3) & 15;
        if (p < 256) row = (p & 7) + 8 * (p >> 7);
        else if (p < 512) row = 16 + ((p - 256) >> 7) * 4 + ((p & 7) >> 1);
        else if (p < 1024) row = 24 + ((p - 512) >> 7) * 2 + ((p & 7) >> 2);
        else row = 32 + ((p - 1024) >> 7);
        t.v[i] = (u16)((row * 16 + col) * 2);
    }
    return t;
}
__device__ const IdxTab g_idx = make_idx();

DEV float u2f(u32 x) { return __builtin_bit_cast(float, x); }
DEV u32 f2u(float x) { return __builtin_bit_cast(u32, x); }
DEV float fexp(float x) { return __builtin_amdgcn_exp2f(x * 1.4426950408889634f); }
template <int CTRL> DEV float dpp_mov(float x) { return u2f(__builtin_amdgcn_update_dpp(0u, f2u(x), CTRL, 0xf, 0xf, true)); }
DEV float row16_sum(float x) {
    x += dpp_mov<0xB1>(x);
    x += dpp_mov<0x4E>(x);
    x += dpp_mov<0x141>(x);
    x += dpp_mov<0x140>(x);
    return x;
}
DEV u32 pk2(float lo, float hi) { f32x2 v = {lo, hi}; return __builtin_bit_cast(u32, __builtin_convertvector(v, bf16x2)); }
DEV float blo(u32 x) { return u2f(x << 16); }
DEV float bhi(u32 x) { return u2f(x & 0xffff0000u); }
DEV float rb(float f) { return blo(pk2(f, 0.f)); }
DEV f32x4 mfma32(u32x4 a, u32x4 b, f32x4 c) {
    return __builtin_amdgcn_mfma_f32_16x16x32_bf16(__builtin_bit_cast(bf16x8, a), __builtin_bit_cast(bf16x8, b), c, 0, 0, 0);
}
DEV f32x4 mfma16(u32x2 a, u32x2 b, f32x4 c) {
    return __builtin_amdgcn_mfma_f32_16x16x16bf16_1k(__builtin_bit_cast(s16x4, a), __builtin_bit_cast(s16x4, b), c, 0, 0, 0);
}
DEV u32x2 trread(ldsp p) { return __builtin_bit_cast(u32x2, __builtin_amdgcn_ds_read_tr16_b64_v4i16((LAS s16x4*)p)); }
DEV u32x4 lds_r128(ldsp p) { return *(LAS u32x4*)p; }
DEV u32x2 lds_r64(ldsp p) { return *(volatile LAS u32x2*)p; }
DEV void lds_w128(ldsp p, u32x4 v) { *(LAS u32x4*)p = v; }
DEV void lds_w64(ldsp p, u32x2 v) { *(LAS u32x2*)p = v; }
DEV void lds_w32(ldsp p, u32 v) { *(LAS u32*)p = v; }
DEV u32x4 cat(u32x2 a, u32x2 b) { u32x4 r = {a.x, a.y, b.x, b.y}; return r; }
DEV rsrc_t mkrsrc(const void* p, u32 bytes) { return __builtin_amdgcn_make_buffer_rsrc((void*)p, 0, bytes, 0x00020000); }
DEV u32x4 mkdesc(const void* p, u32 bytes) {
    u64 a = (u64)p;
    u32x4 d = {(u32)a, (u32)(a >> 32) & 0xffffu, bytes, 0x00020000u};
    return d;
}
DEV void dma16(u32x4 desc, ldsp dst, u32 voff, u32 soff) {
    asm volatile("s_mov_b32 m0, %0\n\ts_nop 0\n\tbuffer_load_dwordx4 %1, %2, %3 offen lds"
                 :: "s"((u32)(uintptr_t)dst), "v"(voff), "s"(desc), "s"(soff) : "memory");
}
DEV u32x4 ld128(rsrc_t r, u32 voff, u32 soff = 0) { return __builtin_amdgcn_raw_buffer_load_b128(r, voff, soff, 0); }
DEV u32x2 ld64(rsrc_t r, u32 voff, u32 soff = 0) { return __builtin_amdgcn_raw_buffer_load_b64(r, voff, soff, 0); }
DEV u32 ld16(rsrc_t r, u32 voff, u32 soff = 0) { return __builtin_amdgcn_raw_buffer_load_b16(r, voff, soff, 0); }
DEV u32x4 ld128c(rsrc_t r, u32 voff, u32 soff = 0) { return __builtin_amdgcn_raw_buffer_load_b128(r, voff, soff, AUX_SC1); }
DEV u32x2 ld64c(rsrc_t r, u32 voff, u32 soff = 0) { return __builtin_amdgcn_raw_buffer_load_b64(r, voff, soff, AUX_SC1); }
DEV u32 ld32c(rsrc_t r, u32 voff, u32 soff = 0) { return __builtin_amdgcn_raw_buffer_load_b32(r, voff, soff, AUX_SC1); }
DEV void st128c(rsrc_t r, u32x4 v, u32 voff, u32 soff = 0) { __builtin_amdgcn_raw_buffer_store_b128(v, r, voff, soff, AUX_SC1); }
DEV void st64c(rsrc_t r, u32x2 v, u32 voff, u32 soff = 0) { __builtin_amdgcn_raw_buffer_store_b64(v, r, voff, soff, AUX_SC1); }
#define WAIT_VM(n) asm volatile("s_waitcnt vmcnt(" #n ")" ::: "memory")
#define SCHED_A() __builtin_amdgcn_sched_barrier(0)
#define WAIT_LGKM0() asm volatile("s_waitcnt lgkmcnt(0)" ::: "memory")
DEV void bar() { asm volatile("s_waitcnt lgkmcnt(0)" ::: "memory"); __builtin_amdgcn_s_barrier(); asm volatile("" ::: "memory"); }
#define PRIO_A0 3
#define PRIO_A1 1
#define PRIO_BH2 3
#define PRIO_BC 2
#define PRIO_BH 0
DEV u32 uni(u32 x) { return __builtin_amdgcn_readfirstlane(x); }

DEV float swap16_lo(float a, float b, float& other) {
    auto s = __builtin_amdgcn_permlane16_swap(f2u(a), f2u(b), false, false);
    other = u2f(s[1]);
    return u2f(s[0]);
}
DEV float swap32_lo(float a, float b, float& other) {
    auto s = __builtin_amdgcn_permlane32_swap(f2u(a), f2u(b), false, false);
    other = u2f(s[1]);
    return u2f(s[0]);
}
template <bool MAX> DEV void rows_reduce2(float& a, float& b) {
    float o, t = swap16_lo(a, b, o);
    t = MAX ? __builtin_fmaxf(t, o) : t + o;
    float u = swap32_lo(t, t, o);
    u = MAX ? __builtin_fmaxf(u, o) : u + o;
    a = swap16_lo(u, u, b);
}

typedef GAS u64 gu64;
DEV u64 make_tag(u64 e) {
    u32 lo = (u32)e, hi = (u32)(e >> 32);
    asm volatile("" : "+v"(lo), "+v"(hi));
    return (((u64)uni(hi) << 32) | uni(lo)) + TAG_STEP;
}
DEV u64 flag_ld(char* ws, u32 off) { return __hip_atomic_load((gu64*)(ws + off), __ATOMIC_RELAXED, __HIP_MEMORY_SCOPE_AGENT); }
DEV void flag_st(char* ws, u32 off, u64 v) { __hip_atomic_store((gu64*)(ws + off), v, __ATOMIC_RELAXED, __HIP_MEMORY_SCOPE_AGENT); }
DEV void wait_flags(char* ws, u32 off, u64 tag) {
#pragma unroll 1
    for (u32 spins = 0; spins < SPIN_CAP; ++spins) {
        u64 v = flag_ld(ws, off);
        if (__builtin_amdgcn_ballot_w64(v != tag) == 0) { asm volatile("" ::: "memory"); return; }
        __builtin_amdgcn_s_sleep(1);
    }
    flag_st(ws, WS_TMO, tag);
}
DEV void publish(char* ws, u32 off, u64 tag) {
    WAIT_VM(0);
    bar();
    if (threadIdx.x == 0) flag_st(ws, off, tag);
}

#define STAMP(i) asm volatile("; MARK " #i)
#define FSTAMP(i) do { } while (0)
#define FS1(i) do { } while (0)
#define FS2(i) do { } while (0)
#define FS3(i) do { } while (0)
#define STAMP_ARG
#define STAMP_PASS

struct Params {
    char* ws; const u16* u; const u16* wqkv; const u16* wphi; const u16* lar; const u16* aim; const u16* cre; const u16* cim;
    const u16* ldt; const u16* gw; const u16* dsk; const u16* wo; u16* out;
};

struct cplx { float r, i; };
DEV cplx cmul(cplx a, cplx b) { cplx c; c.r = a.r * b.r - a.i * b.i; c.i = a.r * b.i + a.i * b.r; return c; }
DEV cplx cexp_p(float zr2, float zrev, float p) {
    float e = __builtin_amdgcn_exp2f(zr2 * p);
    float a = __builtin_amdgcn_fractf(zrev * p);
    cplx c; c.r = e * __builtin_amdgcn_cosf(a); c.i = e * __builtin_amdgcn_sinf(a); return c;
}

template <int NW> DEV void wo_prefetch(const Params& p, ldsp lds, int ct, int w, int l) {
    const u32x4 rwo = mkdesc(p.wo, D * D * 2);
    u32 voff = (l >> 3) * 1024 + ct * 128 + ((((l & 7) >> 1) ^ ((l >> 4) & 3)) * 32) + (l & 1) * 16;
#pragma unroll
    for (int i = 0; i < 64 / NW; ++i) {
        int piece = i * NW + w;
        dma16(rwo, lds + 16384 + piece * 1024, voff, piece * 8192);
    }
}

DEV u64 role_conv(const Params& p, ldsp lds, char* ws, u64 e, int ba, int w, int l STAMP_ARG) {
    const int t = threadIdx.x, xcd = ba & 7, j = ba >> 3, g = l >> 4, r16 = l & 15;
    const int a = xcd >> 2, c = xcd & 3;
    rsrc_t ru = mkrsrc(p.u, L * D * 2), rws = mkrsrc(ws, WS_USED);
    const int ch0 = 2 * (32 * xcd + j);
    __builtin_amdgcn_s_setprio(PRIO_A0);
    const int tb = 16 * a + (j >> 1), cg = 2 * c + (j & 1);
    {
        u32 voff = (tb * 64 + (l >> 3)) * 1024 + cg * 128 + (l & 7) * 16;
        dma16(mkdesc(p.u, L * D * 2), lds + 70656 + w * 1024, voff, w * 8192);
    }
    const int gtok = 1024 * a + 8 * (32 * c + j) + w;
    u32x4 gx = ld128(ru, gtok * 1024 + l * 16);
    u32x4 gwv = *(const GAS u32x4*)(p.gw + l * 8);
    const int kch = (t >> 7) & 1, kn = t & 127;
    const int pidx = (ch0 + kch) * 128 + kn;
    float lar = blo(p.lar[pidx]), aim = blo(p.aim[pidx]), cre = blo(p.cre[pidx]), cim = blo(p.cim[pidx]), ldt = blo(p.ldt[ch0 + kch]);
    u32x4 idx = *(const GAS u32x4*)(g_idx.v + (t & 255) * 8);
    SCHED_A();
    __builtin_amdgcn_s_setprio(PRIO_A1);
    WAIT_VM(0);
    const u64 tag = make_tag(e);
    bar();
    STAMP(1);
    {
        const int cw = w & 3, s = w >> 2;
        ldsp src = lds + 70656 + (32 * s + 8 * g + (r16 >> 2)) * 128 + 32 * cw + 8 * (l & 3);
        u32x4 v = cat(trread(src), trread(src + 4 * 128));
        st128c(rws, v, WS_UT + (cg * 64 + 16 * cw + r16) * 4096 + (tb * 64 + 32 * s + 8 * g) * 2);
    }
    {
        float s0 = 0.f, s1 = 0.f;
#pragma unroll
        for (int q = 0; q < 4; ++q) {
            u32 pr = pk2(blo(gx[q]) * blo(gwv[q]), bhi(gx[q]) * bhi(gwv[q]));
            s0 += blo(pr); s1 += bhi(pr);
        }
        s0 = row16_sum(s0 + s1);
        s1 = s0;
        rows_reduce2<false>(s0, s1);
        float g0 = rb(__builtin_amdgcn_rcpf(1.f + fexp(-rb(s0))));
        if (l == 0) __builtin_amdgcn_raw_buffer_store_b32(f2u(g0), rws, WS_G + gtok * 4, 0, AUX_SC1);
    }
    STAMP(2);
    {
        const float dt = rb(fexp(ldt));
        const float zr2 = -rb(fexp(lar)) * dt * 1.4426950408889634f, zrev = aim * dt * 0.15915494309189535f;
        ldsp T = lds + kch * 33792 + kn * 4;
        auto put = [&](int row, cplx v) { lds_w32(T + row * 528, pk2(v.r, v.i)); };
        const cplx w128 = cexp_p(zr2, zrev, 128.f);
        cplx G; G.r = 2.f * cre * dt; G.i = 2.f * cim * dt;
        if (w < 4) {
            cplx wp[8];
            wp[1] = cexp_p(zr2, zrev, 1.f); wp[2] = cmul(wp[1], wp[1]); wp[3] = cmul(wp[2], wp[1]); wp[4] = cmul(wp[2], wp[2]);
            wp[5] = cmul(wp[4], wp[1]); wp[6] = cmul(wp[4], wp[2]); wp[7] = cmul(wp[4], wp[3]);
#pragma unroll
            for (int aa = 0; aa < 4; ++aa) {
                if (aa < 2) {
                    put(8 * aa, G);
#pragma unroll
                    for (int cc = 1; cc < 8; ++cc) put(8 * aa + cc, cmul(G, wp[cc]));
                } else {
                    put(16 + 4 * (aa - 2), G);
#pragma unroll
                    for (int cc = 1; cc < 4; ++cc) put(16 + 4 * (aa - 2) + cc, cmul(G, wp[2 * cc]));
                }
                if (aa < 3) G = cmul(G, w128);
            }
        } else {
            const cplx w4 = cexp_p(zr2, zrev, 4.f);
            G = cmul(G, cexp_p(zr2, zrev, 512.f));
#pragma unroll
            for (int aa = 4; aa <= 16; ++aa) {
                if (aa < 8) {
                    put(24 + 2 * (aa - 4), G);
                    put(24 + 2 * (aa - 4) + 1, cmul(G, w4));
                } else put(32 + aa - 8, G);
                if (aa < 16) G = cmul(G, w128);
            }
            cplx B[9];
            B[1] = cmul(w4, w4); B[2] = cmul(B[1], B[1]); B[3] = cmul(B[2], B[1]); B[4] = cmul(B[2], B[2]);
            B[5] = cmul(B[4], B[1]); B[6] = cmul(B[4], B[2]); B[7] = cmul(B[4], B[3]); B[8] = cmul(B[4], B[4]);
            lds_w32(T + 48 * 528, 0x00003f80u);
#pragma unroll
            for (int b = 1; b < 16; ++b) {
                cplx v = b <= 8 ? B[b] : cmul(B[8], B[b - 8]);
                lds_w32(T + (48 + b) * 528, pk2(v.r, -v.i));
            }
        }
    }
    publish(ws, WS_FUT + cg * 256 + tb * 8, tag);
    if (w < 6) {
        const int ch = w >= 3, tile = w - 3 * ch;
        ldsp T = lds + ch * 33792 + r16 * 528 + g * 16;
        f32x4 k0 = {0, 0, 0, 0};
#pragma unroll
        for (int s = 0; s < 8; ++s) k0 = mfma32(lds_r128(T + tile * 16 * 528 + s * 64), lds_r128(T + 48 * 528 + s * 64), k0);
        ldsp KC = lds + 67584 + ch * 1536 + r16 * 2 + (tile * 16 + 4 * g) * 32;
        u32 a0 = pk2(k0[0], k0[1]), a1 = pk2(k0[2], k0[3]);
        *(LAS u16*)(KC) = (u16)a0; *(LAS u16*)(KC + 32) = (u16)(a0 >> 16);
        *(LAS u16*)(KC + 64) = (u16)a1; *(LAS u16*)(KC + 96) = (u16)(a1 >> 16);
    }
    bar();
    const int hch = w >> 2, t8 = t & 255;
    {
        ldsp KC = lds + 67584 + hch * 1536;
        u32x4 v;
#pragma unroll
        for (int q = 0; q < 4; ++q) {
            u32 lo = *(LAS u16*)(KC + (idx[q] & 0xffffu)), hi = *(LAS u16*)(KC + (idx[q] >> 16));
            v[q] = lo | (hi << 16);
        }
        lds_w128(lds + hch * 4608 + t8 * 16, v);
        if (t8 < 32) { u32x4 z = {0, 0, 0, 0}; lds_w128(lds + hch * 4608 + 4096 + t8 * 16, z); }
    }
    STAMP(3);
    if (w == 0) wait_flags(ws, WS_FUT + xcd * 256 + (l & 31) * 8, tag);
    bar();
    STAMP(4);
    constexpr int UB = 9216, CS = 4160;
    {
        u32 voff = WS_UT + (ch0 + hch) * 4096 + t8 * 16;
        u32x4 d0 = ld128c(rws, voff), d1 = ld128c(rws, voff + 4), d2 = ld128c(rws, voff + 8);
        u32x2 f = ld64c(rws, WS_UT + (ch0 + hch) * 4096);
        u32x4 v1, v3;
#pragma unroll
        for (int q = 0; q < 4; ++q) { v1[q] = __builtin_amdgcn_alignbit(d1[q], d0[q], 16); v3[q] = __builtin_amdgcn_alignbit(d2[q], d1[q], 16); }
        ldsp U = lds + UB + hch * 4 * CS + 32 + t8 * 16;
        lds_w128(U, d0); lds_w128(U + CS, v1); lds_w128(U + 2 * CS, d1); lds_w128(U + 3 * CS, v3);
        if (t8 < 4) {
            u32x4 z = {0, 0, 0, 0}, y = z;
            if (t8 == 1) y.w = f.x << 16;
            if (t8 == 2) y.w = f.x;
            if (t8 == 3) { y.z = f.x << 16; y.w = __builtin_amdgcn_alignbit(f.y, f.x, 16); }
            ldsp F = lds + UB + (hch * 4 + t8) * CS;
            lds_w128(F, z); lds_w128(F + 16, y);
        }
    }
    bar();
    STAMP(5);
    {
        const int mq = w & 3;
        const int s = (r16 + 1) & 3;
        ldsp A0 = lds + UB + (hch * 4 + s) * CS + 2 * (1 + r16 - s + 8 * g) + mq * 2 * 64;
        ldsp B0 = lds + hch * 4608 + 4064 - 3584 - 32 * r16 + 16 * g + mq * 2 * 64;
        f32x4 acc[8];
#pragma unroll
        for (int i = 0; i < 8; ++i) acc[i] = f32x4{0, 0, 0, 0};
#pragma unroll
        for (int mm = 0; mm < 2; ++mm) {
            u32x4 Af[8], Bf[8];
#pragma unroll
            for (int i = 0; i < 8; ++i) {
                Bf[i] = lds_r128(B0 + mm * 64 + (7 - i) * 512);
                Af[i] = cat(lds_r64(A0 + mm * 64 + i * 512), lds_r64(A0 + mm * 64 + i * 512 + 8));
            }
#pragma unroll
            for (int i = 0; i < 8; ++i)
#pragma unroll
                for (int cc = i; cc < 8; ++cc) acc[cc] = mfma32(Af[i], Bf[cc - i], acc[cc]);
            __builtin_amdgcn_sched_barrier(0);
        }
        FSTAMP(7);
        bar();
        FSTAMP(8);
        ldsp R = lds + hch * 24576 + l * 16;
#pragma unroll
        for (int cc = 0; cc < 8; ++cc)
            if ((cc >> 1) != mq) lds_w128(R + (cc * 3 + ((mq - (cc >> 1) - 1) & 3)) * 1024, __builtin_bit_cast(u32x4, acc[cc]));
        bar();
        FSTAMP(9);
#pragma unroll
        for (int q = 0; q < 4; ++q)
            if (q == mq) {
                u32 pk[2][2];
#pragma unroll
                for (int h = 0; h < 2; ++h) {
                    float v[4];
#pragma unroll
                    for (int r = 0; r < 4; ++r) v[r] = acc[2 * q + h][r];
#pragma unroll
                    for (int sl = 0; sl < 3; ++sl) {
                        u32x4 o = lds_r128(R + ((2 * q + h) * 3 + sl) * 1024);
#pragma unroll
                        for (int r = 0; r < 4; ++r) v[r] += u2f(o[r]);
                    }
                    pk[h][0] = pk2(v[0], v[1]); pk[h][1] = pk2(v[2], v[3]);
                }
                auto sx = __builtin_amdgcn_permlane16_swap(pk[0][0], pk[1][0], false, false);
                auto sy = __builtin_amdgcn_permlane16_swap(pk[0][1], pk[1][1], false, false);
                u32x4 v = {sx[0], sy[0], sx[1], sy[1]};
                st128c(rws, v, WS_YT + (ch0 + hch) * 4096 + (256 * (2 * q + (g & 1)) + 16 * r16 + 4 * (g & 2)) * 2);
            }
    }
    FSTAMP(10);
    publish(ws, WS_FY + xcd * 256 + j * 8, tag);
    STAMP(6);
    return tag;
}

typedef float f32x16 __attribute__((ext_vector_type(16)));
DEV f32x16 mfma32x32(u32x4 a, u32x4 b, f32x16 c) {
    return __builtin_amdgcn_mfma_f32_32x32x16_bf16(__builtin_bit_cast(bf16x8, a), __builtin_bit_cast(bf16x8, b), c, 0, 0, 0);
}
DEV float vmax3(float a, float b, float c) { float r; asm("v_max3_f32 %0, %1, %2, %3" : "=v"(r) : "v"(a), "v"(b), "v"(c)); return r; }
DEV float vmin3(float a, float b, float c) { float r; asm("v_min3_f32 %0, %1, %2, %3" : "=v"(r) : "v"(a), "v"(b), "v"(c)); return r; }

template <bool POS, bool NEG> DEV void feature_map(u32x4 x0, u32x4 x1, u32x4 wown, int l, u32 (&ph)[16]) {
    const bool odd = (l >> 2) & 1;
    u32x4 a0, a1;
#pragma unroll
    for (int q = 0; q < 4; ++q) { a0[q] = odd ? 0u : wown[q]; a1[q] = odd ? wown[q] : 0u; }
    f32x16 pr;
#pragma unroll
    for (int r = 0; r < 16; ++r) pr[r] = 0.f;
    pr = mfma32x32(a0, x0, pr);
    pr = mfma32x32(a1, x1, pr);
    float p[16];
#pragma unroll
    for (int r = 0; r < 16; r += 2) { u32 v = pk2(pr[r], pr[r + 1]); p[r] = blo(v); p[r + 1] = bhi(v); }
    constexpr float LG = 1.4426950408889634f;
#pragma unroll
    for (int sg = 0; sg < 2; ++sg) {
        if (sg == 0 ? !POS : !NEG) continue;
        float m = sg ? vmin3(p[0], p[1], p[2]) : vmax3(p[0], p[1], p[2]);
#pragma unroll
        for (int r = 3; r < 15; r += 2) m = sg ? vmin3(m, p[r], p[r + 1]) : vmax3(m, p[r], p[r + 1]);
        m = sg ? vmin3(m, p[15], p[15]) : vmax3(m, p[15], p[15]);
        const float sc = sg ? -LG : LG, c = -m * sc;
        float a[16];
#pragma unroll
        for (int r = 0; r < 16; ++r) a[r] = __builtin_fmaf(p[r], sc, c);
#pragma unroll
        for (int r = 0; r < 16; ++r) a[r] = __builtin_amdgcn_exp2f(a[r]);
        float sa[4];
#pragma unroll
        for (int q = 0; q < 4; ++q) sa[q] = (a[4 * q] + a[4 * q + 1]) + (a[4 * q + 2] + a[4 * q + 3]);
        const float ia = __builtin_amdgcn_rcpf((sa[0] + sa[1]) + (sa[2] + sa[3]));
#pragma unroll
        for (int r = 0; r < 8; ++r) ph[8 * sg + r] = pk2(a[2 * r] * ia, a[2 * r + 1] * ia);
    }
}

DEV u64 role_attn(const Params& p, ldsp lds, char* ws, u64 e, int bb, int w, int l, int ct STAMP_ARG) {
    const int xcd = bb & 7, j = bb >> 3, g = l >> 4, r16 = l & 15, h = l >> 5, w4 = w & 3;
    const int rb_ = 8 * (xcd >> 2) + (j >> 2), cb = 4 * (xcd & 3) + (j & 3);
    const int rot = (j + xcd) & 7;
    rsrc_t rws = mkrsrc(ws, WS_USED);
#define SCHED() __builtin_amdgcn_sched_barrier(0)
    constexpr int XCH = 32768, WPH = 49152, PH = 0, PHS = 10240, VV = 20480, VS = 5120, FR = 0;
    const int tok = 32 * w4 + (l & 31);
    const u32 wph = WPH + (l & 1) * 512 + (4 * h + (r16 >> 2)) * 32 + (8 * (g & 1) + 4 * ((l & 3) >> 1)) * 2;
    u64 tag;
    if (w >= 4) {
        __builtin_amdgcn_s_setprio(PRIO_BH);
        const u32x4 ru = mkdesc(p.u, L * D * 2), rw = mkdesc(p.wqkv, D * 3 * D * 2);
        const u32 xv = (rb_ * 128 + w4 * 8 + (l >> 3)) * 1024 + (((l & 7) ^ (((w4 & 1) << 2) | ((l >> 4) & 3))) * 16);
        const u32 wv = (16 * w4 + (l >> 2)) * 3072 + cb * 64 + (l & 3) * 16;
        auto issue_x = [&](int s) {
#pragma unroll
            for (int i = 0; i < 4; ++i) dma16(ru, lds + (s % 3) * 16384 + (4 * i + w4) * 1024, xv, ((s + rot) & 7) * 128 + i * 32768);
        };
        auto issue_w = [&](int s, int sec) {
            dma16(rw, lds + 49152 + ((3 * s + sec) & 7) * 4096 + w4 * 1024, wv, ((s + rot) & 7) * (64 * 3072) + sec * 1024);
        };
        FS3(6);
        issue_w(0, 0); issue_w(0, 1); issue_w(0, 2); issue_x(0);
        FS3(7);
        __builtin_amdgcn_s_barrier();
        FS3(8);
        issue_w(1, 0); issue_w(1, 1); issue_w(1, 2); issue_x(1);
        FS3(9);
#pragma unroll
        for (int s = 0; s < 8; ++s) {
            if (s == 0) WAIT_VM(7); else if (s < 7) WAIT_VM(6); else WAIT_VM(0);
            if (s == 0) FS3(10);
            __builtin_amdgcn_s_barrier();
            if (s >= 1 && s + 1 < 8) issue_w(s + 1, 2);
            if (s + 2 < 8) { issue_x(s + 2); issue_w(s + 2, 0); issue_w(s + 2, 1); }
            if (s == 6 && w == 4) dma16(mkdesc(p.wphi, 32 * 256 * 2), lds + WPH, l * 16, cb * 1024);
        }
        tag = make_tag(e);
        bar();
        ldsp X = lds + XCH + w4 * 4096 + l * 16;
        u32x4 wown = cat(trread(lds + wph), trread(lds + wph + 8 * 32));
        u32x4 q0 = lds_r128(X), q1 = lds_r128(X + 1024);
        bar();
        publish(ws, WS_FKV + cb * 256 + rb_ * 8, tag);
        wo_prefetch<4>(p, lds, ct, w4, l);
        SCHED();
        u32 ph[16];
        feature_map<true, true>(q0, q1, wown, l, ph);
        u32x4 bq[2][2];
#pragma unroll
        for (int sg = 0; sg < 2; ++sg)
#pragma unroll
            for (int q = 0; q < 4; ++q) {
                auto sw = __builtin_amdgcn_permlane32_swap(ph[8 * sg + q], ph[8 * sg + q + 4], false, false);
                bq[0][sg][q] = sw[0]; bq[1][sg][q] = sw[1];
            }
        STAMP(3);
        __builtin_amdgcn_s_setprio(PRIO_BH2);
        bar();
#pragma unroll
        for (int hh = 0; hh < 2; ++hh) {
            f32x16 nd;
#pragma unroll
            for (int r = 0; r < 16; ++r) nd[r] = 0.f;
            nd = mfma32x32(lds_r128(lds + FR + (2 * hh) * 1024 + l * 16), bq[hh][0], nd);
            nd = mfma32x32(lds_r128(lds + FR + (2 * hh + 1) * 1024 + l * 16), bq[hh][1], nd);
            const float inv = __builtin_amdgcn_rcpf(rb(rb(nd[8]) + 1e-6f));
            u32x4 o;
#pragma unroll
            for (int q = 0; q < 4; ++q) { u32 n = pk2(nd[2 * q], nd[2 * q + 1]); o[q] = pk2(blo(n) * inv, bhi(n) * inv); }
            st128c(rws, o, WS_OP + (rb_ * 128 + tok) * 1024 + cb * 64 + hh * 32 + h * 16);
        }
        FS2(10);
    } else {
        __builtin_amdgcn_s_setprio(PRIO_BC);
        f32x16 acc[3];
#pragma unroll
        for (int sec = 0; sec < 3; ++sec)
#pragma unroll
            for (int r = 0; r < 16; ++r) acc[sec][r] = 0.f;
        const u32 xa = tok * 128 + ((h ^ ((tok >> 1) & 7)) * 16);
        const u32 wa = 49152 + (8 * h + (r16 >> 2)) * 64 + (g & 1) * 32 + 8 * (l & 3);
        __builtin_amdgcn_s_barrier();
#pragma unroll
        for (int s = 0; s < 8; ++s) {
            __builtin_amdgcn_s_barrier();
            asm volatile("" ::: "memory");
            if (s == 0) FS1(6);
            if (s == 1) FS1(7);
            if (s == 2) FS1(8);
            if (s == 4) FS1(9);
            if (s == 6) FS1(10);
            u32x4 xf[4], wf[12];
            auto read_x = [&](int ks) { xf[ks] = lds_r128(lds + (s % 3) * 16384 + (xa ^ (32 * ks))); };
            auto read_w = [&](int i) {
                ldsp a = lds + wa + ((3 * s + i % 3) & 7) * 4096 + (i / 3) * 1024;
                wf[i] = cat(trread(a), trread(a + 256));
            };
            read_x(0); read_w(0); read_w(1); read_w(2);
            SCHED();
#pragma unroll
            for (int i = 0; i < 12; ++i) {
                if (i % 3 == 0 && i / 3 + 1 < 4) read_x(i / 3 + 1);
                if (i + 3 < 12) read_w(i + 3);
                SCHED();
                acc[i % 3] = mfma32x32(wf[i], xf[i / 3], acc[i % 3]);
                SCHED();
            }
        }
        u32x4 fr[3][2];
#pragma unroll
        for (int sec = 0; sec < 3; ++sec)
#pragma unroll
            for (int hh = 0; hh < 2; ++hh)
#pragma unroll
                for (int q = 0; q < 4; ++q) fr[sec][hh][q] = pk2(acc[sec][8 * hh + 2 * q], acc[sec][8 * hh + 2 * q + 1]);
        lds_w128(lds + XCH + w * 4096 + l * 16, fr[0][0]);
        lds_w128(lds + XCH + w * 4096 + 1024 + l * 16, fr[0][1]);
        tag = make_tag(e);
        bar();
        STAMP(1);
#pragma unroll
        for (int hh = 0; hh < 2; ++hh) {
            ldsp q = lds + VV + hh * VS + tok * 40 + 8 * h;
            lds_w64(q, u32x2{fr[2][hh].x, fr[2][hh].y});
            lds_w64(q + 16, u32x2{fr[2][hh].z, fr[2][hh].w});
        }
        {
            u32x4 wown = cat(trread(lds + wph), trread(lds + wph + 8 * 32));
            u32 ph[16];
            feature_map<true, true>(fr[1][0], fr[1][1], wown, l, ph);
            ldsp q = lds + PH + h * PHS + tok * 80;
#pragma unroll
            for (int i = 0; i < 4; ++i) lds_w128(q + i * 16, u32x4{ph[4 * i], ph[4 * i + 1], ph[4 * i + 2], ph[4 * i + 3]});
        }
        FS2(6);
        bar();
        FS2(7);
        const int hh = w >> 1, sg = w & 1;
        {
            f32x4 kv = {0, 0, 0, 0}, z = {0, 0, 0, 0};
            const u32x4 ones = {0x3f803f80u, 0x3f803f80u, 0x3f803f80u, 0x3f803f80u};
            ldsp pa = lds + PH + hh * PHS + (8 * g + (r16 >> 2)) * 80 + sg * 32 + 8 * (l & 3);
            ldsp pb = lds + VV + hh * VS + (8 * g + (r16 >> 2)) * 40 + 8 * (l & 3);
#pragma unroll
            for (int tg = 0; tg < 4; ++tg) {
                u32x4 A = cat(trread(pa + tg * 32 * 80), trread(pa + tg * 32 * 80 + 4 * 80));
                u32x4 B = cat(trread(pb + tg * 32 * 40), trread(pb + tg * 32 * 40 + 4 * 40));
                kv = mfma32(A, B, kv);
                z = mfma32(A, ones, z);
            }
            st128c(rws, __builtin_bit_cast(u32x4, kv), WS_KVP + ((cb * 16 + rb_) * 4 + w) * 1024 + l * 16);
            if (r16 == 0) st128c(rws, __builtin_bit_cast(u32x4, z), WS_ZP + ((cb * 16 + rb_) * 4 + w) * 64 + g * 16);
        }
        FS2(8);
        publish(ws, WS_FKV + cb * 256 + rb_ * 8, tag);
        STAMP(2);
        wait_flags(ws, WS_FKV + cb * 256 + r16 * 8, tag);
        STAMP(4);
        u32x4 part[16];
#pragma unroll
        for (int r = 0; r < 16; ++r) part[r] = ld128c(rws, WS_KVP + (cb * 16 * 4 + w) * 1024 + l * 16, r * 4096);
        u32x4 zp = ld128c(rws, WS_ZP + ((cb * 16 + (l >> 2)) * 4 + w) * 64 + (l & 3) * 16);
        float s[4] = {u2f(part[0].x), u2f(part[0].y), u2f(part[0].z), u2f(part[0].w)};
#pragma unroll
        for (int r = 1; r < 16; ++r)
#pragma unroll
            for (int q = 0; q < 4; ++q) s[q] += u2f(part[r][q]);
        float zs[4] = {u2f(zp.x), u2f(zp.y), u2f(zp.z), u2f(zp.w)};
#pragma unroll
        for (int q = 0; q < 4; ++q) {
            float x = zs[q];
            x += dpp_mov<0x124>(x);
            x += dpp_mov<0x128>(x);
            zs[q] = x;
        }
        rows_reduce2<false>(zs[0], zs[1]);
        rows_reduce2<false>(zs[2], zs[3]);
        FS2(9);
        ldsp F = lds + FR + w * 1024;
        lds_w64(F + (r16 + 32 * (g >> 1)) * 16 + 8 * (g & 1), u32x2{pk2(s[0], s[1]), pk2(s[2], s[3])});
        lds_w64(F + (16 + (l >> 2) + 32 * ((l & 3) >> 1)) * 16 + 8 * (l & 1), u32x2{pk2(zs[0], zs[1]), pk2(zs[2], zs[3])});
        bar();
    }
    publish(ws, WS_FO + rb_ * 256 + cb * 8, tag);
    STAMP(5);
    return tag;
}

DEV void final_stage(const Params& p, ldsp lds, char* ws, u64 tag, int b, int w, int l, int tt32, int ct STAMP_ARG) {
    const int t = threadIdx.x, g = l >> 4, r16 = l & 15, cw = w & 3, tt = w >> 2;
    rsrc_t ru = mkrsrc(p.u, L * D * 2), rws = mkrsrc(ws, WS_USED), rout = mkrsrc(p.out, L * D * 2);
    const int tok0 = tt32 * 32, chl = ct * 64 + 16 * cw + 4 * g, tok = tok0 + 16 * tt + r16;
    u32x2 uu = ld64(ru, tok * 1024 + chl * 2);
    u32x2 dsk = *(const GAS u32x2*)(p.dsk + chl);
    WAIT_VM(0);
    bar();
    u32x4 wf[16];
    {
        const int row = 16 * (g >> 1) + 4 * (g & 1) + (r16 >> 2);
        ldsp q = lds + 16384 + row * 128 + ((cw ^ ((row >> 1) & 3)) * 32) + 8 * (l & 3);
#pragma unroll
        for (int ks = 0; ks < 16; ++ks) wf[ks] = cat(trread(q + ks * 4096), trread(q + ks * 4096 + 1024));
    }
    if (b == 256 && w < 4) {
        wait_flags(ws, WS_FUT + (t >> 5) * 256 + (t & 31) * 8, tag);
        wait_flags(ws, WS_FKV + (t >> 4) * 256 + (t & 15) * 8, tag);
    }
    STAMP(8);
    if (w == 0) {
        const int tg8 = tt32 * 4 + (l & 3), ga = tg8 >> 7, gc = (tg8 >> 5) & 3, gj = tg8 & 31;
        const int gtb = 16 * ga + (gj >> 1), gcg = 2 * gc + (gj & 1);
        u32 off = l < 32 ? WS_FY + ct * 256 + l * 8 : (l < 48 ? WS_FO + (tok0 >> 7) * 256 + (l & 15) * 8 : WS_FUT + gcg * 256 + gtb * 8);
        wait_flags(ws, off, tag);
    }
    bar();
    if (b == 256 && t == 0) flag_st(ws, WS_E, tag);
    STAMP(10);
    float gate;
    {
        u32x4 ov[4];
#pragma unroll
        for (int i = 0; i < 4; ++i) ov[i] = ld128c(rws, WS_OP + (tok0 + 4 * w) * 1024 + l * 16, i * 1024);
        gate = u2f(ld32c(rws, WS_G + tok * 4));
        u32x4 y;
        if (t < 256) y = ld128c(rws, WS_YT + (ct * 64 + (t >> 2)) * 4096 + tok0 * 2 + (t & 3) * 16);
#pragma unroll
        for (int i = 0; i < 4; ++i) {
            const int tk = 4 * w + i;
            lds_w128(lds + 16384 + tk * 1024 + ((l ^ (tk & 15)) * 16), ov[i]);
        }
        if (t < 256) lds_w128(lds + 49152 + (t >> 2) * 64 + (t & 3) * 16, y);
    }
    bar();
    STAMP(11);
    f32x4 acc = {0, 0, 0, 0}, acc2 = {0, 0, 0, 0};
    {
        u32 ob[4];
#pragma unroll
        for (int k4 = 0; k4 < 4; ++k4) ob[k4] = 16384 + (16 * tt + r16) * 1024 + ((((4 * k4) ^ (r16 & 12)) | (g ^ (r16 & 3))) * 16);
#pragma unroll
        for (int ks = 0; ks < 16; ks += 2) {
            acc = mfma32(wf[ks], lds_r128(lds + ob[ks & 3] + (ks >> 2) * 256), acc);
            acc2 = mfma32(wf[ks + 1], lds_r128(lds + ob[(ks + 1) & 3] + (ks >> 2) * 256), acc2);
        }
    }
    {
        u32x2 y = trread(lds + 49152 + (16 * cw + 4 * g + (r16 >> 2)) * 64 + tt * 32 + 8 * (l & 3));
        const float gg = gate, g1 = rb(1.f - gg);
        u32 t0 = pk2(blo(uu.x) * blo(dsk.x), bhi(uu.x) * bhi(dsk.x)), t1 = pk2(blo(uu.y) * blo(dsk.y), bhi(uu.y) * bhi(dsk.y));
        u32 a0 = pk2(blo(y.x) + blo(t0), bhi(y.x) + bhi(t0)), a1 = pk2(blo(y.y) + blo(t1), bhi(y.y) + bhi(t1));
        u32 b0 = pk2(acc[0] + acc2[0], acc[1] + acc2[1]), b1 = pk2(acc[2] + acc2[2], acc[3] + acc2[3]);
        u32 ga0 = pk2(gg * blo(a0), gg * bhi(a0)), ga1 = pk2(gg * blo(a1), gg * bhi(a1));
        u32 gb0 = pk2(g1 * blo(b0), g1 * bhi(b0)), gb1 = pk2(g1 * blo(b1), g1 * bhi(b1));
        u32x2 o = {pk2(blo(ga0) + blo(gb0), bhi(ga0) + bhi(gb0)), pk2(blo(ga1) + blo(gb1), bhi(ga1) + bhi(gb1))};
        lds_w64(lds + 53248 + (16 * tt + r16) * 144 + 32 * cw + 8 * g, o);
    }
    bar();
    if (t < 256) {
        u32x4 ov = lds_r128(lds + 53248 + (t >> 3) * 144 + (t & 7) * 16);
        __builtin_amdgcn_raw_buffer_store_b128(ov, rout, (tok0 + (t >> 3)) * 1024 + ct * 128 + (t & 7) * 16, 0, 0);
    }
    STAMP(12);
}

__global__ __launch_bounds__(512, 4) void s4d_hedgehog_mix(Params p) {
    extern __shared__ __attribute__((aligned(16))) char smem[];
    ldsp lds = (ldsp)smem;
    char* ws = p.ws;
    const int b = blockIdx.x, l = threadIdx.x & 63, w = uni(threadIdx.x >> 6);
    STAMP(0);
    asm volatile("" :: "s"(p.ws), "s"(p.u), "s"(p.wqkv), "s"(p.ldt), "s"(p.gw));
    const u64 e = flag_ld(ws, WS_E);
    u64 tag;
    const int jj = b >> 3, tt32 = (b & 7) * 8 + (jj & 7), ct = jj >> 3;
    if (b >= 256) {
        tag = role_conv(p, lds, ws, e, b - 256, w, l STAMP_PASS);
        bar();
        wo_prefetch<8>(p, lds, ct, w, l);
    } else {
        tag = role_attn(p, lds, ws, e, b, w, l, ct STAMP_PASS);
    }
    final_stage(p, lds, ws, tag, b, w, l, tt32, ct STAMP_PASS);
}

#if defined(__HIP_DEVICE_COMPILE__)
#pragma clang attribute pop
#endif

extern "C" void kernel_launch(void* const* d_in, const int* in_sizes, int n_in, void* d_out, int out_size, void* d_ws, size_t ws_size,
                              hipStream_t stream) {
    Params p;
    p.u = (const u16*)d_in[0]; p.lar = (const u16*)d_in[1]; p.aim = (const u16*)d_in[2]; p.cre = (const u16*)d_in[3];
    p.cim = (const u16*)d_in[4]; p.ldt = (const u16*)d_in[5]; p.dsk = (const u16*)d_in[6]; p.wqkv = (const u16*)d_in[8];
    p.wphi = (const u16*)d_in[9]; p.wo = (const u16*)d_in[10]; p.gw = (const u16*)d_in[11]; p.out = (u16*)d_out; p.ws = (char*)d_ws;
    (void)hipFuncSetAttribute((const void*)s4d_hedgehog_mix, hipFuncAttributeMaxDynamicSharedMemorySize, LDS_BYTES);
    s4d_hedgehog_mix<<<512, 512, LDS_BYTES, stream>>>(p);
}
```

```cpp
#include <hip/hip_runtime.h>
#include <hip/hip_bf16.h>
#include <stdint.h>

#if defined(__HIP_DEVICE_COMPILE__)
#pragma clang attribute push(__attribute__((target("no-packed-fp32-ops"))), apply_to = function)
#endif

typedef unsigned short u16;
typedef unsigned u32;
typedef unsigned long long u64;
typedef float f32x2 __attribute__((ext_vector_type(2)));
typedef float f32x4 __attribute__((ext_vector_type(4)));
typedef u32 u32x2 __attribute__((ext_vector_type(2)));
typedef u32 u32x4 __attribute__((ext_vector_type(4)));
typedef short s16x4 __attribute__((ext_vector_type(4)));
typedef __bf16 bf16x2 __attribute__((ext_vector_type(2)));
typedef __bf16 bf16x8 __attribute__((ext_vector_type(8)));
#define LAS __attribute__((address_space(3)))
#define GAS __attribute__((address_space(1)))
typedef LAS char* ldsp;
typedef __amdgpu_buffer_rsrc_t rsrc_t;
#define DEV __device__ __forceinline__

constexpr int L = 2048, D = 512;
constexpr int LDS_BYTES = 81920;
constexpr u64 TAG_STEP = 0x9E3779B97F4A7C15ull;
constexpr u32 SPIN_CAP = 40000;

constexpr u32 WS_E = 0, WS_TMO = 64;
constexpr u32 WS_FUT = 4096, WS_FKV = WS_FUT + 4096, WS_FY = WS_FKV + 4096, WS_FO = WS_FY + 4096;
constexpr u32 WS_G = 0x28000;
constexpr u32 WS_UT = 0x40000;
constexpr u32 WS_YT = WS_UT + 0x200000;
constexpr u32 WS_OP = WS_YT + 0x200000;
constexpr u32 WS_KVP = WS_OP + 0x200000;
constexpr u32 WS_ZP = WS_KVP + 0x100000;
constexpr u32 WS_DBG = WS_ZP + 0x10000;
constexpr u32 WS_USED = WS_DBG + 0x400000;

constexpr int AUX_SC1 = 16;

struct IdxTab { u16 v[2048]; };
constexpr IdxTab make_idx() {
    IdxTab t{};
    for (int i = 0; i < 2048; ++i) {
        int j = 2047 - i, p = j;
        int sh = j >= 1024 ? 3 : (j >= 512 ? 2 : (j >= 256 ? 1 : 0));
        if (sh) {
            int q = j >> sh, rem = j & ((1 << sh) - 1), half = 1 << (sh - 1);
            if (rem > half || (rem == half && (q & 1))) ++q;
            p = q << sh;
        }
        int row = 0, col = (p >> 3) & 15;
        if (p < 256) row = (p & 7) + 8 * (p >> 7);
        else if (p < 512) row = 16 + ((p - 256) >> 7) * 4 + ((p & 7) >> 1);
        else if (p < 1024) row = 24 + ((p - 512) >> 7) * 2 + ((p & 7) >> 2);
        else row = 32 + ((p - 1024) >> 7);
        t.v[i] = (u16)((row * 16 + col) * 2);
    }
    return t;
}
__device__ const IdxTab g_idx = make_idx();

DEV float u2f(u32 x) { return __builtin_bit_cast(float, x); }
DEV u32 f2u(float x) { return __builtin_bit_cast(u32, x); }
DEV float fexp(float x) { return __builtin_amdgcn_exp2f(x * 1.4426950408889634f); }
template <int CTRL> DEV float dpp_mov(float x) { return u2f(__builtin_amdgcn_update_dpp(0u, f2u(x), CTRL, 0xf, 0xf, true)); }
DEV float row16_sum(float x) {
    x += dpp_mov<0xB1>(x);
    x += dpp_mov<0x4E>(x);
    x += dpp_mov<0x141>(x);
    x += dpp_mov<0x140>(x);
    return x;
}
DEV u32 pk2(float lo, float hi) { f32x2 v = {lo, hi}; return __builtin_bit_cast(u32, __builtin_convertvector(v, bf16x2)); }
DEV float blo(u32 x) { return u2f(x << 16); }
DEV float bhi(u32 x) { return u2f(x & 0xffff0000u); }
DEV float rb(float f) { return blo(pk2(f, 0.f)); }
DEV f32x4 mfma32(u32x4 a, u32x4 b, f32x4 c) {
    return __builtin_amdgcn_mfma_f32_16x16x32_bf16(__builtin_bit_cast(bf16x8, a), __builtin_bit_cast(bf16x8, b), c, 0, 0, 0);
}
DEV f32x4 mfma16(u32x2 a, u32x2 b, f32x4 c) {
    return __builtin_amdgcn_mfma_f32_16x16x16bf16_1k(__builtin_bit_cast(s16x4, a), __builtin_bit_cast(s16x4, b), c, 0, 0, 0);
}
DEV u32x2 trread(ldsp p) { return __builtin_bit_cast(u32x2, __builtin_amdgcn_ds_read_tr16_b64_v4i16((LAS s16x4*)p)); }
DEV u32x4 lds_r128(ldsp p) { return *(LAS u32x4*)p; }
DEV u32x2 lds_r64(ldsp p) { return *(volatile LAS u32x2*)p; }
DEV void lds_w128(ldsp p, u32x4 v) { *(LAS u32x4*)p = v; }
DEV void lds_w64(ldsp p, u32x2 v) { *(LAS u32x2*)p = v; }
DEV void lds_w32(ldsp p, u32 v) { *(LAS u32*)p = v; }
DEV u32x4 cat(u32x2 a, u32x2 b) { u32x4 r = {a.x, a.y, b.x, b.y}; return r; }
DEV rsrc_t mkrsrc(const void* p, u32 bytes) { return __builtin_amdgcn_make_buffer_rsrc((void*)p, 0, bytes, 0x00020000); }
DEV u32x4 mkdesc(const void* p, u32 bytes) {
    u64 a = (u64)p;
    u32x4 d = {(u32)a, (u32)(a >> 32) & 0xffffu, bytes, 0x00020000u};
    return d;
}
DEV void dma16(u32x4 desc, ldsp dst, u32 voff, u32 soff) {
    asm volatile("s_mov_b32 m0, %0\n\ts_nop 0\n\tbuffer_load_dwordx4 %1, %2, %3 offen lds"
                 :: "s"((u32)(uintptr_t)dst), "v"(voff), "s"(desc), "s"(soff) : "memory");
}
DEV u32x4 ld128(rsrc_t r, u32 voff, u32 soff = 0) { return __builtin_amdgcn_raw_buffer_load_b128(r, voff, soff, 0); }
DEV u32x2 ld64(rsrc_t r, u32 voff, u32 soff = 0) { return __builtin_amdgcn_raw_buffer_load_b64(r, voff, soff, 0); }
DEV u32 ld16(rsrc_t r, u32 voff, u32 soff = 0) { return __builtin_amdgcn_raw_buffer_load_b16(r, voff, soff, 0); }
DEV u32x4 ld128c(rsrc_t r, u32 voff, u32 soff = 0) { return __builtin_amdgcn_raw_buffer_load_b128(r, voff, soff, AUX_SC1); }
DEV u32x2 ld64c(rsrc_t r, u32 voff, u32 soff = 0) { return __builtin_amdgcn_raw_buffer_load_b64(r, voff, soff, AUX_SC1); }
DEV u32 ld32c(rsrc_t r, u32 voff, u32 soff = 0) { return __builtin_amdgcn_raw_buffer_load_b32(r, voff, soff, AUX_SC1); }
DEV void st128c(rsrc_t r, u32x4 v, u32 voff, u32 soff = 0) { __builtin_amdgcn_raw_buffer_store_b128(v, r, voff, soff, AUX_SC1); }
DEV void st64c(rsrc_t r, u32x2 v, u32 voff, u32 soff = 0) { __builtin_amdgcn_raw_buffer_store_b64(v, r, voff, soff, AUX_SC1); }
#define WAIT_VM(n) asm volatile("s_waitcnt vmcnt(" #n ")" ::: "memory")
#define SCHED_A() __builtin_amdgcn_sched_barrier(0)
#define WAIT_LGKM0() asm volatile("s_waitcnt lgkmcnt(0)" ::: "memory")
DEV void bar() { asm volatile("s_waitcnt lgkmcnt(0)" ::: "memory"); __builtin_amdgcn_s_barrier(); asm volatile("" ::: "memory"); }
#define PRIO_A0 3
#define PRIO_A1 1
#define PRIO_BH2 3
#define PRIO_F 3
#define PRIO_BC 2
#define PRIO_BH 0
DEV u32 uni(u32 x) { return __builtin_amdgcn_readfirstlane(x); }

DEV float swap16_lo(float a, float b, float& other) {
    auto s = __builtin_amdgcn_permlane16_swap(f2u(a), f2u(b), false, false);
    other = u2f(s[1]);
    return u2f(s[0]);
}
DEV float swap32_lo(float a, float b, float& other) {
    auto s = __builtin_amdgcn_permlane32_swap(f2u(a), f2u(b), false, false);
    other = u2f(s[1]);
    return u2f(s[0]);
}
template <bool MAX> DEV void rows_reduce2(float& a, float& b) {
    float o, t = swap16_lo(a, b, o);
    t = MAX ? __builtin_fmaxf(t, o) : t + o;
    float u = swap32_lo(t, t, o);
    u = MAX ? __builtin_fmaxf(u, o) : u + o;
    a = swap16_lo(u, u, b);
}

typedef GAS u64 gu64;
DEV u64 make_tag(u64 e) {
    u32 lo = (u32)e, hi = (u32)(e >> 32);
    asm volatile("" : "+v"(lo), "+v"(hi));
    return (((u64)uni(hi) << 32) | uni(lo)) + TAG_STEP;
}
DEV u64 flag_ld(char* ws, u32 off) { return __hip_atomic_load((gu64*)(ws + off), __ATOMIC_RELAXED, __HIP_MEMORY_SCOPE_AGENT); }
DEV void flag_st(char* ws, u32 off, u64 v) { __hip_atomic_store((gu64*)(ws + off), v, __ATOMIC_RELAXED, __HIP_MEMORY_SCOPE_AGENT); }
DEV void wait_flags(char* ws, u32 off, u64 tag) {
#pragma unroll 1
    for (u32 spins = 0; spins < SPIN_CAP; ++spins) {
        u64 v = flag_ld(ws, off);
        if (__builtin_amdgcn_ballot_w64(v != tag) == 0) { asm volatile("" ::: "memory"); return; }
        __builtin_amdgcn_s_sleep(1);
    }
    flag_st(ws, WS_TMO, tag);
}
DEV void publish(char* ws, u32 off, u64 tag) {
    WAIT_VM(0);
    bar();
    if (threadIdx.x == 0) flag_st(ws, off, tag);
}

DEV void publish_counted(char* ws, u32 off, u64 tag, ldsp cnt, u32 n, int l) {
    WAIT_VM(0);
    u32 old = 0;
    if (l == 0) old = __hip_atomic_fetch_add((LAS u32*)cnt, 1u, __ATOMIC_RELAXED, __HIP_MEMORY_SCOPE_WORKGROUP);
    if (l == 0 && old == n - 1) flag_st(ws, off, tag);
}

#define STAMP(i) asm volatile("; MARK " #i)
#define FSTAMP(i) do { } while (0)
#define FS1(i) do { } while (0)
#define FS2(i) do { } while (0)
#define FS3(i) do { } while (0)
#define FS4(i) do { } while (0)
#define STAMP_ARG
#define STAMP_PASS

struct Params {
    char* ws; const u16* u; const u16* wqkv; const u16* wphi; const u16* lar; const u16* aim; const u16* cre; const u16* cim;
    const u16* ldt; const u16* gw; const u16* dsk; const u16* wo; u16* out;
};

struct cplx { float r, i; };
DEV cplx cmul(cplx a, cplx b) { cplx c; c.r = a.r * b.r - a.i * b.i; c.i = a.r * b.i + a.i * b.r; return c; }
DEV cplx cexp_p(float zr2, float zrev, float p) {
    float e = __builtin_amdgcn_exp2f(zr2 * p);
    float a = __builtin_amdgcn_fractf(zrev * p);
    cplx c; c.r = e * __builtin_amdgcn_cosf(a); c.i = e * __builtin_amdgcn_sinf(a); return c;
}

template <int NW> DEV void wo_prefetch(const Params& p, ldsp lds, int ct, int w, int l) {
    const u32x4 rwo = mkdesc(p.wo, D * D * 2);
    u32 voff = (l >> 3) * 1024 + ct * 128 + ((((l & 7) >> 1) ^ ((l >> 4) & 3)) * 32) + (l & 1) * 16;
#pragma unroll
    for (int i = 0; i < 64 / NW; ++i) {
        int piece = i * NW + w;
        dma16(rwo, lds + 16384 + piece * 1024, voff, piece * 8192);
    }
}

DEV u64 role_conv(const Params& p, ldsp lds, char* ws, u64 e, int ba, int w, int l STAMP_ARG) {
    const int t = threadIdx.x, xcd = ba & 7, j = ba >> 3, g = l >> 4, r16 = l & 15;
    const int a = xcd >> 2, c = xcd & 3;
    rsrc_t ru = mkrsrc(p.u, L * D * 2), rws = mkrsrc(ws, WS_USED);
    const int ch0 = 2 * (32 * xcd + j);
    __builtin_amdgcn_s_setprio(PRIO_A0);
    const int tb = 16 * a + (j >> 1), cg = 2 * c + (j & 1);
    {
        u32 voff = (tb * 64 + (l >> 3)) * 1024 + cg * 128 + (l & 7) * 16;
        dma16(mkdesc(p.u, L * D * 2), lds + 70656 + w * 1024, voff, w * 8192);
    }
    const int gtok = 1024 * a + 8 * (32 * c + j) + w;
    u32x4 gx = ld128(ru, gtok * 1024 + l * 16);
    u32x4 gwv = *(const GAS u32x4*)(p.gw + l * 8);
    const int kch = (t >> 7) & 1, kn = t & 127;
    const int pidx = (ch0 + kch) * 128 + kn;
    float lar = blo(p.lar[pidx]), aim = blo(p.aim[pidx]), cre = blo(p.cre[pidx]), cim = blo(p.cim[pidx]), ldt = blo(p.ldt[ch0 + kch]);
    u32x4 idx = *(const GAS u32x4*)(g_idx.v + (t & 255) * 8);
    SCHED_A();
    __builtin_amdgcn_s_setprio(PRIO_A1);
    WAIT_VM(0);
    const u64 tag = make_tag(e);
    bar();
    STAMP(1);
    {
        const int cw = w & 3, s = w >> 2;
        ldsp src = lds + 70656 + (32 * s + 8 * g + (r16 >> 2)) * 128 + 32 * cw + 8 * (l & 3);
        u32x4 v = cat(trread(src), trread(src + 4 * 128));
        st128c(rws, v, WS_UT + (cg * 64 + 16 * cw + r16) * 4096 + (tb * 64 + 32 * s + 8 * g) * 2);
    }
    {
        float s0 = 0.f, s1 = 0.f;
#pragma unroll
        for (int q = 0; q < 4; ++q) {
            u32 pr = pk2(blo(gx[q]) * blo(gwv[q]), bhi(gx[q]) * bhi(gwv[q]));
            s0 += blo(pr); s1 += bhi(pr);
        }
        s0 = row16_sum(s0 + s1);
        s1 = s0;
        rows_reduce2<false>(s0, s1);
        float g0 = rb(__builtin_amdgcn_rcpf(1.f + fexp(-rb(s0))));
        if (l == 0) __builtin_amdgcn_raw_buffer_store_b32(f2u(g0), rws, WS_G + gtok * 4, 0, AUX_SC1);
    }
    STAMP(2);
    {
        const float dt = rb(fexp(ldt));
        const float zr2 = -rb(fexp(lar)) * dt * 1.4426950408889634f, zrev = aim * dt * 0.15915494309189535f;
        ldsp T = lds + kch * 33792 + kn * 4;
        auto put = [&](int row, cplx v) { lds_w32(T + row * 528, pk2(v.r, v.i)); };
        const cplx w128 = cexp_p(zr2, zrev, 128.f);
        cplx G; G.r = 2.f * cre * dt; G.i = 2.f * cim * dt;
        if (w < 4) {
            cplx wp[8];
            wp[1] = cexp_p(zr2, zrev, 1.f); wp[2] = cmul(wp[1], wp[1]); wp[3] = cmul(wp[2], wp[1]); wp[4] = cmul(wp[2], wp[2]);
            wp[5] = cmul(wp[4], wp[1]); wp[6] = cmul(wp[4], wp[2]); wp[7] = cmul(wp[4], wp[3]);
#pragma unroll
            for (int aa = 0; aa < 4; ++aa) {
                if (aa == 1) { FS4(7); publish(ws, WS_FUT + cg * 256 + tb * 8, tag); FS4(8); }
                if (aa < 2) {
                    put(8 * aa, G);
#pragma unroll
                    for (int cc = 1; cc < 8; ++cc) put(8 * aa + cc, cmul(G, wp[cc]));
                } else {
                    put(16 + 4 * (aa - 2), G);
#pragma unroll
                    for (int cc = 1; cc < 4; ++cc) put(16 + 4 * (aa - 2) + cc, cmul(G, wp[2 * cc]));
                }
                if (aa < 3) G = cmul(G, w128);
            }
        } else {
            const cplx w4 = cexp_p(zr2, zrev, 4.f);
            G = cmul(G, cexp_p(zr2, zrev, 512.f));
#pragma unroll
            for (int aa = 4; aa <= 16; ++aa) {
                if (aa == 10) { FS4(7); publish(ws, WS_FUT + cg * 256 + tb * 8, tag); FS4(8); }
                if (aa < 8) {
                    put(24 + 2 * (aa - 4), G);
                    put(24 + 2 * (aa - 4) + 1, cmul(G, w4));
                } else put(32 + aa - 8, G);
                if (aa < 16) G = cmul(G, w128);
            }
            cplx B[9];
            B[1] = cmul(w4, w4); B[2] = cmul(B[1], B[1]); B[3] = cmul(B[2], B[1]); B[4] = cmul(B[2], B[2]);
            B[5] = cmul(B[4], B[1]); B[6] = cmul(B[4], B[2]); B[7] = cmul(B[4], B[3]); B[8] = cmul(B[4], B[4]);
            lds_w32(T + 48 * 528, 0x00003f80u);
#pragma unroll
            for (int b = 1; b < 16; ++b) {
                cplx v = b <= 8 ? B[b] : cmul(B[8], B[b - 8]);
                lds_w32(T + (48 + b) * 528, pk2(v.r, -v.i));
            }
        }
    }
    FS4(9);
    bar();
    FS4(10);
    if (w == 7) wait_flags(ws, WS_FUT + xcd * 256 + (l & 31) * 8, tag);
    if (w < 6) {
        const int ch = w >= 3, tile = w - 3 * ch;
        ldsp T = lds + ch * 33792 + r16 * 528 + g * 16;
        f32x4 k0 = {0, 0, 0, 0};
#pragma unroll
        for (int s = 0; s < 8; ++s) k0 = mfma32(lds_r128(T + tile * 16 * 528 + s * 64), lds_r128(T + 48 * 528 + s * 64), k0);
        ldsp KC = lds + 67584 + ch * 1536 + r16 * 2 + (tile * 16 + 4 * g) * 32;
        u32 a0 = pk2(k0[0], k0[1]), a1 = pk2(k0[2], k0[3]);
        *(LAS u16*)(KC) = (u16)a0; *(LAS u16*)(KC + 32) = (u16)(a0 >> 16);
        *(LAS u16*)(KC + 64) = (u16)a1; *(LAS u16*)(KC + 96) = (u16)(a1 >> 16);
    }
    bar();
    STAMP(3);
    const int hch = w >> 2, t8 = t & 255;
    const u32 uoff = WS_UT + (ch0 + hch) * 4096 + t8 * 16;
    u32x4 d0 = ld128c(rws, uoff), d1 = ld128c(rws, uoff + 4), d2 = ld128c(rws, uoff + 8);
    u32x2 f = ld64c(rws, WS_UT + (ch0 + hch) * 4096);
    SCHED_A();
    {
        ldsp KC = lds + 67584 + hch * 1536;
        u32x4 v;
#pragma unroll
        for (int q = 0; q < 4; ++q) {
            u32 lo = *(LAS u16*)(KC + (idx[q] & 0xffffu)), hi = *(LAS u16*)(KC + (idx[q] >> 16));
            v[q] = lo | (hi << 16);
        }
        lds_w128(lds + hch * 4608 + t8 * 16, v);
        if (t8 < 32) { u32x4 z = {0, 0, 0, 0}; lds_w128(lds + hch * 4608 + 4096 + t8 * 16, z); }
    }
    SCHED_A();
    STAMP(4);
    constexpr int UB = 9216, CS = 4160;
    {
        u32x4 v1, v3;
#pragma unroll
        for (int q = 0; q < 4; ++q) { v1[q] = __builtin_amdgcn_alignbit(d1[q], d0[q], 16); v3[q] = __builtin_amdgcn_alignbit(d2[q], d1[q], 16); }
        ldsp U = lds + UB + hch * 4 * CS + 32 + t8 * 16;
        lds_w128(U, d0); lds_w128(U + CS, v1); lds_w128(U + 2 * CS, d1); lds_w128(U + 3 * CS, v3);
        if (t8 < 4) {
            u32x4 z = {0, 0, 0, 0}, y = z;
            if (t8 == 1) y.w = f.x << 16;
            if (t8 == 2) y.w = f.x;
            if (t8 == 3) { y.z = f.x << 16; y.w = __builtin_amdgcn_alignbit(f.y, f.x, 16); }
            ldsp F = lds + UB + (hch * 4 + t8) * CS;
            lds_w128(F, z); lds_w128(F + 16, y);
        }
    }
    bar();
    STAMP(5);
    {
        const int mq = w & 3;
        const int s = (r16 + 1) & 3;
        ldsp A0 = lds + UB + (hch * 4 + s) * CS + 2 * (1 + r16 - s + 8 * g) + mq * 2 * 64;
        ldsp B0 = lds + hch * 4608 + 4064 - 3584 - 32 * r16 + 16 * g + mq * 2 * 64;
        f32x4 acc[8];
#pragma unroll
        for (int i = 0; i < 8; ++i) acc[i] = f32x4{0, 0, 0, 0};
#pragma unroll
        for (int mm = 0; mm < 2; ++mm) {
            u32x4 Af[8], Bf[8];
#pragma unroll
            for (int i = 0; i < 8; ++i) {
                Bf[i] = lds_r128(B0 + mm * 64 + (7 - i) * 512);
                Af[i] = cat(lds_r64(A0 + mm * 64 + i * 512), lds_r64(A0 + mm * 64 + i * 512 + 8));
            }
#pragma unroll
            for (int i = 0; i < 8; ++i)
#pragma unroll
                for (int cc = i; cc < 8; ++cc) acc[cc] = mfma32(Af[i], Bf[cc - i], acc[cc]);
            __builtin_amdgcn_sched_barrier(0);
        }
        FSTAMP(7);
        bar();
        FSTAMP(8);
        ldsp R = lds + hch * 24576 + l * 16;
#pragma unroll
        for (int cc = 0; cc < 8; ++cc)
            if ((cc >> 1) != mq) lds_w128(R + (cc * 3 + ((mq - (cc >> 1) - 1) & 3)) * 1024, __builtin_bit_cast(u32x4, acc[cc]));
        bar();
        FSTAMP(9);
#pragma unroll
        for (int q = 0; q < 4; ++q)
            if (q == mq) {
                u32 pk[2][2];
#pragma unroll
                for (int h = 0; h < 2; ++h) {
                    float v[4];
#pragma unroll
                    for (int r = 0; r < 4; ++r) v[r] = acc[2 * q + h][r];
#pragma unroll
                    for (int sl = 0; sl < 3; ++sl) {
                        u32x4 o = lds_r128(R + ((2 * q + h) * 3 + sl) * 1024);
#pragma unroll
                        for (int r = 0; r < 4; ++r) v[r] += u2f(o[r]);
                    }
                    pk[h][0] = pk2(v[0], v[1]); pk[h][1] = pk2(v[2], v[3]);
                }
                auto sx = __builtin_amdgcn_permlane16_swap(pk[0][0], pk[1][0], false, false);
                auto sy = __builtin_amdgcn_permlane16_swap(pk[0][1], pk[1][1], false, false);
                u32x4 v = {sx[0], sy[0], sx[1], sy[1]};
                st128c(rws, v, WS_YT + (ch0 + hch) * 4096 + (256 * (2 * q + (g & 1)) + 16 * r16 + 4 * (g & 2)) * 2);
            }
    }
    FSTAMP(10);
    publish(ws, WS_FY + xcd * 256 + j * 8, tag);
    STAMP(6);
    return tag;
}

typedef float f32x16 __attribute__((ext_vector_type(16)));
DEV f32x16 mfma32x32(u32x4 a, u32x4 b, f32x16 c) {
    return __builtin_amdgcn_mfma_f32_32x32x16_bf16(__builtin_bit_cast(bf16x8, a), __builtin_bit_cast(bf16x8, b), c, 0, 0, 0);
}
DEV float vmax3(float a, float b, float c) { float r; asm("v_max3_f32 %0, %1, %2, %3" : "=v"(r) : "v"(a), "v"(b), "v"(c)); return r; }
DEV float vmin3(float a, float b, float c) { float r; asm("v_min3_f32 %0, %1, %2, %3" : "=v"(r) : "v"(a), "v"(b), "v"(c)); return r; }

template <bool POS, bool NEG> DEV void feature_map(u32x4 x0, u32x4 x1, u32x4 wown, int l, u32 (&ph)[16]) {
    const bool odd = (l >> 2) & 1;
    u32x4 a0, a1;
#pragma unroll
    for (int q = 0; q < 4; ++q) { a0[q] = odd ? 0u : wown[q]; a1[q] = odd ? wown[q] : 0u; }
    f32x16 pr;
#pragma unroll
    for (int r = 0; r < 16; ++r) pr[r] = 0.f;
    pr = mfma32x32(a0, x0, pr);
    pr = mfma32x32(a1, x1, pr);
    float p[16];
#pragma unroll
    for (int r = 0; r < 16; r += 2) { u32 v = pk2(pr[r], pr[r + 1]); p[r] = blo(v); p[r + 1] = bhi(v); }
    constexpr float LG = 1.4426950408889634f;
#pragma unroll
    for (int sg = 0; sg < 2; ++sg) {
        if (sg == 0 ? !POS : !NEG) continue;
        float m = sg ? vmin3(p[0], p[1], p[2]) : vmax3(p[0], p[1], p[2]);
#pragma unroll
        for (int r = 3; r < 15; r += 2) m = sg ? vmin3(m, p[r], p[r + 1]) : vmax3(m, p[r], p[r + 1]);
        m = sg ? vmin3(m, p[15], p[15]) : vmax3(m, p[15], p[15]);
        const float sc = sg ? -LG : LG, c = -m * sc;
        float a[16];
#pragma unroll
        for (int r = 0; r < 16; ++r) a[r] = __builtin_fmaf(p[r], sc, c);
#pragma unroll
        for (int r = 0; r < 16; ++r) a[r] = __builtin_amdgcn_exp2f(a[r]);
        float sa[4];
#pragma unroll
        for (int q = 0; q < 4; ++q) sa[q] = (a[4 * q] + a[4 * q + 1]) + (a[4 * q + 2] + a[4 * q + 3]);
        const float ia = __builtin_amdgcn_rcpf((sa[0] + sa[1]) + (sa[2] + sa[3]));
#pragma unroll
        for (int r = 0; r < 8; ++r) ph[8 * sg + r] = pk2(a[2 * r] * ia, a[2 * r + 1] * ia);
    }
}

DEV void role_attn(const Params& p, ldsp lds, char* ws, u64 e, int bb, int w, int l STAMP_ARG) {
    const int xcd = bb & 7, j = bb >> 3, g = l >> 4, r16 = l & 15, h = l >> 5, w4 = w & 3;
    const int rb_ = 8 * (xcd >> 2) + (j >> 2), cb = 4 * (xcd & 3) + (j & 3);
    const int rot = (j + xcd) & 7;
    rsrc_t rws = mkrsrc(ws, WS_USED);
#define SCHED() __builtin_amdgcn_sched_barrier(0)
    constexpr int XCH = 32768, WPH = 49152, PH = 51200, PHS = 10240, VV = 71680, VS = 5120, FR = 0, CNT = 8192;
    const int tok = 32 * w4 + (l & 31);
    const u32 wph = WPH + (l & 1) * 512 + (4 * h + (r16 >> 2)) * 32 + (8 * (g & 1) + 4 * ((l & 3) >> 1)) * 2;
    u64 tag;
    if (w >= 4) {
        __builtin_amdgcn_s_setprio(PRIO_BH);
        const u32x4 ru = mkdesc(p.u, L * D * 2), rw = mkdesc(p.wqkv, D * 3 * D * 2);
        const u32 xv = (rb_ * 128 + w4 * 8 + (l >> 3)) * 1024 + (((l & 7) ^ (((w4 & 1) << 2) | ((l >> 4) & 3))) * 16);
        const u32 wv = (16 * w4 + (l >> 2)) * 3072 + cb * 64 + (l & 3) * 16;
        auto issue_x = [&](int s) {
#pragma unroll
            for (int i = 0; i < 4; ++i) dma16(ru, lds + (s % 3) * 16384 + (4 * i + w4) * 1024, xv, ((s + rot) & 7) * 128 + i * 32768);
        };
        auto issue_w = [&](int s, int sec) {
            dma16(rw, lds + 49152 + ((3 * s + sec) & 7) * 4096 + w4 * 1024, wv, ((s + rot) & 7) * (64 * 3072) + sec * 1024);
        };
        FS3(6);
        issue_w(0, 0); issue_w(0, 1); issue_w(0, 2); issue_x(0);
        FS3(7);
        __builtin_amdgcn_s_barrier();
        FS3(8);
        issue_w(1, 0); issue_w(1, 1); issue_w(1, 2); issue_x(1);
        FS3(9);
#pragma unroll
        for (int s = 0; s < 8; ++s) {
            if (s == 0) WAIT_VM(7); else if (s < 7) WAIT_VM(6); else WAIT_VM(0);
            if (s == 0) FS3(10);
            __builtin_amdgcn_s_barrier();
            if (s >= 1 && s + 1 < 8) issue_w(s + 1, 2);
            if (s + 2 < 8) { issue_x(s + 2); issue_w(s + 2, 0); issue_w(s + 2, 1); }
            if (s == 6 && w == 4) dma16(mkdesc(p.wphi, 32 * 256 * 2), lds + WPH, l * 16, cb * 1024);
            if (s == 7 && w == 4 && l < 2) lds_w32(lds + CNT + l * 4, 0u);
        }
        tag = make_tag(e);
        bar();
        ldsp X = lds + XCH + w4 * 4096 + l * 16;
        u32x4 wown = cat(trread(lds + wph), trread(lds + wph + 8 * 32));
        u32x4 q0 = lds_r128(X), q1 = lds_r128(X + 1024);
        bar();
        bar();
        u32 ph[16];
        feature_map<true, true>(q0, q1, wown, l, ph);
        u32x4 bq[2][2];
#pragma unroll
        for (int sg = 0; sg < 2; ++sg)
#pragma unroll
            for (int q = 0; q < 4; ++q) {
                auto sw = __builtin_amdgcn_permlane32_swap(ph[8 * sg + q], ph[8 * sg + q + 4], false, false);
                bq[0][sg][q] = sw[0]; bq[1][sg][q] = sw[1];
            }
        STAMP(3);
        __builtin_amdgcn_s_setprio(PRIO_BH2);
        bar();
#pragma unroll
        for (int hh = 0; hh < 2; ++hh) {
            f32x16 nd;
#pragma unroll
            for (int r = 0; r < 16; ++r) nd[r] = 0.f;
            nd = mfma32x32(lds_r128(lds + FR + (2 * hh) * 1024 + l * 16), bq[hh][0], nd);
            nd = mfma32x32(lds_r128(lds + FR + (2 * hh + 1) * 1024 + l * 16), bq[hh][1], nd);
            const float inv = __builtin_amdgcn_rcpf(rb(rb(nd[8]) + 1e-6f));
            u32x4 o;
#pragma unroll
            for (int q = 0; q < 4; ++q) { u32 n = pk2(nd[2 * q], nd[2 * q + 1]); o[q] = pk2(blo(n) * inv, bhi(n) * inv); }
            st128c(rws, o, WS_OP + (rb_ * 128 + tok) * 1024 + cb * 64 + hh * 32 + h * 16);
        }
        FS2(10);
        publish_counted(ws, WS_FO + rb_ * 256 + cb * 8, tag, lds + CNT + 4, 4, l);
        STAMP(5);
    } else {
        __builtin_amdgcn_s_setprio(PRIO_BC);
        f32x16 acc[3];
#pragma unroll
        for (int sec = 0; sec < 3; ++sec)
#pragma unroll
            for (int r = 0; r < 16; ++r) acc[sec][r] = 0.f;
        const u32 xa = tok * 128 + ((h ^ ((tok >> 1) & 7)) * 16);
        const u32 wa = 49152 + (8 * h + (r16 >> 2)) * 64 + (g & 1) * 32 + 8 * (l & 3);
        __builtin_amdgcn_s_barrier();
#pragma unroll
        for (int s = 0; s < 8; ++s) {
            __builtin_amdgcn_s_barrier();
            asm volatile("" ::: "memory");
            if (s == 0) FS1(6);
            if (s == 1) FS1(7);
            if (s == 2) FS1(8);
            if (s == 4) FS1(9);
            if (s == 6) FS1(10);
            u32x4 xf[4], wf[12];
            auto read_x = [&](int ks) { xf[ks] = lds_r128(lds + (s % 3) * 16384 + (xa ^ (32 * ks))); };
            auto read_w = [&](int i) {
                ldsp a = lds + wa + ((3 * s + i % 3) & 7) * 4096 + (i / 3) * 1024;
                wf[i] = cat(trread(a), trread(a + 256));
            };
            read_x(0); read_w(0); read_w(1); read_w(2);
            SCHED();
#pragma unroll
            for (int i = 0; i < 12; ++i) {
                if (i % 3 == 0 && i / 3 + 1 < 4) read_x(i / 3 + 1);
                if (i + 3 < 12) read_w(i + 3);
                SCHED();
                acc[i % 3] = mfma32x32(wf[i], xf[i / 3], acc[i % 3]);
                SCHED();
            }
        }
        u32x4 fr[3][2];
#pragma unroll
        for (int sec = 0; sec < 3; ++sec)
#pragma unroll
            for (int hh = 0; hh < 2; ++hh)
#pragma unroll
                for (int q = 0; q < 4; ++q) fr[sec][hh][q] = pk2(acc[sec][8 * hh + 2 * q], acc[sec][8 * hh + 2 * q + 1]);
        lds_w128(lds + XCH + w * 4096 + l * 16, fr[0][0]);
        lds_w128(lds + XCH + w * 4096 + 1024 + l * 16, fr[0][1]);
        tag = make_tag(e);
        bar();
        STAMP(1);
#pragma unroll
        for (int hh = 0; hh < 2; ++hh) {
            ldsp q = lds + VV + hh * VS + tok * 40 + 8 * h;
            lds_w64(q, u32x2{fr[2][hh].x, fr[2][hh].y});
            lds_w64(q + 16, u32x2{fr[2][hh].z, fr[2][hh].w});
        }
        {
            u32x4 wown = cat(trread(lds + wph), trread(lds + wph + 8 * 32));
            u32 ph[16];
            feature_map<true, true>(fr[1][0], fr[1][1], wown, l, ph);
            ldsp q = lds + PH + h * PHS + tok * 80;
#pragma unroll
            for (int i = 0; i < 4; ++i) lds_w128(q + i * 16, u32x4{ph[4 * i], ph[4 * i + 1], ph[4 * i + 2], ph[4 * i + 3]});
        }
        FS2(6);
        bar();
        FS2(7);
        const int hh = w >> 1, sg = w & 1;
        {
            f32x4 kv = {0, 0, 0, 0}, z = {0, 0, 0, 0};
            const u32x4 ones = {0x3f803f80u, 0x3f803f80u, 0x3f803f80u, 0x3f803f80u};
            ldsp pa = lds + PH + hh * PHS + (8 * g + (r16 >> 2)) * 80 + sg * 32 + 8 * (l & 3);
            ldsp pb = lds + VV + hh * VS + (8 * g + (r16 >> 2)) * 40 + 8 * (l & 3);
#pragma unroll
            for (int tg = 0; tg < 4; ++tg) {
                u32x4 A = cat(trread(pa + tg * 32 * 80), trread(pa + tg * 32 * 80 + 4 * 80));
                u32x4 B = cat(trread(pb + tg * 32 * 40), trread(pb + tg * 32 * 40 + 4 * 40));
                kv = mfma32(A, B, kv);
                z = mfma32(A, ones, z);
            }
            st128c(rws, __builtin_bit_cast(u32x4, kv), WS_KVP + ((cb * 16 + rb_) * 4 + w) * 1024 + l * 16);
            if (r16 == 0) st128c(rws, __builtin_bit_cast(u32x4, z), WS_ZP + ((cb * 16 + rb_) * 4 + w) * 64 + g * 16);
        }
        FS2(8);
        bar();
        publish_counted(ws, WS_FKV + cb * 256 + rb_ * 8, tag, lds + CNT, 4, l);
        STAMP(2);
        wait_flags(ws, WS_FKV + cb * 256 + r16 * 8, tag);
        STAMP(4);
        u32x4 part[16];
#pragma unroll
        for (int r = 0; r < 16; ++r) part[r] = ld128c(rws, WS_KVP + (cb * 16 * 4 + w) * 1024 + l * 16, r * 4096);
        u32x4 zp = ld128c(rws, WS_ZP + ((cb * 16 + (l >> 2)) * 4 + w) * 64 + (l & 3) * 16);
        float s[4] = {u2f(part[0].x), u2f(part[0].y), u2f(part[0].z), u2f(part[0].w)};
#pragma unroll
        for (int r = 1; r < 16; ++r)
#pragma unroll
            for (int q = 0; q < 4; ++q) s[q] += u2f(part[r][q]);
        float zs[4] = {u2f(zp.x), u2f(zp.y), u2f(zp.z), u2f(zp.w)};
#pragma unroll
        for (int q = 0; q < 4; ++q) {
            float x = zs[q];
            x += dpp_mov<0x124>(x);
            x += dpp_mov<0x128>(x);
            zs[q] = x;
        }
        rows_reduce2<false>(zs[0], zs[1]);
        rows_reduce2<false>(zs[2], zs[3]);
        FS2(9);
        ldsp F = lds + FR + w * 1024;
        lds_w64(F + (r16 + 32 * (g >> 1)) * 16 + 8 * (g & 1), u32x2{pk2(s[0], s[1]), pk2(s[2], s[3])});
        lds_w64(F + (16 + (l >> 2) + 32 * ((l & 3) >> 1)) * 16 + 8 * (l & 1), u32x2{pk2(zs[0], zs[1]), pk2(zs[2], zs[3])});
        bar();
    }
}

DEV void final_stage(const Params& p, ldsp lds, char* ws, u64 tag, int ba, int w, int l STAMP_ARG) {
    const int t = threadIdx.x, g = l >> 4, r16 = l & 15, cw = w & 3, th = w >> 2;
    const int tt64 = 4 * (ba & 7) + ((ba >> 3) & 3), ct = ba >> 5;
    rsrc_t ru = mkrsrc(p.u, L * D * 2), rws = mkrsrc(ws, WS_USED), rout = mkrsrc(p.out, L * D * 2);
    const int tok0 = tt64 * 64, chl = ct * 64 + 16 * cw + 4 * g, tok = tok0 + 32 * th + r16;
    constexpr int YL = 4096;
    bar();
    wo_prefetch<8>(p, lds, ct, w, l);
    u32x2 uu[2];
#pragma unroll
    for (int tt = 0; tt < 2; ++tt) uu[tt] = ld64(ru, (tok + 16 * tt) * 1024 + chl * 2);
    const u32x2 dsk = *(const GAS u32x2*)(p.dsk + chl);
    if (ba == 0 && w >= 4) {
        const int q = t - 256;
        wait_flags(ws, WS_FUT + (q >> 5) * 256 + (q & 31) * 8, tag);
        wait_flags(ws, WS_FKV + (q >> 4) * 256 + (q & 15) * 8, tag);
    }
    if (w == 0) {
        const int tg8 = tt64 * 8 + (l & 7), ga = tg8 >> 7, gc = (tg8 >> 5) & 3, gj = tg8 & 31;
        const int gtb = 16 * ga + (gj >> 1), gcg = 2 * gc + (gj & 1);
        wait_flags(ws, l < 32 ? WS_FY + ct * 256 + l * 8 : WS_FUT + gcg * 256 + gtb * 8, tag);
    }
    WAIT_VM(0);
    bar();
    if (ba == 0 && t == 0) flag_st(ws, WS_E, tag);
    STAMP(8);
    float gate[2];
    {
        u32x4 y = ld128c(rws, WS_YT + (ct * 64 + (t >> 3)) * 4096 + tok0 * 2 + (t & 7) * 16);
#pragma unroll
        for (int tt = 0; tt < 2; ++tt) gate[tt] = u2f(ld32c(rws, WS_G + (tok + 16 * tt) * 4));
        lds_w128(lds + YL + (t >> 3) * 128 + (t & 7) * 16, y);
    }
    u32x4 wf[16];
    {
        const int row = 16 * (g >> 1) + 4 * (g & 1) + (r16 >> 2);
        ldsp q = lds + 16384 + row * 128 + ((cw ^ ((row >> 1) & 3)) * 32) + 8 * (l & 3);
#pragma unroll
        for (int ks = 0; ks < 16; ++ks) wf[ks] = cat(trread(q + ks * 4096), trread(q + ks * 4096 + 1024));
    }
    bar();
    float ga[2][4], g1[2];
#pragma unroll
    for (int tt = 0; tt < 2; ++tt) {
        u32x2 y = trread(lds + YL + (16 * cw + 4 * g + (r16 >> 2)) * 128 + (32 * th + 16 * tt) * 2 + 8 * (l & 3));
        const float gg = gate[tt];
        g1[tt] = rb(1.f - gg);
        u32 t0 = pk2(blo(uu[tt].x) * blo(dsk.x), bhi(uu[tt].x) * bhi(dsk.x)), t1 = pk2(blo(uu[tt].y) * blo(dsk.y), bhi(uu[tt].y) * bhi(dsk.y));
        u32 a0 = pk2(blo(y.x) + blo(t0), bhi(y.x) + bhi(t0)), a1 = pk2(blo(y.y) + blo(t1), bhi(y.y) + bhi(t1));
        u32 ga0 = pk2(gg * blo(a0), gg * bhi(a0)), ga1 = pk2(gg * blo(a1), gg * bhi(a1));
        ga[tt][0] = blo(ga0); ga[tt][1] = bhi(ga0); ga[tt][2] = blo(ga1); ga[tt][3] = bhi(ga1);
        asm volatile("" : "+v"(ga[tt][0]), "+v"(ga[tt][1]), "+v"(ga[tt][2]), "+v"(ga[tt][3]), "+v"(g1[tt]));
    }
    STAMP(9);
    __builtin_amdgcn_s_setprio(PRIO_F);
    if (w == 0) wait_flags(ws, WS_FO + (tok0 >> 7) * 256 + r16 * 8, tag);
    bar();
    STAMP(10);
#pragma unroll
    for (int hf = 0; hf < 2; ++hf) {
        u32x4 ov[4];
#pragma unroll
        for (int i = 0; i < 4; ++i) ov[i] = ld128c(rws, WS_OP + (tok0 + 8 * w + 4 * hf) * 1024 + l * 16, i * 1024);
#pragma unroll
        for (int i = 0; i < 4; ++i) {
            const int tk = 8 * w + 4 * hf + i;
            lds_w128(lds + 16384 + tk * 1024 + ((l ^ (tk & 15)) * 16), ov[i]);
        }
    }
    bar();
    STAMP(11);
    f32x4 acc[2] = {f32x4{0, 0, 0, 0}, f32x4{0, 0, 0, 0}};
    {
        u32 ob[4];
#pragma unroll
        for (int k4 = 0; k4 < 4; ++k4) ob[k4] = 16384 + (32 * th + r16) * 1024 + ((((4 * k4) ^ (r16 & 12)) | (g ^ (r16 & 3))) * 16);
#pragma unroll
        for (int ks = 0; ks < 16; ++ks)
#pragma unroll
            for (int tt = 0; tt < 2; ++tt)
                acc[tt] = mfma32(wf[ks], lds_r128(lds + ob[ks & 3] + (ks >> 2) * 256 + tt * 16384), acc[tt]);
    }
#pragma unroll
    for (int tt = 0; tt < 2; ++tt) {
        u32x2 o = {pk2(__builtin_fmaf(g1[tt], acc[tt][0], ga[tt][0]), __builtin_fmaf(g1[tt], acc[tt][1], ga[tt][1])),
                   pk2(__builtin_fmaf(g1[tt], acc[tt][2], ga[tt][2]), __builtin_fmaf(g1[tt], acc[tt][3], ga[tt][3]))};
        __builtin_amdgcn_raw_buffer_store_b64(o, rout, (tok + 16 * tt) * 1024 + chl * 2, 0, 0);
    }
    STAMP(12);
}

__global__ __launch_bounds__(512, 4) void s4d_hedgehog_mix(Params p) {
    extern __shared__ __attribute__((aligned(16))) char smem[];
    ldsp lds = (ldsp)smem;
    char* ws = p.ws;
    const int b = blockIdx.x, l = threadIdx.x & 63, w = uni(threadIdx.x >> 6);
    STAMP(0);
    asm volatile("" :: "s"(p.ws), "s"(p.u), "s"(p.wqkv), "s"(p.ldt), "s"(p.gw));
    const u64 e = flag_ld(ws, WS_E);
    if (b >= 256) {
        const u64 tag = role_conv(p, lds, ws, e, b - 256, w, l STAMP_PASS);
        final_stage(p, lds, ws, tag, b - 256, w, l STAMP_PASS);
    } else {
        role_attn(p, lds, ws, e, b, w, l STAMP_PASS);
    }
}

#if defined(__HIP_DEVICE_COMPILE__)
#pragma clang attribute pop
#endif

extern "C" void kernel_launch(void* const* d_in, const int* in_sizes, int n_in, void* d_out, int out_size, void* d_ws, size_t ws_size,
                              hipStream_t stream) {
    Params p;
    p.u = (const u16*)d_in[0]; p.lar = (const u16*)d_in[1]; p.aim = (const u16*)d_in[2]; p.cre = (const u16*)d_in[3];
    p.cim = (const u16*)d_in[4]; p.ldt = (const u16*)d_in[5]; p.dsk = (const u16*)d_in[6]; p.wqkv = (const u16*)d_in[8];
    p.wphi = (const u16*)d_in[9]; p.wo = (const u16*)d_in[10]; p.gw = (const u16*)d_in[11]; p.out = (u16*)d_out; p.ws = (char*)d_ws;
    (void)hipFuncSetAttribute((const void*)s4d_hedgehog_mix, hipFuncAttributeMaxDynamicSharedMemorySize, LDS_BYTES);
    s4d_hedgehog_mix<<<512, 512, LDS_BYTES, stream>>>(p);
}
```

```cpp
#include <hip/hip_runtime.h>
#include <hip/hip_bf16.h>
#include <stdint.h>

#if defined(__HIP_DEVICE_COMPILE__)
#pragma clang attribute push(__attribute__((target("no-packed-fp32-ops"))), apply_to = function)
#endif

typedef unsigned short u16;
typedef unsigned u32;
typedef unsigned long long u64;
typedef float f32x2 __attribute__((ext_vector_type(2)));
typedef float f32x4 __attribute__((ext_vector_type(4)));
typedef u32 u32x2 __attribute__((ext_vector_type(2)));
typedef u32 u32x4 __attribute__((ext_vector_type(4)));
typedef short s16x4 __attribute__((ext_vector_type(4)));
typedef __bf16 bf16x2 __attribute__((ext_vector_type(2)));
typedef __bf16 bf16x8 __attribute__((ext_vector_type(8)));
#define LAS __attribute__((address_space(3)))
#define GAS __attribute__((address_space(1)))
typedef LAS char* ldsp;
typedef __amdgpu_buffer_rsrc_t rsrc_t;
#define DEV __device__ __forceinline__

constexpr int L = 2048, D = 512;
constexpr int LDS_BYTES = 81920;
constexpr u64 TAG_STEP = 0x9E3779B97F4A7C15ull;
constexpr u32 SPIN_CAP = 40000;

constexpr u32 WS_E = 0, WS_TMO = 64;
constexpr u32 WS_FUT = 4096, WS_FKV = WS_FUT + 4096, WS_FY = WS_FKV + 4096, WS_FO = WS_FY + 4096;
constexpr u32 WS_G = 0x28000;
constexpr u32 WS_UT = 0x40000;
constexpr u32 WS_YT = WS_UT + 0x200000;
constexpr u32 WS_OP = WS_YT + 0x200000;
constexpr u32 WS_KVP = WS_OP + 0x200000;
constexpr u32 WS_ZP = WS_KVP + 0x100000;
constexpr u32 WS_DBG = WS_ZP + 0x10000;
constexpr u32 WS_USED = WS_DBG + 0x400000;

constexpr int AUX_SC1 = 16;

struct IdxTab { u16 v[2048]; };
constexpr IdxTab make_idx() {
    IdxTab t{};
    for (int i = 0; i < 2048; ++i) {
        int j = 2047 - i, p = j;
        int sh = j >= 1024 ? 3 : (j >= 512 ? 2 : (j >= 256 ? 1 : 0));
        if (sh) {
            int q = j >> sh, rem = j & ((1 << sh) - 1), half = 1 << (sh - 1);
            if (rem > half || (rem == half && (q & 1))) ++q;
            p = q << sh;
        }
        int row = 0, col = (p >> 3) & 15;
        if (p < 256) row = (p & 7) + 8 * (p >> 7);
        else if (p < 512) row = 16 + ((p - 256) >> 7) * 4 + ((p & 7) >> 1);
        else if (p < 1024) row = 24 + ((p - 512) >> 7) * 2 + ((p & 7) >> 2);
        else row = 32 + ((p - 1024) >> 7);
        t.v[i] = (u16)((row * 16 + col) * 2);
    }
    return t;
}
__device__ const IdxTab g_idx = make_idx();

DEV float u2f(u32 x) { return __builtin_bit_cast(float, x); }
DEV u32 f2u(float x) { return __builtin_bit_cast(u32, x); }
DEV float fexp(float x) { return __builtin_amdgcn_exp2f(x * 1.4426950408889634f); }
template <int CTRL> DEV float dpp_mov(float x) { return u2f(__builtin_amdgcn_update_dpp(0u, f2u(x), CTRL, 0xf, 0xf, true)); }
DEV float row16_sum(float x) {
    x += dpp_mov<0xB1>(x);
    x += dpp_mov<0x4E>(x);
    x += dpp_mov<0x141>(x);
    x += dpp_mov<0x140>(x);
    return x;
}
DEV u32 pk2(float lo, float hi) { f32x2 v = {lo, hi}; return __builtin_bit_cast(u32, __builtin_convertvector(v, bf16x2)); }
DEV float blo(u32 x) { return u2f(x << 16); }
DEV float bhi(u32 x) { return u2f(x & 0xffff0000u); }
DEV float rb(float f) { return blo(pk2(f, 0.f)); }
DEV f32x4 mfma32(u32x4 a, u32x4 b, f32x4 c) {
    return __builtin_amdgcn_mfma_f32_16x16x32_bf16(__builtin_bit_cast(bf16x8, a), __builtin_bit_cast(bf16x8, b), c, 0, 0, 0);
}
DEV f32x4 mfma16(u32x2 a, u32x2 b, f32x4 c) {
    return __builtin_amdgcn_mfma_f32_16x16x16bf16_1k(__builtin_bit_cast(s16x4, a), __builtin_bit_cast(s16x4, b), c, 0, 0, 0);
}
DEV u32x2 trread(ldsp p) { return __builtin_bit_cast(u32x2, __builtin_amdgcn_ds_read_tr16_b64_v4i16((LAS s16x4*)p)); }
DEV u32x4 lds_r128(ldsp p) { return *(LAS u32x4*)p; }
DEV u32x2 lds_r64(ldsp p) { return *(volatile LAS u32x2*)p; }
DEV void lds_w128(ldsp p, u32x4 v) { *(LAS u32x4*)p = v; }
DEV void lds_w64(ldsp p, u32x2 v) { *(LAS u32x2*)p = v; }
DEV void lds_w32(ldsp p, u32 v) { *(LAS u32*)p = v; }
DEV u32x4 cat(u32x2 a, u32x2 b) { u32x4 r = {a.x, a.y, b.x, b.y}; return r; }
DEV rsrc_t mkrsrc(const void* p, u32 bytes) { return __builtin_amdgcn_make_buffer_rsrc((void*)p, 0, bytes, 0x00020000); }
DEV u32x4 mkdesc(const void* p, u32 bytes) {
    u64 a = (u64)p;
    u32x4 d = {(u32)a, (u32)(a >> 32) & 0xffffu, bytes, 0x00020000u};
    return d;
}
DEV void dma16(u32x4 desc, ldsp dst, u32 voff, u32 soff) {
    asm volatile("s_mov_b32 m0, %0\n\ts_nop 0\n\tbuffer_load_dwordx4 %1, %2, %3 offen lds"
                 :: "s"((u32)(uintptr_t)dst), "v"(voff), "s"(desc), "s"(soff) : "memory");
}
DEV u32x4 ld128(rsrc_t r, u32 voff, u32 soff = 0) { return __builtin_amdgcn_raw_buffer_load_b128(r, voff, soff, 0); }
DEV u32x2 ld64(rsrc_t r, u32 voff, u32 soff = 0) { return __builtin_amdgcn_raw_buffer_load_b64(r, voff, soff, 0); }
DEV u32 ld16(rsrc_t r, u32 voff, u32 soff = 0) { return __builtin_amdgcn_raw_buffer_load_b16(r, voff, soff, 0); }
DEV u32x4 ld128c(rsrc_t r, u32 voff, u32 soff = 0) { return __builtin_amdgcn_raw_buffer_load_b128(r, voff, soff, AUX_SC1); }
DEV u32x2 ld64c(rsrc_t r, u32 voff, u32 soff = 0) { return __builtin_amdgcn_raw_buffer_load_b64(r, voff, soff, AUX_SC1); }
DEV u32 ld32c(rsrc_t r, u32 voff, u32 soff = 0) { return __builtin_amdgcn_raw_buffer_load_b32(r, voff, soff, AUX_SC1); }
DEV void st128c(rsrc_t r, u32x4 v, u32 voff, u32 soff = 0) { __builtin_amdgcn_raw_buffer_store_b128(v, r, voff, soff, AUX_SC1); }
DEV void st64c(rsrc_t r, u32x2 v, u32 voff, u32 soff = 0) { __builtin_amdgcn_raw_buffer_store_b64(v, r, voff, soff, AUX_SC1); }
#define WAIT_VM(n) asm volatile("s_waitcnt vmcnt(" #n ")" ::: "memory")
#define SCHED_A() __builtin_amdgcn_sched_barrier(0)
#define WAIT_LGKM0() asm volatile("s_waitcnt lgkmcnt(0)" ::: "memory")
DEV void bar() { asm volatile("s_waitcnt lgkmcnt(0)" ::: "memory"); __builtin_amdgcn_s_barrier(); asm volatile("" ::: "memory"); }
#define PRIO_A0 3
#define PRIO_A1 1
#define PRIO_BH2 3
#define PRIO_F 3
#define PRIO_BC 2
#define PRIO_BH 0
DEV u32 uni(u32 x) { return __builtin_amdgcn_readfirstlane(x); }

DEV float swap16_lo(float a, float b, float& other) {
    auto s = __builtin_amdgcn_permlane16_swap(f2u(a), f2u(b), false, false);
    other = u2f(s[1]);
    return u2f(s[0]);
}
DEV float swap32_lo(float a, float b, float& other) {
    auto s = __builtin_amdgcn_permlane32_swap(f2u(a), f2u(b), false, false);
    other = u2f(s[1]);
    return u2f(s[0]);
}
template <bool MAX> DEV void rows_reduce2(float& a, float& b) {
    float o, t = swap16_lo(a, b, o);
    t = MAX ? __builtin_fmaxf(t, o) : t + o;
    float u = swap32_lo(t, t, o);
    u = MAX ? __builtin_fmaxf(u, o) : u + o;
    a = swap16_lo(u, u, b);
}

typedef GAS u64 gu64;
DEV u64 make_tag(u64 e) {
    u32 lo = (u32)e, hi = (u32)(e >> 32);
    asm volatile("" : "+v"(lo), "+v"(hi));
    return (((u64)uni(hi) << 32) | uni(lo)) + TAG_STEP;
}
DEV u64 flag_ld(char* ws, u32 off) { return __hip_atomic_load((gu64*)(ws + off), __ATOMIC_RELAXED, __HIP_MEMORY_SCOPE_AGENT); }
DEV void flag_st(char* ws, u32 off, u64 v) { __hip_atomic_store((gu64*)(ws + off), v, __ATOMIC_RELAXED, __HIP_MEMORY_SCOPE_AGENT); }
DEV void wait_flags(char* ws, u32 off, u64 tag) {
#pragma unroll 1
    for (u32 spins = 0; spins < SPIN_CAP; ++spins) {
        u64 v = flag_ld(ws, off);
        if (__builtin_amdgcn_ballot_w64(v != tag) == 0) { asm volatile("" ::: "memory"); return; }
        __builtin_amdgcn_s_sleep(1);
    }
    flag_st(ws, WS_TMO, tag);
}
DEV void publish(char* ws, u32 off, u64 tag) {
    WAIT_VM(0);
    bar();
    if (threadIdx.x == 0) flag_st(ws, off, tag);
}

DEV void publish_counted(char* ws, u32 off, u64 tag, ldsp cnt, u32 n, int l) {
    WAIT_VM(0);
    u32 old = 0;
    if (l == 0) old = __hip_atomic_fetch_add((LAS u32*)cnt, 1u, __ATOMIC_RELAXED, __HIP_MEMORY_SCOPE_WORKGROUP);
    if (l == 0 && old == n - 1) flag_st(ws, off, tag);
}

#define STAMP(i) asm volatile("; MARK " #i)
#define FSTAMP(i) do { } while (0)
#define FS1(i) do { } while (0)
#define FS2(i) do { } while (0)
#define FS3(i) do { } while (0)
#define FS4(i) do { } while (0)
#define STAMP_ARG
#define STAMP_PASS

struct Params {
    char* ws; const u16* u; const u16* wqkv; const u16* wphi; const u16* lar; const u16* aim; const u16* cre; const u16* cim;
    const u16* ldt; const u16* gw; const u16* dsk; const u16* wo; u16* out;
};

struct cplx { float r, i; };
DEV cplx cmul(cplx a, cplx b) { cplx c; c.r = a.r * b.r - a.i * b.i; c.i = a.r * b.i + a.i * b.r; return c; }
DEV cplx cexp_p(float zr2, float zrev, float p) {
    float e = __builtin_amdgcn_exp2f(zr2 * p);
    float a = __builtin_amdgcn_fractf(zrev * p);
    cplx c; c.r = e * __builtin_amdgcn_cosf(a); c.i = e * __builtin_amdgcn_sinf(a); return c;
}

template <int NW> DEV void wo_prefetch(const Params& p, ldsp lds, int ct, int w, int l) {
    const u32x4 rwo = mkdesc(p.wo, D * D * 2);
    u32 voff = (l >> 3) * 1024 + ct * 128 + ((((l & 7) >> 1) ^ ((l >> 4) & 3)) * 32) + (l & 1) * 16;
#pragma unroll
    for (int i = 0; i < 64 / NW; ++i) {
        int piece = i * NW + w;
        dma16(rwo, lds + 16384 + piece * 1024, voff, piece * 8192);
    }
}

DEV u32 touch_pages(const Params& p, int b, int l) {
    const int k = l & 15;
    const char* a = p.ws + WS_KVP + (b & 255) * 4096;
    if (k == 1) a = p.ws + WS_OP + (size_t)(b & 255) * 8192;
    if (k == 2) a = p.ws + WS_YT + (size_t)(b & 255) * 8192;
    if (k == 3) a = p.ws + WS_UT + (size_t)(b & 255) * 8192;
    if (k == 4) a = p.ws + WS_ZP + (b & 255) * 256;
    if (k == 5) a = p.ws + WS_G;
    if (k == 6) a = (const char*)p.out + (size_t)(b & 255) * 8192;
    if (k == 7) a = (const char*)p.wo + (size_t)(b & 255) * 2048;
    if (k == 8) a = (const char*)p.dsk;
    if (k == 9) a = (const char*)p.wphi;
    if (k == 10) a = p.ws + WS_OP + 0x1ff000;
    if (k == 11) a = p.ws + WS_YT + 0x1ff000;
    if (k == 12) a = p.ws + WS_UT + 0x1ff000;
    if (k == 13) a = (const char*)p.out + 0x1ff000;
    return *(const volatile GAS u32*)a;
}

DEV u64 role_conv(const Params& p, ldsp lds, char* ws, u64 e, int ba, int w, int l STAMP_ARG) {
    const int t = threadIdx.x, xcd = ba & 7, j = ba >> 3, g = l >> 4, r16 = l & 15;
    const int a = xcd >> 2, c = xcd & 3;
    rsrc_t ru = mkrsrc(p.u, L * D * 2), rws = mkrsrc(ws, WS_USED);
    const int ch0 = 2 * (32 * xcd + j);
    __builtin_amdgcn_s_setprio(PRIO_A0);
    const int tb = 16 * a + (j >> 1), cg = 2 * c + (j & 1);
    {
        u32 voff = (tb * 64 + (l >> 3)) * 1024 + cg * 128 + (l & 7) * 16;
        dma16(mkdesc(p.u, L * D * 2), lds + 70656 + w * 1024, voff, w * 8192);
    }
    const int gtok = 1024 * a + 8 * (32 * c + j) + w;
    u32x4 gx = ld128(ru, gtok * 1024 + l * 16);
    u32x4 gwv = *(const GAS u32x4*)(p.gw + l * 8);
    const int kch = (t >> 7) & 1, kn = t & 127;
    const int pidx = (ch0 + kch) * 128 + kn;
    float lar = blo(p.lar[pidx]), aim = blo(p.aim[pidx]), cre = blo(p.cre[pidx]), cim = blo(p.cim[pidx]), ldt = blo(p.ldt[ch0 + kch]);
    u32x4 idx = *(const GAS u32x4*)(g_idx.v + (t & 255) * 8);
    if (w == 7) { u32 x = touch_pages(p, ba, l); asm volatile("" :: "v"(x)); }
    SCHED_A();
    __builtin_amdgcn_s_setprio(PRIO_A1);
    WAIT_VM(0);
    const u64 tag = make_tag(e);
    bar();
    STAMP(1);
    {
        const int cw = w & 3, s = w >> 2;
        ldsp src = lds + 70656 + (32 * s + 8 * g + (r16 >> 2)) * 128 + 32 * cw + 8 * (l & 3);
        u32x4 v = cat(trread(src), trread(src + 4 * 128));
        st128c(rws, v, WS_UT + (cg * 64 + 16 * cw + r16) * 4096 + (tb * 64 + 32 * s + 8 * g) * 2);
    }
    {
        float s0 = 0.f, s1 = 0.f;
#pragma unroll
        for (int q = 0; q < 4; ++q) {
            u32 pr = pk2(blo(gx[q]) * blo(gwv[q]), bhi(gx[q]) * bhi(gwv[q]));
            s0 += blo(pr); s1 += bhi(pr);
        }
        s0 = row16_sum(s0 + s1);
        s1 = s0;
        rows_reduce2<false>(s0, s1);
        float g0 = rb(__builtin_amdgcn_rcpf(1.f + fexp(-rb(s0))));
        if (l == 0) __builtin_amdgcn_raw_buffer_store_b32(f2u(g0), rws, WS_G + gtok * 4, 0, AUX_SC1);
    }
    STAMP(2);
    {
        const float dt = rb(fexp(ldt));
        const float zr2 = -rb(fexp(lar)) * dt * 1.4426950408889634f, zrev = aim * dt * 0.15915494309189535f;
        ldsp T = lds + kch * 33792 + kn * 4;
        auto put = [&](int row, cplx v) { lds_w32(T + row * 528, pk2(v.r, v.i)); };
        const cplx w128 = cexp_p(zr2, zrev, 128.f);
        cplx G; G.r = 2.f * cre * dt; G.i = 2.f * cim * dt;
        if (w < 4) {
            cplx wp[8];
            wp[1] = cexp_p(zr2, zrev, 1.f); wp[2] = cmul(wp[1], wp[1]); wp[3] = cmul(wp[2], wp[1]); wp[4] = cmul(wp[2], wp[2]);
            wp[5] = cmul(wp[4], wp[1]); wp[6] = cmul(wp[4], wp[2]); wp[7] = cmul(wp[4], wp[3]);
#pragma unroll
            for (int aa = 0; aa < 4; ++aa) {
                if (aa == 1) { FS4(7); publish(ws, WS_FUT + cg * 256 + tb * 8, tag); FS4(8); }
                if (aa < 2) {
                    put(8 * aa, G);
#pragma unroll
                    for (int cc = 1; cc < 8; ++cc) put(8 * aa + cc, cmul(G, wp[cc]));
                } else {
                    put(16 + 4 * (aa - 2), G);
#pragma unroll
                    for (int cc = 1; cc < 4; ++cc) put(16 + 4 * (aa - 2) + cc, cmul(G, wp[2 * cc]));
                }
                if (aa < 3) G = cmul(G, w128);
            }
        } else {
            const cplx w4 = cexp_p(zr2, zrev, 4.f);
            G = cmul(G, cexp_p(zr2, zrev, 512.f));
#pragma unroll
            for (int aa = 4; aa <= 16; ++aa) {
                if (aa == 10) { FS4(7); publish(ws, WS_FUT + cg * 256 + tb * 8, tag); FS4(8); }
                if (aa < 8) {
                    put(24 + 2 * (aa - 4), G);
                    put(24 + 2 * (aa - 4) + 1, cmul(G, w4));
                } else put(32 + aa - 8, G);
                if (aa < 16) G = cmul(G, w128);
            }
            cplx B[9];
            B[1] = cmul(w4, w4); B[2] = cmul(B[1], B[1]); B[3] = cmul(B[2], B[1]); B[4] = cmul(B[2], B[2]);
            B[5] = cmul(B[4], B[1]); B[6] = cmul(B[4], B[2]); B[7] = cmul(B[4], B[3]); B[8] = cmul(B[4], B[4]);
            lds_w32(T + 48 * 528, 0x00003f80u);
#pragma unroll
            for (int b = 1; b < 16; ++b) {
                cplx v = b <= 8 ? B[b] : cmul(B[8], B[b - 8]);
                lds_w32(T + (48 + b) * 528, pk2(v.r, -v.i));
            }
        }
    }
    FS4(9);
    bar();
    FS4(10);
    if (w == 7) wait_flags(ws, WS_FUT + xcd * 256 + (l & 31) * 8, tag);
    if (w < 6) {
        const int ch = w >= 3, tile = w - 3 * ch;
        ldsp T = lds + ch * 33792 + r16 * 528 + g * 16;
        f32x4 k0 = {0, 0, 0, 0};
#pragma unroll
        for (int s = 0; s < 8; ++s) k0 = mfma32(lds_r128(T + tile * 16 * 528 + s * 64), lds_r128(T + 48 * 528 + s * 64), k0);
        ldsp KC = lds + 67584 + ch * 1536 + r16 * 2 + (tile * 16 + 4 * g) * 32;
        u32 a0 = pk2(k0[0], k0[1]), a1 = pk2(k0[2], k0[3]);
        *(LAS u16*)(KC) = (u16)a0; *(LAS u16*)(KC + 32) = (u16)(a0 >> 16);
        *(LAS u16*)(KC + 64) = (u16)a1; *(LAS u16*)(KC + 96) = (u16)(a1 >> 16);
    }
    bar();
    STAMP(3);
    const int hch = w >> 2, t8 = t & 255;
    const u32 uoff = WS_UT + (ch0 + hch) * 4096 + t8 * 16;
    u32x4 d0 = ld128c(rws, uoff), d1 = ld128c(rws, uoff + 4), d2 = ld128c(rws, uoff + 8);
    u32x2 f = ld64c(rws, WS_UT + (ch0 + hch) * 4096);
    SCHED_A();
    {
        ldsp KC = lds + 67584 + hch * 1536;
        u32x4 v;
#pragma unroll
        for (int q = 0; q < 4; ++q) {
            u32 lo = *(LAS u16*)(KC + (idx[q] & 0xffffu)), hi = *(LAS u16*)(KC + (idx[q] >> 16));
            v[q] = lo | (hi << 16);
        }
        lds_w128(lds + hch * 4608 + t8 * 16, v);
        if (t8 < 32) { u32x4 z = {0, 0, 0, 0}; lds_w128(lds + hch * 4608 + 4096 + t8 * 16, z); }
    }
    SCHED_A();
    STAMP(4);
    constexpr int UB = 9216, CS = 4160;
    {
        u32x4 v1, v3;
#pragma unroll
        for (int q = 0; q < 4; ++q) { v1[q] = __builtin_amdgcn_alignbit(d1[q], d0[q], 16); v3[q] = __builtin_amdgcn_alignbit(d2[q], d1[q], 16); }
        ldsp U = lds + UB + hch * 4 * CS + 32 + t8 * 16;
        lds_w128(U, d0); lds_w128(U + CS, v1); lds_w128(U + 2 * CS, d1); lds_w128(U + 3 * CS, v3);
        if (t8 < 4) {
            u32x4 z = {0, 0, 0, 0}, y = z;
            if (t8 == 1) y.w = f.x << 16;
            if (t8 == 2) y.w = f.x;
            if (t8 == 3) { y.z = f.x << 16; y.w = __builtin_amdgcn_alignbit(f.y, f.x, 16); }
            ldsp F = lds + UB + (hch * 4 + t8) * CS;
            lds_w128(F, z); lds_w128(F + 16, y);
        }
    }
    bar();
    STAMP(5);
    {
        const int mq = w & 3;
        const int s = (r16 + 1) & 3;
        ldsp A0 = lds + UB + (hch * 4 + s) * CS + 2 * (1 + r16 - s + 8 * g) + mq * 2 * 64;
        ldsp B0 = lds + hch * 4608 + 4064 - 3584 - 32 * r16 + 16 * g + mq * 2 * 64;
        f32x4 acc[8];
#pragma unroll
        for (int i = 0; i < 8; ++i) acc[i] = f32x4{0, 0, 0, 0};
#pragma unroll
        for (int mm = 0; mm < 2; ++mm) {
            u32x4 Af[8], Bf[8];
#pragma unroll
            for (int i = 0; i < 8; ++i) {
                Bf[i] = lds_r128(B0 + mm * 64 + (7 - i) * 512);
                Af[i] = cat(lds_r64(A0 + mm * 64 + i * 512), lds_r64(A0 + mm * 64 + i * 512 + 8));
            }
#pragma unroll
            for (int i = 0; i < 8; ++i)
#pragma unroll
                for (int cc = i; cc < 8; ++cc) acc[cc] = mfma32(Af[i], Bf[cc - i], acc[cc]);
            __builtin_amdgcn_sched_barrier(0);
        }
        FSTAMP(7);
        bar();
        FSTAMP(8);
        ldsp R = lds + hch * 24576 + l * 16;
#pragma unroll
        for (int cc = 0; cc < 8; ++cc)
            if ((cc >> 1) != mq) lds_w128(R + (cc * 3 + ((mq - (cc >> 1) - 1) & 3)) * 1024, __builtin_bit_cast(u32x4, acc[cc]));
        bar();
        FSTAMP(9);
#pragma unroll
        for (int q = 0; q < 4; ++q)
            if (q == mq) {
                u32 pk[2][2];
#pragma unroll
                for (int h = 0; h < 2; ++h) {
                    float v[4];
#pragma unroll
                    for (int r = 0; r < 4; ++r) v[r] = acc[2 * q + h][r];
#pragma unroll
                    for (int sl = 0; sl < 3; ++sl) {
                        u32x4 o = lds_r128(R + ((2 * q + h) * 3 + sl) * 1024);
#pragma unroll
                        for (int r = 0; r < 4; ++r) v[r] += u2f(o[r]);
                    }
                    pk[h][0] = pk2(v[0], v[1]); pk[h][1] = pk2(v[2], v[3]);
                }
                auto sx = __builtin_amdgcn_permlane16_swap(pk[0][0], pk[1][0], false, false);
                auto sy = __builtin_amdgcn_permlane16_swap(pk[0][1], pk[1][1], false, false);
                u32x4 v = {sx[0], sy[0], sx[1], sy[1]};
                st128c(rws, v, WS_YT + (ch0 + hch) * 4096 + (256 * (2 * q + (g & 1)) + 16 * r16 + 4 * (g & 2)) * 2);
            }
    }
    FSTAMP(10);
    publish(ws, WS_FY + xcd * 256 + j * 8, tag);
    STAMP(6);
    return tag;
}

typedef float f32x16 __attribute__((ext_vector_type(16)));
DEV f32x16 mfma32x32(u32x4 a, u32x4 b, f32x16 c) {
    return __builtin_amdgcn_mfma_f32_32x32x16_bf16(__builtin_bit_cast(bf16x8, a), __builtin_bit_cast(bf16x8, b), c, 0, 0, 0);
}
DEV float vmax3(float a, float b, float c) { float r; asm("v_max3_f32 %0, %1, %2, %3" : "=v"(r) : "v"(a), "v"(b), "v"(c)); return r; }
DEV float vmin3(float a, float b, float c) { float r; asm("v_min3_f32 %0, %1, %2, %3" : "=v"(r) : "v"(a), "v"(b), "v"(c)); return r; }

template <bool POS, bool NEG> DEV void feature_map(u32x4 x0, u32x4 x1, u32x4 wown, int l, u32 (&ph)[16]) {
    const bool odd = (l >> 2) & 1;
    u32x4 a0, a1;
#pragma unroll
    for (int q = 0; q < 4; ++q) { a0[q] = odd ? 0u : wown[q]; a1[q] = odd ? wown[q] : 0u; }
    f32x16 pr;
#pragma unroll
    for (int r = 0; r < 16; ++r) pr[r] = 0.f;
    pr = mfma32x32(a0, x0, pr);
    pr = mfma32x32(a1, x1, pr);
    float p[16];
#pragma unroll
    for (int r = 0; r < 16; r += 2) { u32 v = pk2(pr[r], pr[r + 1]); p[r] = blo(v); p[r + 1] = bhi(v); }
    constexpr float LG = 1.4426950408889634f;
#pragma unroll
    for (int sg = 0; sg < 2; ++sg) {
        if (sg == 0 ? !POS : !NEG) continue;
        float m = sg ? vmin3(p[0], p[1], p[2]) : vmax3(p[0], p[1], p[2]);
#pragma unroll
        for (int r = 3; r < 15; r += 2) m = sg ? vmin3(m, p[r], p[r + 1]) : vmax3(m, p[r], p[r + 1]);
        m = sg ? vmin3(m, p[15], p[15]) : vmax3(m, p[15], p[15]);
        const float sc = sg ? -LG : LG, c = -m * sc;
        float a[16];
#pragma unroll
        for (int r = 0; r < 16; ++r) a[r] = __builtin_fmaf(p[r], sc, c);
#pragma unroll
        for (int r = 0; r < 16; ++r) a[r] = __builtin_amdgcn_exp2f(a[r]);
        float sa[4];
#pragma unroll
        for (int q = 0; q < 4; ++q) sa[q] = (a[4 * q] + a[4 * q + 1]) + (a[4 * q + 2] + a[4 * q + 3]);
        const float ia = __builtin_amdgcn_rcpf((sa[0] + sa[1]) + (sa[2] + sa[3]));
#pragma unroll
        for (int r = 0; r < 8; ++r) ph[8 * sg + r] = pk2(a[2 * r] * ia, a[2 * r + 1] * ia);
    }
}

DEV void role_attn(const Params& p, ldsp lds, char* ws, u64 e, int bb, int w, int l STAMP_ARG) {
    const int xcd = bb & 7, j = bb >> 3, g = l >> 4, r16 = l & 15, h = l >> 5, w4 = w & 3;
    const int rb_ = 8 * (xcd >> 2) + (j >> 2), cb = 4 * (xcd & 3) + (j & 3);
    const int rot = (j + xcd) & 7;
    rsrc_t rws = mkrsrc(ws, WS_USED);
#define SCHED() __builtin_amdgcn_sched_barrier(0)
    constexpr int XCH = 32768, WPH = 49152, PH = 51200, PHS = 10240, VV = 71680, VS = 5120, FR = 0, CNT = 8192;
    const int tok = 32 * w4 + (l & 31);
    const u32 wph = WPH + (l & 1) * 512 + (4 * h + (r16 >> 2)) * 32 + (8 * (g & 1) + 4 * ((l & 3) >> 1)) * 2;
    u64 tag;
    if (w >= 4) {
        __builtin_amdgcn_s_setprio(PRIO_BH);
        const u32x4 ru = mkdesc(p.u, L * D * 2), rw = mkdesc(p.wqkv, D * 3 * D * 2);
        const u32 xv = (rb_ * 128 + w4 * 8 + (l >> 3)) * 1024 + (((l & 7) ^ (((w4 & 1) << 2) | ((l >> 4) & 3))) * 16);
        const u32 wv = (16 * w4 + (l >> 2)) * 3072 + cb * 64 + (l & 3) * 16;
        auto issue_x = [&](int s) {
#pragma unroll
            for (int i = 0; i < 4; ++i) dma16(ru, lds + (s % 3) * 16384 + (4 * i + w4) * 1024, xv, ((s + rot) & 7) * 128 + i * 32768);
        };
        auto issue_w = [&](int s, int sec) {
            dma16(rw, lds + 49152 + ((3 * s + sec) & 7) * 4096 + w4 * 1024, wv, ((s + rot) & 7) * (64 * 3072) + sec * 1024);
        };
        FS3(6);
        issue_w(0, 0); issue_w(0, 1); issue_w(0, 2); issue_x(0);
        FS3(7);
        __builtin_amdgcn_s_barrier();
        FS3(8);
        issue_w(1, 0); issue_w(1, 1); issue_w(1, 2); issue_x(1);
        FS3(9);
#pragma unroll
        for (int s = 0; s < 8; ++s) {
            if (s == 0) WAIT_VM(7); else if (s < 7) WAIT_VM(6); else WAIT_VM(0);
            if (s == 0) FS3(10);
            __builtin_amdgcn_s_barrier();
            if (s >= 1 && s + 1 < 8) issue_w(s + 1, 2);
            if (s + 2 < 8) { issue_x(s + 2); issue_w(s + 2, 0); issue_w(s + 2, 1); }
            if (s == 6 && w == 4) dma16(mkdesc(p.wphi, 32 * 256 * 2), lds + WPH, l * 16, cb * 1024);
            if (s == 7 && w == 4 && l < 2) lds_w32(lds + CNT + l * 4, 0u);
        }
        tag = make_tag(e);
        bar();
        ldsp X = lds + XCH + w4 * 4096 + l * 16;
        u32x4 wown = cat(trread(lds + wph), trread(lds + wph + 8 * 32));
        u32x4 q0 = lds_r128(X), q1 = lds_r128(X + 1024);
        bar();
        bar();
        u32 ph[16];
        feature_map<true, true>(q0, q1, wown, l, ph);
        u32x4 bq[2][2];
#pragma unroll
        for (int sg = 0; sg < 2; ++sg)
#pragma unroll
            for (int q = 0; q < 4; ++q) {
                auto sw = __builtin_amdgcn_permlane32_swap(ph[8 * sg + q], ph[8 * sg + q + 4], false, false);
                bq[0][sg][q] = sw[0]; bq[1][sg][q] = sw[1];
            }
        STAMP(3);
        __builtin_amdgcn_s_setprio(PRIO_BH2);
        bar();
#pragma unroll
        for (int hh = 0; hh < 2; ++hh) {
            f32x16 nd;
#pragma unroll
            for (int r = 0; r < 16; ++r) nd[r] = 0.f;
            nd = mfma32x32(lds_r128(lds + FR + (2 * hh) * 1024 + l * 16), bq[hh][0], nd);
            nd = mfma32x32(lds_r128(lds + FR + (2 * hh + 1) * 1024 + l * 16), bq[hh][1], nd);
            const float inv = __builtin_amdgcn_rcpf(rb(rb(nd[8]) + 1e-6f));
            u32x4 o;
#pragma unroll
            for (int q = 0; q < 4; ++q) { u32 n = pk2(nd[2 * q], nd[2 * q + 1]); o[q] = pk2(blo(n) * inv, bhi(n) * inv); }
            st128c(rws, o, WS_OP + (rb_ * 128 + tok) * 1024 + cb * 64 + hh * 32 + h * 16);
        }
        FS2(10);
        publish_counted(ws, WS_FO + rb_ * 256 + cb * 8, tag, lds + CNT + 4, 4, l);
        STAMP(5);
    } else {
        __builtin_amdgcn_s_setprio(PRIO_BC);
        f32x16 acc[3];
#pragma unroll
        for (int sec = 0; sec < 3; ++sec)
#pragma unroll
            for (int r = 0; r < 16; ++r) acc[sec][r] = 0.f;
        const u32 xa = tok * 128 + ((h ^ ((tok >> 1) & 7)) * 16);
        const u32 wa = 49152 + (8 * h + (r16 >> 2)) * 64 + (g & 1) * 32 + 8 * (l & 3);
        __builtin_amdgcn_s_barrier();
#pragma unroll
        for (int s = 0; s < 8; ++s) {
            __builtin_amdgcn_s_barrier();
            asm volatile("" ::: "memory");
            if (s == 0) FS1(6);
            if (s == 1) FS1(7);
            if (s == 2) FS1(8);
            if (s == 4) FS1(9);
            if (s == 6) FS1(10);
            u32x4 xf[4], wf[12];
            auto read_x = [&](int ks) { xf[ks] = lds_r128(lds + (s % 3) * 16384 + (xa ^ (32 * ks))); };
            auto read_w = [&](int i) {
                ldsp a = lds + wa + ((3 * s + i % 3) & 7) * 4096 + (i / 3) * 1024;
                wf[i] = cat(trread(a), trread(a + 256));
            };
            read_x(0); read_w(0); read_w(1); read_w(2);
            SCHED();
#pragma unroll
            for (int i = 0; i < 12; ++i) {
                if (i % 3 == 0 && i / 3 + 1 < 4) read_x(i / 3 + 1);
                if (i + 3 < 12) read_w(i + 3);
                SCHED();
                acc[i % 3] = mfma32x32(wf[i], xf[i / 3], acc[i % 3]);
                SCHED();
            }
        }
        u32x4 fr[3][2];
#pragma unroll
        for (int sec = 0; sec < 3; ++sec)
#pragma unroll
            for (int hh = 0; hh < 2; ++hh)
#pragma unroll
                for (int q = 0; q < 4; ++q) fr[sec][hh][q] = pk2(acc[sec][8 * hh + 2 * q], acc[sec][8 * hh + 2 * q + 1]);
        lds_w128(lds + XCH + w * 4096 + l * 16, fr[0][0]);
        lds_w128(lds + XCH + w * 4096 + 1024 + l * 16, fr[0][1]);
        tag = make_tag(e);
        bar();
        STAMP(1);
#pragma unroll
        for (int hh = 0; hh < 2; ++hh) {
            ldsp q = lds + VV + hh * VS + tok * 40 + 8 * h;
            lds_w64(q, u32x2{fr[2][hh].x, fr[2][hh].y});
            lds_w64(q + 16, u32x2{fr[2][hh].z, fr[2][hh].w});
        }
        {
            u32x4 wown = cat(trread(lds + wph), trread(lds + wph + 8 * 32));
            u32 ph[16];
            feature_map<true, true>(fr[1][0], fr[1][1], wown, l, ph);
            ldsp q = lds + PH + h * PHS + tok * 80;
#pragma unroll
            for (int i = 0; i < 4; ++i) lds_w128(q + i * 16, u32x4{ph[4 * i], ph[4 * i + 1], ph[4 * i + 2], ph[4 * i + 3]});
        }
        FS2(6);
        bar();
        FS2(7);
        const int hh = w >> 1, sg = w & 1;
        {
            f32x4 kv = {0, 0, 0, 0}, z = {0, 0, 0, 0};
            const u32x4 ones = {0x3f803f80u, 0x3f803f80u, 0x3f803f80u, 0x3f803f80u};
            ldsp pa = lds + PH + hh * PHS + (8 * g + (r16 >> 2)) * 80 + sg * 32 + 8 * (l & 3);
            ldsp pb = lds + VV + hh * VS + (8 * g + (r16 >> 2)) * 40 + 8 * (l & 3);
#pragma unroll
            for (int tg = 0; tg < 4; ++tg) {
                u32x4 A = cat(trread(pa + tg * 32 * 80), trread(pa + tg * 32 * 80 + 4 * 80));
                u32x4 B = cat(trread(pb + tg * 32 * 40), trread(pb + tg * 32 * 40 + 4 * 40));
                kv = mfma32(A, B, kv);
                z = mfma32(A, ones, z);
            }
            st128c(rws, __builtin_bit_cast(u32x4, kv), WS_KVP + ((cb * 16 + rb_) * 4 + w) * 1024 + l * 16);
            if (r16 == 0) st128c(rws, __builtin_bit_cast(u32x4, z), WS_ZP + ((cb * 16 + rb_) * 4 + w) * 64 + g * 16);
        }
        FS2(8);
        bar();
        publish_counted(ws, WS_FKV + cb * 256 + rb_ * 8, tag, lds + CNT, 4, l);
        STAMP(2);
        wait_flags(ws, WS_FKV + cb * 256 + r16 * 8, tag);
        STAMP(4);
        u32x4 part[16];
#pragma unroll
        for (int r = 0; r < 16; ++r) part[r] = ld128c(rws, WS_KVP + (cb * 16 * 4 + w) * 1024 + l * 16, r * 4096);
        u32x4 zp = ld128c(rws, WS_ZP + ((cb * 16 + (l >> 2)) * 4 + w) * 64 + (l & 3) * 16);
        float s[4] = {u2f(part[0].x), u2f(part[0].y), u2f(part[0].z), u2f(part[0].w)};
#pragma unroll
        for (int r = 1; r < 16; ++r)
#pragma unroll
            for (int q = 0; q < 4; ++q) s[q] += u2f(part[r][q]);
        float zs[4] = {u2f(zp.x), u2f(zp.y), u2f(zp.z), u2f(zp.w)};
#pragma unroll
        for (int q = 0; q < 4; ++q) {
            float x = zs[q];
            x += dpp_mov<0x124>(x);
            x += dpp_mov<0x128>(x);
            zs[q] = x;
        }
        rows_reduce2<false>(zs[0], zs[1]);
        rows_reduce2<false>(zs[2], zs[3]);
        FS2(9);
        ldsp F = lds + FR + w * 1024;
        lds_w64(F + (r16 + 32 * (g >> 1)) * 16 + 8 * (g & 1), u32x2{pk2(s[0], s[1]), pk2(s[2], s[3])});
        lds_w64(F + (16 + (l >> 2) + 32 * ((l & 3) >> 1)) * 16 + 8 * (l & 1), u32x2{pk2(zs[0], zs[1]), pk2(zs[2], zs[3])});
        bar();
    }
}

DEV void final_stage(const Params& p, ldsp lds, char* ws, u64 tag, int ba, int w, int l STAMP_ARG) {
    const int t = threadIdx.x, g = l >> 4, r16 = l & 15, cw = w & 3, th = w >> 2;
    const int tt64 = 4 * (ba & 7) + ((ba >> 3) & 3), ct = ba >> 5;
    rsrc_t ru = mkrsrc(p.u, L * D * 2), rws = mkrsrc(ws, WS_USED), rout = mkrsrc(p.out, L * D * 2);
    const int tok0 = tt64 * 64, chl = ct * 64 + 16 * cw + 4 * g, tok = tok0 + 32 * th + r16;
    constexpr int YL = 4096;
    bar();
    wo_prefetch<8>(p, lds, ct, w, l);
    u32x2 uu[2];
#pragma unroll
    for (int tt = 0; tt < 2; ++tt) uu[tt] = ld64(ru, (tok + 16 * tt) * 1024 + chl * 2);
    const u32x2 dsk = *(const GAS u32x2*)(p.dsk + chl);
    if (ba == 0 && w >= 4) {
        const int q = t - 256;
        wait_flags(ws, WS_FUT + (q >> 5) * 256 + (q & 31) * 8, tag);
        wait_flags(ws, WS_FKV + (q >> 4) * 256 + (q & 15) * 8, tag);
    }
    if (w == 0) {
        const int tg8 = tt64 * 8 + (l & 7), ga = tg8 >> 7, gc = (tg8 >> 5) & 3, gj = tg8 & 31;
        const int gtb = 16 * ga + (gj >> 1), gcg = 2 * gc + (gj & 1);
        wait_flags(ws, l < 32 ? WS_FY + ct * 256 + l * 8 : WS_FUT + gcg * 256 + gtb * 8, tag);
    }
    WAIT_VM(0);
    bar();
    if (ba == 0 && t == 0) flag_st(ws, WS_E, tag);
    STAMP(8);
    float gate[2];
    {
        u32x4 y = ld128c(rws, WS_YT + (ct * 64 + (t >> 3)) * 4096 + tok0 * 2 + (t & 7) * 16);
#pragma unroll
        for (int tt = 0; tt < 2; ++tt) gate[tt] = u2f(ld32c(rws, WS_G + (tok + 16 * tt) * 4));
        lds_w128(lds + YL + (t >> 3) * 128 + (t & 7) * 16, y);
    }
    u32x4 wf[16];
    {
        const int row = 16 * (g >> 1) + 4 * (g & 1) + (r16 >> 2);
        ldsp q = lds + 16384 + row * 128 + ((cw ^ ((row >> 1) & 3)) * 32) + 8 * (l & 3);
#pragma unroll
        for (int ks = 0; ks < 16; ++ks) wf[ks] = cat(trread(q + ks * 4096), trread(q + ks * 4096 + 1024));
    }
    bar();
    float ga[2][4], g1[2];
#pragma unroll
    for (int tt = 0; tt < 2; ++tt) {
        u32x2 y = trread(lds + YL + (16 * cw + 4 * g + (r16 >> 2)) * 128 + (32 * th + 16 * tt) * 2 + 8 * (l & 3));
        const float gg = gate[tt];
        g1[tt] = rb(1.f - gg);
        u32 t0 = pk2(blo(uu[tt].x) * blo(dsk.x), bhi(uu[tt].x) * bhi(dsk.x)), t1 = pk2(blo(uu[tt].y) * blo(dsk.y), bhi(uu[tt].y) * bhi(dsk.y));
        u32 a0 = pk2(blo(y.x) + blo(t0), bhi(y.x) + bhi(t0)), a1 = pk2(blo(y.y) + blo(t1), bhi(y.y) + bhi(t1));
        u32 ga0 = pk2(gg * blo(a0), gg * bhi(a0)), ga1 = pk2(gg * blo(a1), gg * bhi(a1));
        ga[tt][0] = blo(ga0); ga[tt][1] = bhi(ga0); ga[tt][2] = blo(ga1); ga[tt][3] = bhi(ga1);
        asm volatile("" : "+v"(ga[tt][0]), "+v"(ga[tt][1]), "+v"(ga[tt][2]), "+v"(ga[tt][3]), "+v"(g1[tt]));
    }
    STAMP(9);
    __builtin_amdgcn_s_setprio(PRIO_F);
    if (w == 0) wait_flags(ws, WS_FO + (tok0 >> 7) * 256 + r16 * 8, tag);
    bar();
    STAMP(10);
#pragma unroll
    for (int hf = 0; hf < 2; ++hf) {
        u32x4 ov[4];
#pragma unroll
        for (int i = 0; i < 4; ++i) ov[i] = ld128c(rws, WS_OP + (tok0 + 8 * w + 4 * hf) * 1024 + l * 16, i * 1024);
#pragma unroll
        for (int i = 0; i < 4; ++i) {
            const int tk = 8 * w + 4 * hf + i;
            lds_w128(lds + 16384 + tk * 1024 + ((l ^ (tk & 15)) * 16), ov[i]);
        }
    }
    bar();
    STAMP(11);
    f32x4 acc[2] = {f32x4{0, 0, 0, 0}, f32x4{0, 0, 0, 0}};
    {
        u32 ob[4];
#pragma unroll
        for (int k4 = 0; k4 < 4; ++k4) ob[k4] = 16384 + (32 * th + r16) * 1024 + ((((4 * k4) ^ (r16 & 12)) | (g ^ (r16 & 3))) * 16);
#pragma unroll
        for (int ks = 0; ks < 16; ++ks)
#pragma unroll
            for (int tt = 0; tt < 2; ++tt)
                acc[tt] = mfma32(wf[ks], lds_r128(lds + ob[ks & 3] + (ks >> 2) * 256 + tt * 16384), acc[tt]);
    }
#pragma unroll
    for (int tt = 0; tt < 2; ++tt) {
        u32x2 o = {pk2(__builtin_fmaf(g1[tt], acc[tt][0], ga[tt][0]), __builtin_fmaf(g1[tt], acc[tt][1], ga[tt][1])),
                   pk2(__builtin_fmaf(g1[tt], acc[tt][2], ga[tt][2]), __builtin_fmaf(g1[tt], acc[tt][3], ga[tt][3]))};
        __builtin_amdgcn_raw_buffer_store_b64(o, rout, (tok + 16 * tt) * 1024 + chl * 2, 0, 0);
    }
    STAMP(12);
}

__global__ __launch_bounds__(512, 4) void s4d_hedgehog_mix(Params p) {
    extern __shared__ __attribute__((aligned(16))) char smem[];
    ldsp lds = (ldsp)smem;
    char* ws = p.ws;
    const int b = blockIdx.x, l = threadIdx.x & 63, w = uni(threadIdx.x >> 6);
    STAMP(0);
    asm volatile("" :: "s"(p.ws), "s"(p.u), "s"(p.wqkv), "s"(p.ldt), "s"(p.gw));
    const u64 e = flag_ld(ws, WS_E);
    if (b >= 256) {
        const u64 tag = role_conv(p, lds, ws, e, b - 256, w, l STAMP_PASS);
        final_stage(p, lds, ws, tag, b - 256, w, l STAMP_PASS);
    } else {
        role_attn(p, lds, ws, e, b, w, l STAMP_PASS);
    }
}

#if defined(__HIP_DEVICE_COMPILE__)
#pragma clang attribute pop
#endif

extern "C" void kernel_launch(void* const* d_in, const int* in_sizes, int n_in, void* d_out, int out_size, void* d_ws, size_t ws_size,
                              hipStream_t stream) {
    Params p;
    p.u = (const u16*)d_in[0]; p.lar = (const u16*)d_in[1]; p.aim = (const u16*)d_in[2]; p.cre = (const u16*)d_in[3];
    p.cim = (const u16*)d_in[4]; p.ldt = (const u16*)d_in[5]; p.dsk = (const u16*)d_in[6]; p.wqkv = (const u16*)d_in[8];
    p.wphi = (const u16*)d_in[9]; p.wo = (const u16*)d_in[10]; p.gw = (const u16*)d_in[11]; p.out = (u16*)d_out; p.ws = (char*)d_ws;
    (void)hipFuncSetAttribute((const void*)s4d_hedgehog_mix, hipFuncAttributeMaxDynamicSharedMemorySize, LDS_BYTES);
    s4d_hedgehog_mix<<<512, 512, LDS_BYTES, stream>>>(p);
}
```

```cpp
#define POST_K 512
#define POST_T 64
#define POST_L 81920
#include <hip/hip_runtime.h>
#include <hip/hip_bf16.h>
#include <stdint.h>

#if defined(__HIP_DEVICE_COMPILE__)
#pragma clang attribute push(__attribute__((target("no-packed-fp32-ops"))), apply_to = function)
#endif

typedef unsigned short u16;
typedef unsigned u32;
typedef unsigned long long u64;
typedef float f32x2 __attribute__((ext_vector_type(2)));
typedef float f32x4 __attribute__((ext_vector_type(4)));
typedef u32 u32x2 __attribute__((ext_vector_type(2)));
typedef u32 u32x4 __attribute__((ext_vector_type(4)));
typedef short s16x4 __attribute__((ext_vector_type(4)));
typedef __bf16 bf16x2 __attribute__((ext_vector_type(2)));
typedef __bf16 bf16x8 __attribute__((ext_vector_type(8)));
#define LAS __attribute__((address_space(3)))
#define GAS __attribute__((address_space(1)))
typedef LAS char* ldsp;
typedef __amdgpu_buffer_rsrc_t rsrc_t;
#define DEV __device__ __forceinline__

constexpr int L = 2048, D = 512;
constexpr int LDS_BYTES = 81920;
constexpr u64 TAG_STEP = 0x9E3779B97F4A7C15ull;
constexpr u32 SPIN_CAP = 40000;

constexpr u32 WS_E = 0, WS_TMO = 64;
constexpr u32 WS_FUT = 4096, WS_FKV = WS_FUT + 4096, WS_FY = WS_FKV + 4096, WS_FO = WS_FY + 4096;
constexpr u32 WS_G = 0x28000;
constexpr u32 WS_UT = 0x40000;
constexpr u32 WS_YT = WS_UT + 0x200000;
constexpr u32 WS_OP = WS_YT + 0x200000;
constexpr u32 WS_KVP = WS_OP + 0x200000;
constexpr u32 WS_ZP = WS_KVP + 0x100000;
constexpr u32 WS_DBG = WS_ZP + 0x10000;
constexpr u32 WS_USED = WS_DBG + 0x400000;

constexpr int AUX_SC1 = 16;

struct IdxTab { u16 v[2048]; };
constexpr IdxTab make_idx() {
    IdxTab t{};
    for (int i = 0; i < 2048; ++i) {
        int j = 2047 - i, p = j;
        int sh = j >= 1024 ? 3 : (j >= 512 ? 2 : (j >= 256 ? 1 : 0));
        if (sh) {
            int q = j >> sh, rem = j & ((1 << sh) - 1), half = 1 << (sh - 1);
            if (rem > half || (rem == half && (q & 1))) ++q;
            p = q << sh;
        }
        int row = 0, col = (p >> 3) & 15;
        if (p < 256) row = (p & 7) + 8 * (p >> 7);
        else if (p < 512) row = 16 + ((p - 256) >> 7) * 4 + ((p & 7) >> 1);
        else if (p < 1024) row = 24 + ((p - 512) >> 7) * 2 + ((p & 7) >> 2);
        else row = 32 + ((p - 1024) >> 7);
        t.v[i] = (u16)((row * 16 + col) * 2);
    }
    return t;
}
__device__ const IdxTab g_idx = make_idx();

DEV float u2f(u32 x) { return __builtin_bit_cast(float, x); }
DEV u32 f2u(float x) { return __builtin_bit_cast(u32, x); }
DEV float fexp(float x) { return __builtin_amdgcn_exp2f(x * 1.4426950408889634f); }
template <int CTRL> DEV float dpp_mov(float x) { return u2f(__builtin_amdgcn_update_dpp(0u, f2u(x), CTRL, 0xf, 0xf, true)); }
DEV float row16_sum(float x) {
    x += dpp_mov<0xB1>(x);
    x += dpp_mov<0x4E>(x);
    x += dpp_mov<0x141>(x);
    x += dpp_mov<0x140>(x);
    return x;
}
DEV u32 pk2(float lo, float hi) { f32x2 v = {lo, hi}; return __builtin_bit_cast(u32, __builtin_convertvector(v, bf16x2)); }
DEV float blo(u32 x) { return u2f(x << 16); }
DEV float bhi(u32 x) { return u2f(x & 0xffff0000u); }
DEV float rb(float f) { return blo(pk2(f, 0.f)); }
DEV f32x4 mfma32(u32x4 a, u32x4 b, f32x4 c) {
    return __builtin_amdgcn_mfma_f32_16x16x32_bf16(__builtin_bit_cast(bf16x8, a), __builtin_bit_cast(bf16x8, b), c, 0, 0, 0);
}
DEV f32x4 mfma16(u32x2 a, u32x2 b, f32x4 c) {
    return __builtin_amdgcn_mfma_f32_16x16x16bf16_1k(__builtin_bit_cast(s16x4, a), __builtin_bit_cast(s16x4, b), c, 0, 0, 0);
}
DEV u32x2 trread(ldsp p) { return __builtin_bit_cast(u32x2, __builtin_amdgcn_ds_read_tr16_b64_v4i16((LAS s16x4*)p)); }
DEV u32x4 lds_r128(ldsp p) { return *(LAS u32x4*)p; }
DEV u32x2 lds_r64(ldsp p) { return *(volatile LAS u32x2*)p; }
DEV void lds_w128(ldsp p, u32x4 v) { *(LAS u32x4*)p = v; }
DEV void lds_w64(ldsp p, u32x2 v) { *(LAS u32x2*)p = v; }
DEV void lds_w32(ldsp p, u32 v) { *(LAS u32*)p = v; }
DEV u32x4 cat(u32x2 a, u32x2 b) { u32x4 r = {a.x, a.y, b.x, b.y}; return r; }
DEV rsrc_t mkrsrc(const void* p, u32 bytes) { return __builtin_amdgcn_make_buffer_rsrc((void*)p, 0, bytes, 0x00020000); }
DEV u32x4 mkdesc(const void* p, u32 bytes) {
    u64 a = (u64)p;
    u32x4 d = {(u32)a, (u32)(a >> 32) & 0xffffu, bytes, 0x00020000u};
    return d;
}
DEV void dma16(u32x4 desc, ldsp dst, u32 voff, u32 soff) {
    asm volatile("s_mov_b32 m0, %0\n\ts_nop 0\n\tbuffer_load_dwordx4 %1, %2, %3 offen lds"
                 :: "s"((u32)(uintptr_t)dst), "v"(voff), "s"(desc), "s"(soff) : "memory");
}
DEV u32x4 ld128(rsrc_t r, u32 voff, u32 soff = 0) { return __builtin_amdgcn_raw_buffer_load_b128(r, voff, soff, 0); }
DEV u32x2 ld64(rsrc_t r, u32 voff, u32 soff = 0) { return __builtin_amdgcn_raw_buffer_load_b64(r, voff, soff, 0); }
DEV u32 ld16(rsrc_t r, u32 voff, u32 soff = 0) { return __builtin_amdgcn_raw_buffer_load_b16(r, voff, soff, 0); }
DEV u32x4 ld128c(rsrc_t r, u32 voff, u32 soff = 0) { return __builtin_amdgcn_raw_buffer_load_b128(r, voff, soff, AUX_SC1); }
DEV u32x2 ld64c(rsrc_t r, u32 voff, u32 soff = 0) { return __builtin_amdgcn_raw_buffer_load_b64(r, voff, soff, AUX_SC1); }
DEV u32 ld32c(rsrc_t r, u32 voff, u32 soff = 0) { return __builtin_amdgcn_raw_buffer_load_b32(r, voff, soff, AUX_SC1); }
DEV void st128c(rsrc_t r, u32x4 v, u32 voff, u32 soff = 0) { __builtin_amdgcn_raw_buffer_store_b128(v, r, voff, soff, AUX_SC1); }
DEV void st64c(rsrc_t r, u32x2 v, u32 voff, u32 soff = 0) { __builtin_amdgcn_raw_buffer_store_b64(v, r, voff, soff, AUX_SC1); }
#define WAIT_VM(n) asm volatile("s_waitcnt vmcnt(" #n ")" ::: "memory")
#define SCHED_A() __builtin_amdgcn_sched_barrier(0)
#define WAIT_LGKM0() asm volatile("s_waitcnt lgkmcnt(0)" ::: "memory")
DEV void bar() { asm volatile("s_waitcnt lgkmcnt(0)" ::: "memory"); __builtin_amdgcn_s_barrier(); asm volatile("" ::: "memory"); }
#define PRIO_A0 3
#define PRIO_A1 1
#define PRIO_BH2 3
#define PRIO_F 3
#define PRIO_BC 2
#define PRIO_BH 0
DEV u32 uni(u32 x) { return __builtin_amdgcn_readfirstlane(x); }

DEV float swap16_lo(float a, float b, float& other) {
    auto s = __builtin_amdgcn_permlane16_swap(f2u(a), f2u(b), false, false);
    other = u2f(s[1]);
    return u2f(s[0]);
}
DEV float swap32_lo(float a, float b, float& other) {
    auto s = __builtin_amdgcn_permlane32_swap(f2u(a), f2u(b), false, false);
    other = u2f(s[1]);
    return u2f(s[0]);
}
template <bool MAX> DEV void rows_reduce2(float& a, float& b) {
    float o, t = swap16_lo(a, b, o);
    t = MAX ? __builtin_fmaxf(t, o) : t + o;
    float u = swap32_lo(t, t, o);
    u = MAX ? __builtin_fmaxf(u, o) : u + o;
    a = swap16_lo(u, u, b);
}

typedef GAS u64 gu64;
DEV u64 make_tag(u64 e) {
    u32 lo = (u32)e, hi = (u32)(e >> 32);
    asm volatile("" : "+v"(lo), "+v"(hi));
    return (((u64)uni(hi) << 32) | uni(lo)) + TAG_STEP;
}
DEV u64 flag_ld(char* ws, u32 off) { return __hip_atomic_load((gu64*)(ws + off), __ATOMIC_RELAXED, __HIP_MEMORY_SCOPE_AGENT); }
DEV void flag_st(char* ws, u32 off, u64 v) { __hip_atomic_store((gu64*)(ws + off), v, __ATOMIC_RELAXED, __HIP_MEMORY_SCOPE_AGENT); }
DEV void wait_flags(char* ws, u32 off, u64 tag) {
#pragma unroll 1
    for (u32 spins = 0; spins < SPIN_CAP; ++spins) {
        u64 v = flag_ld(ws, off);
        if (__builtin_amdgcn_ballot_w64(v != tag) == 0) { asm volatile("" ::: "memory"); return; }
        __builtin_amdgcn_s_sleep(1);
    }
    flag_st(ws, WS_TMO, tag);
}
DEV void publish(char* ws, u32 off, u64 tag) {
    WAIT_VM(0);
    bar();
    if (threadIdx.x == 0) flag_st(ws, off, tag);
}

DEV void publish_counted(char* ws, u32 off, u64 tag, ldsp cnt, u32 n, int l) {
    WAIT_VM(0);
    u32 old = 0;
    if (l == 0) old = __hip_atomic_fetch_add((LAS u32*)cnt, 1u, __ATOMIC_RELAXED, __HIP_MEMORY_SCOPE_WORKGROUP);
    if (l == 0 && old == n - 1) flag_st(ws, off, tag);
}

#define STAMP(i) asm volatile("; MARK " #i)
#define FSTAMP(i) do { } while (0)
#define FS1(i) do { } while (0)
#define FS2(i) do { } while (0)
#define FS3(i) do { } while (0)
#define FS4(i) do { } while (0)
#define STAMP_ARG
#define STAMP_PASS

struct Params {
    char* ws; const u16* u; const u16* wqkv; const u16* wphi; const u16* lar; const u16* aim; const u16* cre; const u16* cim;
    const u16* ldt; const u16* gw; const u16* dsk; const u16* wo; u16* out;
};

struct cplx { float r, i; };
DEV cplx cmul(cplx a, cplx b) { cplx c; c.r = a.r * b.r - a.i * b.i; c.i = a.r * b.i + a.i * b.r; return c; }
DEV cplx cexp_p(float zr2, float zrev, float p) {
    float e = __builtin_amdgcn_exp2f(zr2 * p);
    float a = __builtin_amdgcn_fractf(zrev * p);
    cplx c; c.r = e * __builtin_amdgcn_cosf(a); c.i = e * __builtin_amdgcn_sinf(a); return c;
}

template <int NW> DEV void wo_prefetch(const Params& p, ldsp lds, int ct, int w, int l) {
    const u32x4 rwo = mkdesc(p.wo, D * D * 2);
    u32 voff = (l >> 3) * 1024 + ct * 128 + ((((l & 7) >> 1) ^ ((l >> 4) & 3)) * 32) + (l & 1) * 16;
#pragma unroll
    for (int i = 0; i < 64 / NW; ++i) {
        int piece = i * NW + w;
        dma16(rwo, lds + 16384 + piece * 1024, voff, piece * 8192);
    }
}

DEV u32 touch_pages(const Params& p, int b, int l) {
    const int k = l & 15;
    const char* a = p.ws + WS_KVP + (b & 255) * 4096;
    if (k == 1) a = p.ws + WS_OP + (size_t)(b & 255) * 8192;
    if (k == 2) a = p.ws + WS_YT + (size_t)(b & 255) * 8192;
    if (k == 3) a = p.ws + WS_UT + (size_t)(b & 255) * 8192;
    if (k == 4) a = p.ws + WS_ZP + (b & 255) * 256;
    if (k == 5) a = p.ws + WS_G;
    if (k == 6) a = (const char*)p.out + (size_t)(b & 255) * 8192;
    if (k == 7) a = (const char*)p.wo + (size_t)(b & 255) * 2048;
    if (k == 8) a = (const char*)p.dsk;
    if (k == 9) a = (const char*)p.wphi;
    if (k == 10) a = p.ws + WS_OP + 0x1ff000;
    if (k == 11) a = p.ws + WS_YT + 0x1ff000;
    if (k == 12) a = p.ws + WS_UT + 0x1ff000;
    if (k == 13) a = (const char*)p.out + 0x1ff000;
    return *(const volatile GAS u32*)a;
}

DEV u64 role_conv(const Params& p, ldsp lds, char* ws, u64 e, int ba, int w, int l STAMP_ARG) {
    const int t = threadIdx.x, xcd = ba & 7, j = ba >> 3, g = l >> 4, r16 = l & 15;
    const int a = xcd >> 2, c = xcd & 3;
    rsrc_t ru = mkrsrc(p.u, L * D * 2), rws = mkrsrc(ws, WS_USED);
    const int ch0 = 2 * (32 * xcd + j);
    __builtin_amdgcn_s_setprio(PRIO_A0);
    const int tb = 16 * a + (j >> 1), cg = 2 * c + (j & 1);
    {
        u32 voff = (tb * 64 + (l >> 3)) * 1024 + cg * 128 + (l & 7) * 16;
        dma16(mkdesc(p.u, L * D * 2), lds + 70656 + w * 1024, voff, w * 8192);
    }
    const int gtok = 1024 * a + 8 * (32 * c + j) + w;
    u32x4 gx = ld128(ru, gtok * 1024 + l * 16);
    u32x4 gwv = *(const GAS u32x4*)(p.gw + l * 8);
    const int kch = (t >> 7) & 1, kn = t & 127;
    const int pidx = (ch0 + kch) * 128 + kn;
    float lar = blo(p.lar[pidx]), aim = blo(p.aim[pidx]), cre = blo(p.cre[pidx]), cim = blo(p.cim[pidx]), ldt = blo(p.ldt[ch0 + kch]);
    u32x4 idx = *(const GAS u32x4*)(g_idx.v + (t & 255) * 8);
    if (w == 7) { u32 x = touch_pages(p, ba, l); asm volatile("" :: "v"(x)); }
    SCHED_A();
    __builtin_amdgcn_s_setprio(PRIO_A1);
    WAIT_VM(0);
    const u64 tag = make_tag(e);
    bar();
    STAMP(1);
    {
        const int cw = w & 3, s = w >> 2;
        ldsp src = lds + 70656 + (32 * s + 8 * g + (r16 >> 2)) * 128 + 32 * cw + 8 * (l & 3);
        u32x4 v = cat(trread(src), trread(src + 4 * 128));
        st128c(rws, v, WS_UT + (cg * 64 + 16 * cw + r16) * 4096 + (tb * 64 + 32 * s + 8 * g) * 2);
    }
    {
        float s0 = 0.f, s1 = 0.f;
#pragma unroll
        for (int q = 0; q < 4; ++q) {
            u32 pr = pk2(blo(gx[q]) * blo(gwv[q]), bhi(gx[q]) * bhi(gwv[q]));
            s0 += blo(pr); s1 += bhi(pr);
        }
        s0 = row16_sum(s0 + s1);
        s1 = s0;
        rows_reduce2<false>(s0, s1);
        float g0 = rb(__builtin_amdgcn_rcpf(1.f + fexp(-rb(s0))));
        if (l == 0) __builtin_amdgcn_raw_buffer_store_b32(f2u(g0), rws, WS_G + gtok * 4, 0, AUX_SC1);
    }
    STAMP(2);
    {
        const float dt = rb(fexp(ldt));
        const float zr2 = -rb(fexp(lar)) * dt * 1.4426950408889634f, zrev = aim * dt * 0.15915494309189535f;
        ldsp T = lds + kch * 33792 + kn * 4;
        auto put = [&](int row, cplx v) { lds_w32(T + row * 528, pk2(v.r, v.i)); };
        const cplx w128 = cexp_p(zr2, zrev, 128.f);
        cplx G; G.r = 2.f * cre * dt; G.i = 2.f * cim * dt;
        if (w < 4) {
            cplx wp[8];
            wp[1] = cexp_p(zr2, zrev, 1.f); wp[2] = cmul(wp[1], wp[1]); wp[3] = cmul(wp[2], wp[1]); wp[4] = cmul(wp[2], wp[2]);
            wp[5] = cmul(wp[4], wp[1]); wp[6] = cmul(wp[4], wp[2]); wp[7] = cmul(wp[4], wp[3]);
#pragma unroll
            for (int aa = 0; aa < 4; ++aa) {
                if (aa == 1) { FS4(7); publish(ws, WS_FUT + cg * 256 + tb * 8, tag); FS4(8); }
                if (aa < 2) {
                    put(8 * aa, G);
#pragma unroll
                    for (int cc = 1; cc < 8; ++cc) put(8 * aa + cc, cmul(G, wp[cc]));
                } else {
                    put(16 + 4 * (aa - 2), G);
#pragma unroll
                    for (int cc = 1; cc < 4; ++cc) put(16 + 4 * (aa - 2) + cc, cmul(G, wp[2 * cc]));
                }
                if (aa < 3) G = cmul(G, w128);
            }
        } else {
            const cplx w4 = cexp_p(zr2, zrev, 4.f);
            G = cmul(G, cexp_p(zr2, zrev, 512.f));
#pragma unroll
            for (int aa = 4; aa <= 16; ++aa) {
                if (aa == 10) { FS4(7); publish(ws, WS_FUT + cg * 256 + tb * 8, tag); FS4(8); }
                if (aa < 8) {
                    put(24 + 2 * (aa - 4), G);
                    put(24 + 2 * (aa - 4) + 1, cmul(G, w4));
                } else put(32 + aa - 8, G);
                if (aa < 16) G = cmul(G, w128);
            }
            cplx B[9];
            B[1] = cmul(w4, w4); B[2] = cmul(B[1], B[1]); B[3] = cmul(B[2], B[1]); B[4] = cmul(B[2], B[2]);
            B[5] = cmul(B[4], B[1]); B[6] = cmul(B[4], B[2]); B[7] = cmul(B[4], B[3]); B[8] = cmul(B[4], B[4]);
            lds_w32(T + 48 * 528, 0x00003f80u);
#pragma unroll
            for (int b = 1; b < 16; ++b) {
                cplx v = b <= 8 ? B[b] : cmul(B[8], B[b - 8]);
                lds_w32(T + (48 + b) * 528, pk2(v.r, -v.i));
            }
        }
    }
    FS4(9);
    bar();
    FS4(10);
    if (w == 7) wait_flags(ws, WS_FUT + xcd * 256 + (l & 31) * 8, tag);
    if (w < 6) {
        const int ch = w >= 3, tile = w - 3 * ch;
        ldsp T = lds + ch * 33792 + r16 * 528 + g * 16;
        f32x4 k0 = {0, 0, 0, 0};
#pragma unroll
        for (int s = 0; s < 8; ++s) k0 = mfma32(lds_r128(T + tile * 16 * 528 + s * 64), lds_r128(T + 48 * 528 + s * 64), k0);
        ldsp KC = lds + 67584 + ch * 1536 + r16 * 2 + (tile * 16 + 4 * g) * 32;
        u32 a0 = pk2(k0[0], k0[1]), a1 = pk2(k0[2], k0[3]);
        *(LAS u16*)(KC) = (u16)a0; *(LAS u16*)(KC + 32) = (u16)(a0 >> 16);
        *(LAS u16*)(KC + 64) = (u16)a1; *(LAS u16*)(KC + 96) = (u16)(a1 >> 16);
    }
    bar();
    STAMP(3);
    const int hch = w >> 2, t8 = t & 255;
    const u32 uoff = WS_UT + (ch0 + hch) * 4096 + t8 * 16;
    u32x4 d0 = ld128c(rws, uoff), d1 = ld128c(rws, uoff + 4), d2 = ld128c(rws, uoff + 8);
    u32x2 f = ld64c(rws, WS_UT + (ch0 + hch) * 4096);
    SCHED_A();
    {
        ldsp KC = lds + 67584 + hch * 1536;
        u32x4 v;
#pragma unroll
        for (int q = 0; q < 4; ++q) {
            u32 lo = *(LAS u16*)(KC + (idx[q] & 0xffffu)), hi = *(LAS u16*)(KC + (idx[q] >> 16));
            v[q] = lo | (hi << 16);
        }
        lds_w128(lds + hch * 4608 + t8 * 16, v);
        if (t8 < 32) { u32x4 z = {0, 0, 0, 0}; lds_w128(lds + hch * 4608 + 4096 + t8 * 16, z); }
    }
    SCHED_A();
    STAMP(4);
    constexpr int UB = 9216, CS = 4160;
    {
        u32x4 v1, v3;
#pragma unroll
        for (int q = 0; q < 4; ++q) { v1[q] = __builtin_amdgcn_alignbit(d1[q], d0[q], 16); v3[q] = __builtin_amdgcn_alignbit(d2[q], d1[q], 16); }
        ldsp U = lds + UB + hch * 4 * CS + 32 + t8 * 16;
        lds_w128(U, d0); lds_w128(U + CS, v1); lds_w128(U + 2 * CS, d1); lds_w128(U + 3 * CS, v3);
        if (t8 < 4) {
            u32x4 z = {0, 0, 0, 0}, y = z;
            if (t8 == 1) y.w = f.x << 16;
            if (t8 == 2) y.w = f.x;
            if (t8 == 3) { y.z = f.x << 16; y.w = __builtin_amdgcn_alignbit(f.y, f.x, 16); }
            ldsp F = lds + UB + (hch * 4 + t8) * CS;
            lds_w128(F, z); lds_w128(F + 16, y);
        }
    }
    bar();
    STAMP(5);
    {
        const int mq = w & 3;
        const int s = (r16 + 1) & 3;
        ldsp A0 = lds + UB + (hch * 4 + s) * CS + 2 * (1 + r16 - s + 8 * g) + mq * 2 * 64;
        ldsp B0 = lds + hch * 4608 + 4064 - 3584 - 32 * r16 + 16 * g + mq * 2 * 64;
        f32x4 acc[8];
#pragma unroll
        for (int i = 0; i < 8; ++i) acc[i] = f32x4{0, 0, 0, 0};
#pragma unroll
        for (int mm = 0; mm < 2; ++mm) {
            u32x4 Af[8], Bf[8];
#pragma unroll
            for (int i = 0; i < 8; ++i) {
                Bf[i] = lds_r128(B0 + mm * 64 + (7 - i) * 512);
                Af[i] = cat(lds_r64(A0 + mm * 64 + i * 512), lds_r64(A0 + mm * 64 + i * 512 + 8));
            }
#pragma unroll
            for (int i = 0; i < 8; ++i)
#pragma unroll
                for (int cc = i; cc < 8; ++cc) acc[cc] = mfma32(Af[i], Bf[cc - i], acc[cc]);
            __builtin_amdgcn_sched_barrier(0);
        }
        FSTAMP(7);
        bar();
        FSTAMP(8);
        ldsp R = lds + hch * 24576 + l * 16;
#pragma unroll
        for (int cc = 0; cc < 8; ++cc)
            if ((cc >> 1) != mq) lds_w128(R + (cc * 3 + ((mq - (cc >> 1) - 1) & 3)) * 1024, __builtin_bit_cast(u32x4, acc[cc]));
        bar();
        FSTAMP(9);
#pragma unroll
        for (int q = 0; q < 4; ++q)
            if (q == mq) {
                u32 pk[2][2];
#pragma unroll
                for (int h = 0; h < 2; ++h) {
                    float v[4];
#pragma unroll
                    for (int r = 0; r < 4; ++r) v[r] = acc[2 * q + h][r];
#pragma unroll
                    for (int sl = 0; sl < 3; ++sl) {
                        u32x4 o = lds_r128(R + ((2 * q + h) * 3 + sl) * 1024);
#pragma unroll
                        for (int r = 0; r < 4; ++r) v[r] += u2f(o[r]);
                    }
                    pk[h][0] = pk2(v[0], v[1]); pk[h][1] = pk2(v[2], v[3]);
                }
                auto sx = __builtin_amdgcn_permlane16_swap(pk[0][0], pk[1][0], false, false);
                auto sy = __builtin_amdgcn_permlane16_swap(pk[0][1], pk[1][1], false, false);
                u32x4 v = {sx[0], sy[0], sx[1], sy[1]};
                st128c(rws, v, WS_YT + (ch0 + hch) * 4096 + (256 * (2 * q + (g & 1)) + 16 * r16 + 4 * (g & 2)) * 2);
            }
    }
    FSTAMP(10);
    publish(ws, WS_FY + xcd * 256 + j * 8, tag);
    STAMP(6);
    return tag;
}

typedef float f32x16 __attribute__((ext_vector_type(16)));
DEV f32x16 mfma32x32(u32x4 a, u32x4 b, f32x16 c) {
    return __builtin_amdgcn_mfma_f32_32x32x16_bf16(__builtin_bit_cast(bf16x8, a), __builtin_bit_cast(bf16x8, b), c, 0, 0, 0);
}
DEV float vmax3(float a, float b, float c) { float r; asm("v_max3_f32 %0, %1, %2, %3" : "=v"(r) : "v"(a), "v"(b), "v"(c)); return r; }
DEV float vmin3(float a, float b, float c) { float r; asm("v_min3_f32 %0, %1, %2, %3" : "=v"(r) : "v"(a), "v"(b), "v"(c)); return r; }

template <bool POS, bool NEG> DEV void feature_map(u32x4 x0, u32x4 x1, u32x4 wown, int l, u32 (&ph)[16]) {
    const bool odd = (l >> 2) & 1;
    u32x4 a0, a1;
#pragma unroll
    for (int q = 0; q < 4; ++q) { a0[q] = odd ? 0u : wown[q]; a1[q] = odd ? wown[q] : 0u; }
    f32x16 pr;
#pragma unroll
    for (int r = 0; r < 16; ++r) pr[r] = 0.f;
    pr = mfma32x32(a0, x0, pr);
    pr = mfma32x32(a1, x1, pr);
    float p[16];
#pragma unroll
    for (int r = 0; r < 16; r += 2) { u32 v = pk2(pr[r], pr[r + 1]); p[r] = blo(v); p[r + 1] = bhi(v); }
    constexpr float LG = 1.4426950408889634f;
#pragma unroll
    for (int sg = 0; sg < 2; ++sg) {
        if (sg == 0 ? !POS : !NEG) continue;
        float m = sg ? vmin3(p[0], p[1], p[2]) : vmax3(p[0], p[1], p[2]);
#pragma unroll
        for (int r = 3; r < 15; r += 2) m = sg ? vmin3(m, p[r], p[r + 1]) : vmax3(m, p[r], p[r + 1]);
        m = sg ? vmin3(m, p[15], p[15]) : vmax3(m, p[15], p[15]);
        const float sc = sg ? -LG : LG, c = -m * sc;
        float a[16];
#pragma unroll
        for (int r = 0; r < 16; ++r) a[r] = __builtin_fmaf(p[r], sc, c);
#pragma unroll
        for (int r = 0; r < 16; ++r) a[r] = __builtin_amdgcn_exp2f(a[r]);
        float sa[4];
#pragma unroll
        for (int q = 0; q < 4; ++q) sa[q] = (a[4 * q] + a[4 * q + 1]) + (a[4 * q + 2] + a[4 * q + 3]);
        const float ia = __builtin_amdgcn_rcpf((sa[0] + sa[1]) + (sa[2] + sa[3]));
#pragma unroll
        for (int r = 0; r < 8; ++r) ph[8 * sg + r] = pk2(a[2 * r] * ia, a[2 * r + 1] * ia);
    }
}

DEV void role_attn(const Params& p, ldsp lds, char* ws, u64 e, int bb, int w, int l STAMP_ARG) {
    const int xcd = bb & 7, j = bb >> 3, g = l >> 4, r16 = l & 15, h = l >> 5, w4 = w & 3;
    const int rb_ = 8 * (xcd >> 2) + (j >> 2), cb = 4 * (xcd & 3) + (j & 3);
    const int rot = (j + xcd) & 7;
    rsrc_t rws = mkrsrc(ws, WS_USED);
#define SCHED() __builtin_amdgcn_sched_barrier(0)
    constexpr int XCH = 32768, WPH = 49152, PH = 51200, PHS = 10240, VV = 71680, VS = 5120, FR = 0, CNT = 8192;
    const int tok = 32 * w4 + (l & 31);
    const u32 wph = WPH + (l & 1) * 512 + (4 * h + (r16 >> 2)) * 32 + (8 * (g & 1) + 4 * ((l & 3) >> 1)) * 2;
    u64 tag;
    if (w >= 4) {
        __builtin_amdgcn_s_setprio(PRIO_BH);
        const u32x4 ru = mkdesc(p.u, L * D * 2), rw = mkdesc(p.wqkv, D * 3 * D * 2);
        const u32 xv = (rb_ * 128 + w4 * 8 + (l >> 3)) * 1024 + (((l & 7) ^ (((w4 & 1) << 2) | ((l >> 4) & 3))) * 16);
        const u32 wv = (16 * w4 + (l >> 2)) * 3072 + cb * 64 + (l & 3) * 16;
        auto issue_x = [&](int s) {
#pragma unroll
            for (int i = 0; i < 4; ++i) dma16(ru, lds + (s % 3) * 16384 + (4 * i + w4) * 1024, xv, ((s + rot) & 7) * 128 + i * 32768);
        };
        auto issue_w = [&](int s, int sec) {
            dma16(rw, lds + 49152 + ((3 * s + sec) & 7) * 4096 + w4 * 1024, wv, ((s + rot) & 7) * (64 * 3072) + sec * 1024);
        };
        FS3(6);
        issue_w(0, 0); issue_w(0, 1); issue_w(0, 2); issue_x(0);
        FS3(7);
        __builtin_amdgcn_s_barrier();
        FS3(8);
        issue_w(1, 0); issue_w(1, 1); issue_w(1, 2); issue_x(1);
        FS3(9);
#pragma unroll
        for (int s = 0; s < 8; ++s) {
            if (s == 0) WAIT_VM(7); else if (s < 7) WAIT_VM(6); else WAIT_VM(0);
            if (s == 0) FS3(10);
            __builtin_amdgcn_s_barrier();
            if (s >= 1 && s + 1 < 8) issue_w(s + 1, 2);
            if (s + 2 < 8) { issue_x(s + 2); issue_w(s + 2, 0); issue_w(s + 2, 1); }
            if (s == 6 && w == 4) dma16(mkdesc(p.wphi, 32 * 256 * 2), lds + WPH, l * 16, cb * 1024);
            if (s == 7 && w == 4 && l < 2) lds_w32(lds + CNT + l * 4, 0u);
        }
        tag = make_tag(e);
        bar();
        ldsp X = lds + XCH + w4 * 4096 + l * 16;
        u32x4 wown = cat(trread(lds + wph), trread(lds + wph + 8 * 32));
        u32x4 q0 = lds_r128(X), q1 = lds_r128(X + 1024);
        bar();
        bar();
        u32 ph[16];
        feature_map<true, true>(q0, q1, wown, l, ph);
        u32x4 bq[2][2];
#pragma unroll
        for (int sg = 0; sg < 2; ++sg)
#pragma unroll
            for (int q = 0; q < 4; ++q) {
                auto sw = __builtin_amdgcn_permlane32_swap(ph[8 * sg + q], ph[8 * sg + q + 4], false, false);
                bq[0][sg][q] = sw[0]; bq[1][sg][q] = sw[1];
            }
        STAMP(3);
        __builtin_amdgcn_s_setprio(PRIO_BH2);
        bar();
#pragma unroll
        for (int hh = 0; hh < 2; ++hh) {
            f32x16 nd;
#pragma unroll
            for (int r = 0; r < 16; ++r) nd[r] = 0.f;
            nd = mfma32x32(lds_r128(lds + FR + (2 * hh) * 1024 + l * 16), bq[hh][0], nd);
            nd = mfma32x32(lds_r128(lds + FR + (2 * hh + 1) * 1024 + l * 16), bq[hh][1], nd);
            const float inv = __builtin_amdgcn_rcpf(rb(rb(nd[8]) + 1e-6f));
            u32x4 o;
#pragma unroll
            for (int q = 0; q < 4; ++q) { u32 n = pk2(nd[2 * q], nd[2 * q + 1]); o[q] = pk2(blo(n) * inv, bhi(n) * inv); }
            st128c(rws, o, WS_OP + (rb_ * 128 + tok) * 1024 + cb * 64 + hh * 32 + h * 16);
        }
        FS2(10);
        publish_counted(ws, WS_FO + rb_ * 256 + cb * 8, tag, lds + CNT + 4, 4, l);
        STAMP(5);
    } else {
        __builtin_amdgcn_s_setprio(PRIO_BC);
        f32x16 acc[3];
#pragma unroll
        for (int sec = 0; sec < 3; ++sec)
#pragma unroll
            for (int r = 0; r < 16; ++r) acc[sec][r] = 0.f;
        const u32 xa = tok * 128 + ((h ^ ((tok >> 1) & 7)) * 16);
        const u32 wa = 49152 + (8 * h + (r16 >> 2)) * 64 + (g & 1) * 32 + 8 * (l & 3);
        __builtin_amdgcn_s_barrier();
#pragma unroll
        for (int s = 0; s < 8; ++s) {
            __builtin_amdgcn_s_barrier();
            asm volatile("" ::: "memory");
            if (s == 0) FS1(6);
            if (s == 1) FS1(7);
            if (s == 2) FS1(8);
            if (s == 4) FS1(9);
            if (s == 6) FS1(10);
            u32x4 xf[4], wf[12];
            auto read_x = [&](int ks) { xf[ks] = lds_r128(lds + (s % 3) * 16384 + (xa ^ (32 * ks))); };
            auto read_w = [&](int i) {
                ldsp a = lds + wa + ((3 * s + i % 3) & 7) * 4096 + (i / 3) * 1024;
                wf[i] = cat(trread(a), trread(a + 256));
            };
            read_x(0); read_w(0); read_w(1); read_w(2);
            SCHED();
#pragma unroll
            for (int i = 0; i < 12; ++i) {
                if (i % 3 == 0 && i / 3 + 1 < 4) read_x(i / 3 + 1);
                if (i + 3 < 12) read_w(i + 3);
                SCHED();
                acc[i % 3] = mfma32x32(wf[i], xf[i / 3], acc[i % 3]);
                SCHED();
            }
        }
        u32x4 fr[3][2];
#pragma unroll
        for (int sec = 0; sec < 3; ++sec)
#pragma unroll
            for (int hh = 0; hh < 2; ++hh)
#pragma unroll
                for (int q = 0; q < 4; ++q) fr[sec][hh][q] = pk2(acc[sec][8 * hh + 2 * q], acc[sec][8 * hh + 2 * q + 1]);
        lds_w128(lds + XCH + w * 4096 + l * 16, fr[0][0]);
        lds_w128(lds + XCH + w * 4096 + 1024 + l * 16, fr[0][1]);
        tag = make_tag(e);
        bar();
        STAMP(1);
#pragma unroll
        for (int hh = 0; hh < 2; ++hh) {
            ldsp q = lds + VV + hh * VS + tok * 40 + 8 * h;
            lds_w64(q, u32x2{fr[2][hh].x, fr[2][hh].y});
            lds_w64(q + 16, u32x2{fr[2][hh].z, fr[2][hh].w});
        }
        {
            u32x4 wown = cat(trread(lds + wph), trread(lds + wph + 8 * 32));
            u32 ph[16];
            feature_map<true, true>(fr[1][0], fr[1][1], wown, l, ph);
            ldsp q = lds + PH + h * PHS + tok * 80;
#pragma unroll
            for (int i = 0; i < 4; ++i) lds_w128(q + i * 16, u32x4{ph[4 * i], ph[4 * i + 1], ph[4 * i + 2], ph[4 * i + 3]});
        }
        FS2(6);
        bar();
        FS2(7);
        const int hh = w >> 1, sg = w & 1;
        {
            f32x4 kv = {0, 0, 0, 0}, z = {0, 0, 0, 0};
            const u32x4 ones = {0x3f803f80u, 0x3f803f80u, 0x3f803f80u, 0x3f803f80u};
            ldsp pa = lds + PH + hh * PHS + (8 * g + (r16 >> 2)) * 80 + sg * 32 + 8 * (l & 3);
            ldsp pb = lds + VV + hh * VS + (8 * g + (r16 >> 2)) * 40 + 8 * (l & 3);
#pragma unroll
            for (int tg = 0; tg < 4; ++tg) {
                u32x4 A = cat(trread(pa + tg * 32 * 80), trread(pa + tg * 32 * 80 + 4 * 80));
                u32x4 B = cat(trread(pb + tg * 32 * 40), trread(pb + tg * 32 * 40 + 4 * 40));
                kv = mfma32(A, B, kv);
                z = mfma32(A, ones, z);
            }
            st128c(rws, __builtin_bit_cast(u32x4, kv), WS_KVP + ((cb * 16 + rb_) * 4 + w) * 1024 + l * 16);
            if (r16 == 0) st128c(rws, __builtin_bit_cast(u32x4, z), WS_ZP + ((cb * 16 + rb_) * 4 + w) * 64 + g * 16);
        }
        FS2(8);
        bar();
        publish_counted(ws, WS_FKV + cb * 256 + rb_ * 8, tag, lds + CNT, 4, l);
        STAMP(2);
        wait_flags(ws, WS_FKV + cb * 256 + r16 * 8, tag);
        STAMP(4);
        u32x4 part[16];
#pragma unroll
        for (int r = 0; r < 16; ++r) part[r] = ld128c(rws, WS_KVP + (cb * 16 * 4 + w) * 1024 + l * 16, r * 4096);
        u32x4 zp = ld128c(rws, WS_ZP + ((cb * 16 + (l >> 2)) * 4 + w) * 64 + (l & 3) * 16);
        float s[4] = {u2f(part[0].x), u2f(part[0].y), u2f(part[0].z), u2f(part[0].w)};
#pragma unroll
        for (int r = 1; r < 16; ++r)
#pragma unroll
            for (int q = 0; q < 4; ++q) s[q] += u2f(part[r][q]);
        float zs[4] = {u2f(zp.x), u2f(zp.y), u2f(zp.z), u2f(zp.w)};
#pragma unroll
        for (int q = 0; q < 4; ++q) {
            float x = zs[q];
            x += dpp_mov<0x124>(x);
            x += dpp_mov<0x128>(x);
            zs[q] = x;
        }
        rows_reduce2<false>(zs[0], zs[1]);
        rows_reduce2<false>(zs[2], zs[3]);
        FS2(9);
        ldsp F = lds + FR + w * 1024;
        lds_w64(F + (r16 + 32 * (g >> 1)) * 16 + 8 * (g & 1), u32x2{pk2(s[0], s[1]), pk2(s[2], s[3])});
        lds_w64(F + (16 + (l >> 2) + 32 * ((l & 3) >> 1)) * 16 + 8 * (l & 1), u32x2{pk2(zs[0], zs[1]), pk2(zs[2], zs[3])});
        bar();
    }
}

DEV void final_stage(const Params& p, ldsp lds, char* ws, u64 tag, int ba, int w, int l STAMP_ARG) {
    const int t = threadIdx.x, g = l >> 4, r16 = l & 15, cw = w & 3, th = w >> 2;
    const int tt64 = 4 * (ba & 7) + ((ba >> 3) & 3), ct = ba >> 5;
    rsrc_t ru = mkrsrc(p.u, L * D * 2), rws = mkrsrc(ws, WS_USED), rout = mkrsrc(p.out, L * D * 2);
    const int tok0 = tt64 * 64, chl = ct * 64 + 16 * cw + 4 * g, tok = tok0 + 32 * th + r16;
    constexpr int YL = 4096;
    bar();
    wo_prefetch<8>(p, lds, ct, w, l);
    u32x2 uu[2];
#pragma unroll
    for (int tt = 0; tt < 2; ++tt) uu[tt] = ld64(ru, (tok + 16 * tt) * 1024 + chl * 2);
    const u32x2 dsk = *(const GAS u32x2*)(p.dsk + chl);
    if (ba == 0 && w >= 4) {
        const int q = t - 256;
        wait_flags(ws, WS_FUT + (q >> 5) * 256 + (q & 31) * 8, tag);
        wait_flags(ws, WS_FKV + (q >> 4) * 256 + (q & 15) * 8, tag);
    }
    if (w == 0) {
        const int tg8 = tt64 * 8 + (l & 7), ga = tg8 >> 7, gc = (tg8 >> 5) & 3, gj = tg8 & 31;
        const int gtb = 16 * ga + (gj >> 1), gcg = 2 * gc + (gj & 1);
        wait_flags(ws, l < 32 ? WS_FY + ct * 256 + l * 8 : WS_FUT + gcg * 256 + gtb * 8, tag);
    }
    WAIT_VM(0);
    bar();
    if (ba == 0 && t == 0) flag_st(ws, WS_E, tag);
    STAMP(8);
    float gate[2];
    {
        u32x4 y = ld128c(rws, WS_YT + (ct * 64 + (t >> 3)) * 4096 + tok0 * 2 + (t & 7) * 16);
#pragma unroll
        for (int tt = 0; tt < 2; ++tt) gate[tt] = u2f(ld32c(rws, WS_G + (tok + 16 * tt) * 4));
        lds_w128(lds + YL + (t >> 3) * 128 + (t & 7) * 16, y);
    }
    u32x4 wf[16];
    {
        const int row = 16 * (g >> 1) + 4 * (g & 1) + (r16 >> 2);
        ldsp q = lds + 16384 + row * 128 + ((cw ^ ((row >> 1) & 3)) * 32) + 8 * (l & 3);
#pragma unroll
        for (int ks = 0; ks < 16; ++ks) wf[ks] = cat(trread(q + ks * 4096), trread(q + ks * 4096 + 1024));
    }
    bar();
    float ga[2][4], g1[2];
#pragma unroll
    for (int tt = 0; tt < 2; ++tt) {
        u32x2 y = trread(lds + YL + (16 * cw + 4 * g + (r16 >> 2)) * 128 + (32 * th + 16 * tt) * 2 + 8 * (l & 3));
        const float gg = gate[tt];
        g1[tt] = rb(1.f - gg);
        u32 t0 = pk2(blo(uu[tt].x) * blo(dsk.x), bhi(uu[tt].x) * bhi(dsk.x)), t1 = pk2(blo(uu[tt].y) * blo(dsk.y), bhi(uu[tt].y) * bhi(dsk.y));
        u32 a0 = pk2(blo(y.x) + blo(t0), bhi(y.x) + bhi(t0)), a1 = pk2(blo(y.y) + blo(t1), bhi(y.y) + bhi(t1));
        u32 ga0 = pk2(gg * blo(a0), gg * bhi(a0)), ga1 = pk2(gg * blo(a1), gg * bhi(a1));
        ga[tt][0] = blo(ga0); ga[tt][1] = bhi(ga0); ga[tt][2] = blo(ga1); ga[tt][3] = bhi(ga1);
        asm volatile("" : "+v"(ga[tt][0]), "+v"(ga[tt][1]), "+v"(ga[tt][2]), "+v"(ga[tt][3]), "+v"(g1[tt]));
    }
    STAMP(9);
    __builtin_amdgcn_s_setprio(PRIO_F);
    if (w == 0) wait_flags(ws, WS_FO + (tok0 >> 7) * 256 + r16 * 8, tag);
    bar();
    STAMP(10);
#pragma unroll
    for (int hf = 0; hf < 2; ++hf) {
        u32x4 ov[4];
#pragma unroll
        for (int i = 0; i < 4; ++i) ov[i] = ld128c(rws, WS_OP + (tok0 + 8 * w + 4 * hf) * 1024 + l * 16, i * 1024);
#pragma unroll
        for (int i = 0; i < 4; ++i) {
            const int tk = 8 * w + 4 * hf + i;
            lds_w128(lds + 16384 + tk * 1024 + ((l ^ (tk & 15)) * 16), ov[i]);
        }
    }
    bar();
    STAMP(11);
    f32x4 acc[2] = {f32x4{0, 0, 0, 0}, f32x4{0, 0, 0, 0}};
    {
        u32 ob[4];
#pragma unroll
        for (int k4 = 0; k4 < 4; ++k4) ob[k4] = 16384 + (32 * th + r16) * 1024 + ((((4 * k4) ^ (r16 & 12)) | (g ^ (r16 & 3))) * 16);
#pragma unroll
        for (int ks = 0; ks < 16; ++ks)
#pragma unroll
            for (int tt = 0; tt < 2; ++tt)
                acc[tt] = mfma32(wf[ks], lds_r128(lds + ob[ks & 3] + (ks >> 2) * 256 + tt * 16384), acc[tt]);
    }
#pragma unroll
    for (int tt = 0; tt < 2; ++tt) {
        u32x2 o = {pk2(__builtin_fmaf(g1[tt], acc[tt][0], ga[tt][0]), __builtin_fmaf(g1[tt], acc[tt][1], ga[tt][1])),
                   pk2(__builtin_fmaf(g1[tt], acc[tt][2], ga[tt][2]), __builtin_fmaf(g1[tt], acc[tt][3], ga[tt][3]))};
        __builtin_amdgcn_raw_buffer_store_b64(o, rout, (tok + 16 * tt) * 1024 + chl * 2, 0, 0);
    }
    STAMP(12);
}

__global__ __launch_bounds__(512, 4) void s4d_hedgehog_mix(Params p) {
    extern __shared__ __attribute__((aligned(16))) char smem[];
    ldsp lds = (ldsp)smem;
    char* ws = p.ws;
    const int b = blockIdx.x, l = threadIdx.x & 63, w = uni(threadIdx.x >> 6);
    STAMP(0);
    asm volatile("" :: "s"(p.ws), "s"(p.u), "s"(p.wqkv), "s"(p.ldt), "s"(p.gw));
    const u64 e = flag_ld(ws, WS_E);
    if (b >= 256) {
        const u64 tag = role_conv(p, lds, ws, e, b - 256, w, l STAMP_PASS);
        final_stage(p, lds, ws, tag, b - 256, w, l STAMP_PASS);
    } else {
        role_attn(p, lds, ws, e, b, w, l STAMP_PASS);
    }
}

__global__ void k_tiny(char* ws) { if (ws == nullptr) __builtin_trap(); }
#if defined(__HIP_DEVICE_COMPILE__)
#pragma clang attribute pop
#endif

extern "C" void kernel_launch(void* const* d_in, const int* in_sizes, int n_in, void* d_out, int out_size, void* d_ws, size_t ws_size,
                              hipStream_t stream) {
    Params p;
    p.u = (const u16*)d_in[0]; p.lar = (const u16*)d_in[1]; p.aim = (const u16*)d_in[2]; p.cre = (const u16*)d_in[3];
    p.cim = (const u16*)d_in[4]; p.ldt = (const u16*)d_in[5]; p.dsk = (const u16*)d_in[6]; p.wqkv = (const u16*)d_in[8];
    p.wphi = (const u16*)d_in[9]; p.wo = (const u16*)d_in[10]; p.gw = (const u16*)d_in[11]; p.out = (u16*)d_out; p.ws = (char*)d_ws;
    (void)hipFuncSetAttribute((const void*)s4d_hedgehog_mix, hipFuncAttributeMaxDynamicSharedMemorySize, LDS_BYTES);
    s4d_hedgehog_mix<<<512, 512, LDS_BYTES, stream>>>(p);
    (void)hipFuncSetAttribute((const void*)k_tiny, hipFuncAttributeMaxDynamicSharedMemorySize, LDS_BYTES);
    k_tiny<<<POST_K, POST_T, POST_L, stream>>>(p.ws);
}
```

```cpp
#define ENC_SKEW 510
#define ENC_REF 256
#include <hip/hip_runtime.h>
#include <hip/hip_bf16.h>
#include <stdint.h>

#if defined(__HIP_DEVICE_COMPILE__)
#pragma clang attribute push(__attribute__((target("no-packed-fp32-ops"))), apply_to = function)
#endif

typedef unsigned short u16;
typedef unsigned u32;
typedef unsigned long long u64;
typedef float f32x2 __attribute__((ext_vector_type(2)));
typedef float f32x4 __attribute__((ext_vector_type(4)));
typedef u32 u32x2 __attribute__((ext_vector_type(2)));
typedef u32 u32x4 __attribute__((ext_vector_type(4)));
typedef short s16x4 __attribute__((ext_vector_type(4)));
typedef __bf16 bf16x2 __attribute__((ext_vector_type(2)));
typedef __bf16 bf16x8 __attribute__((ext_vector_type(8)));
#define LAS __attribute__((address_space(3)))
#define GAS __attribute__((address_space(1)))
typedef LAS char* ldsp;
typedef __amdgpu_buffer_rsrc_t rsrc_t;
#define DEV __device__ __forceinline__

constexpr int L = 2048, D = 512;
constexpr int LDS_BYTES = 81920;
constexpr u64 TAG_STEP = 0x9E3779B97F4A7C15ull;
constexpr u32 SPIN_CAP = 40000;

constexpr u32 WS_E = 0, WS_TMO = 64;
constexpr u32 WS_FUT = 4096, WS_FKV = WS_FUT + 4096, WS_FY = WS_FKV + 4096, WS_FO = WS_FY + 4096;
constexpr u32 WS_G = 0x28000;
constexpr u32 WS_UT = 0x40000;
constexpr u32 WS_YT = WS_UT + 0x200000;
constexpr u32 WS_OP = WS_YT + 0x200000;
constexpr u32 WS_KVP = WS_OP + 0x200000;
constexpr u32 WS_ZP = WS_KVP + 0x100000;
constexpr u32 WS_DBG = WS_ZP + 0x10000;
constexpr u32 WS_USED = WS_DBG + 0x400000;

constexpr int AUX_SC1 = 16;

struct IdxTab { u16 v[2048]; };
constexpr IdxTab make_idx() {
    IdxTab t{};
    for (int i = 0; i < 2048; ++i) {
        int j = 2047 - i, p = j;
        int sh = j >= 1024 ? 3 : (j >= 512 ? 2 : (j >= 256 ? 1 : 0));
        if (sh) {
            int q = j >> sh, rem = j & ((1 << sh) - 1), half = 1 << (sh - 1);
            if (rem > half || (rem == half && (q & 1))) ++q;
            p = q << sh;
        }
        int row = 0, col = (p >> 3) & 15;
        if (p < 256) row = (p & 7) + 8 * (p >> 7);
        else if (p < 512) row = 16 + ((p - 256) >> 7) * 4 + ((p & 7) >> 1);
        else if (p < 1024) row = 24 + ((p - 512) >> 7) * 2 + ((p & 7) >> 2);
        else row = 32 + ((p - 1024) >> 7);
        t.v[i] = (u16)((row * 16 + col) * 2);
    }
    return t;
}
__device__ const IdxTab g_idx = make_idx();

DEV float u2f(u32 x) { return __builtin_bit_cast(float, x); }
DEV u32 f2u(float x) { return __builtin_bit_cast(u32, x); }
DEV float fexp(float x) { return __builtin_amdgcn_exp2f(x * 1.4426950408889634f); }
template <int CTRL> DEV float dpp_mov(float x) { return u2f(__builtin_amdgcn_update_dpp(0u, f2u(x), CTRL, 0xf, 0xf, true)); }
DEV float row16_sum(float x) {
    x += dpp_mov<0xB1>(x);
    x += dpp_mov<0x4E>(x);
    x += dpp_mov<0x141>(x);
    x += dpp_mov<0x140>(x);
    return x;
}
DEV u32 pk2(float lo, float hi) { f32x2 v = {lo, hi}; return __builtin_bit_cast(u32, __builtin_convertvector(v, bf16x2)); }
DEV float blo(u32 x) { return u2f(x << 16); }
DEV float bhi(u32 x) { return u2f(x & 0xffff0000u); }
DEV float rb(float f) { return blo(pk2(f, 0.f)); }
DEV f32x4 mfma32(u32x4 a, u32x4 b, f32x4 c) {
    return __builtin_amdgcn_mfma_f32_16x16x32_bf16(__builtin_bit_cast(bf16x8, a), __builtin_bit_cast(bf16x8, b), c, 0, 0, 0);
}
DEV f32x4 mfma16(u32x2 a, u32x2 b, f32x4 c) {
    return __builtin_amdgcn_mfma_f32_16x16x16bf16_1k(__builtin_bit_cast(s16x4, a), __builtin_bit_cast(s16x4, b), c, 0, 0, 0);
}
DEV u32x2 trread(ldsp p) { return __builtin_bit_cast(u32x2, __builtin_amdgcn_ds_read_tr16_b64_v4i16((LAS s16x4*)p)); }
DEV u32x4 lds_r128(ldsp p) { return *(LAS u32x4*)p; }
DEV u32x2 lds_r64(ldsp p) { return *(volatile LAS u32x2*)p; }
DEV void lds_w128(ldsp p, u32x4 v) { *(LAS u32x4*)p = v; }
DEV void lds_w64(ldsp p, u32x2 v) { *(LAS u32x2*)p = v; }
DEV void lds_w32(ldsp p, u32 v) { *(LAS u32*)p = v; }
DEV u32x4 cat(u32x2 a, u32x2 b) { u32x4 r = {a.x, a.y, b.x, b.y}; return r; }
DEV rsrc_t mkrsrc(const void* p, u32 bytes) { return __builtin_amdgcn_make_buffer_rsrc((void*)p, 0, bytes, 0x00020000); }
DEV u32x4 mkdesc(const void* p, u32 bytes) {
    u64 a = (u64)p;
    u32x4 d = {(u32)a, (u32)(a >> 32) & 0xffffu, bytes, 0x00020000u};
    return d;
}
DEV void dma16(u32x4 desc, ldsp dst, u32 voff, u32 soff) {
    asm volatile("s_mov_b32 m0, %0\n\ts_nop 0\n\tbuffer_load_dwordx4 %1, %2, %3 offen lds"
                 :: "s"((u32)(uintptr_t)dst), "v"(voff), "s"(desc), "s"(soff) : "memory");
}
DEV u32x4 ld128(rsrc_t r, u32 voff, u32 soff = 0) { return __builtin_amdgcn_raw_buffer_load_b128(r, voff, soff, 0); }
DEV u32x2 ld64(rsrc_t r, u32 voff, u32 soff = 0) { return __builtin_amdgcn_raw_buffer_load_b64(r, voff, soff, 0); }
DEV u32 ld16(rsrc_t r, u32 voff, u32 soff = 0) { return __builtin_amdgcn_raw_buffer_load_b16(r, voff, soff, 0); }
DEV u32x4 ld128c(rsrc_t r, u32 voff, u32 soff = 0) { return __builtin_amdgcn_raw_buffer_load_b128(r, voff, soff, AUX_SC1); }
DEV u32x2 ld64c(rsrc_t r, u32 voff, u32 soff = 0) { return __builtin_amdgcn_raw_buffer_load_b64(r, voff, soff, AUX_SC1); }
DEV u32 ld32c(rsrc_t r, u32 voff, u32 soff = 0) { return __builtin_amdgcn_raw_buffer_load_b32(r, voff, soff, AUX_SC1); }
DEV void st128c(rsrc_t r, u32x4 v, u32 voff, u32 soff = 0) { __builtin_amdgcn_raw_buffer_store_b128(v, r, voff, soff, AUX_SC1); }
DEV void st64c(rsrc_t r, u32x2 v, u32 voff, u32 soff = 0) { __builtin_amdgcn_raw_buffer_store_b64(v, r, voff, soff, AUX_SC1); }
#define WAIT_VM(n) asm volatile("s_waitcnt vmcnt(" #n ")" ::: "memory")
#define SCHED_A() __builtin_amdgcn_sched_barrier(0)
#define WAIT_LGKM0() asm volatile("s_waitcnt lgkmcnt(0)" ::: "memory")
DEV void bar() { asm volatile("s_waitcnt lgkmcnt(0)" ::: "memory"); __builtin_amdgcn_s_barrier(); asm volatile("" ::: "memory"); }
#define PRIO_A0 3
#define PRIO_A1 1
#define PRIO_BH2 3
#define PRIO_F 3
#define PRIO_BC 2
#define PRIO_BH 0
DEV u32 uni(u32 x) { return __builtin_amdgcn_readfirstlane(x); }

DEV float swap16_lo(float a, float b, float& other) {
    auto s = __builtin_amdgcn_permlane16_swap(f2u(a), f2u(b), false, false);
    other = u2f(s[1]);
    return u2f(s[0]);
}
DEV float swap32_lo(float a, float b, float& other) {
    auto s = __builtin_amdgcn_permlane32_swap(f2u(a), f2u(b), false, false);
    other = u2f(s[1]);
    return u2f(s[0]);
}
template <bool MAX> DEV void rows_reduce2(float& a, float& b) {
    float o, t = swap16_lo(a, b, o);
    t = MAX ? __builtin_fmaxf(t, o) : t + o;
    float u = swap32_lo(t, t, o);
    u = MAX ? __builtin_fmaxf(u, o) : u + o;
    a = swap16_lo(u, u, b);
}

typedef GAS u64 gu64;
DEV u64 make_tag(u64 e) {
    u32 lo = (u32)e, hi = (u32)(e >> 32);
    asm volatile("" : "+v"(lo), "+v"(hi));
    return (((u64)uni(hi) << 32) | uni(lo)) + TAG_STEP;
}
DEV u64 flag_ld(char* ws, u32 off) { return __hip_atomic_load((gu64*)(ws + off), __ATOMIC_RELAXED, __HIP_MEMORY_SCOPE_AGENT); }
DEV void flag_st(char* ws, u32 off, u64 v) { __hip_atomic_store((gu64*)(ws + off), v, __ATOMIC_RELAXED, __HIP_MEMORY_SCOPE_AGENT); }
DEV void wait_flags(char* ws, u32 off, u64 tag) {
#pragma unroll 1
    for (u32 spins = 0; spins < SPIN_CAP; ++spins) {
        u64 v = flag_ld(ws, off);
        if (__builtin_amdgcn_ballot_w64(v != tag) == 0) { asm volatile("" ::: "memory"); return; }
        __builtin_amdgcn_s_sleep(1);
    }
    flag_st(ws, WS_TMO, tag);
}
DEV void publish(char* ws, u32 off, u64 tag) {
    WAIT_VM(0);
    bar();
    if (threadIdx.x == 0) flag_st(ws, off, tag);
}

DEV void publish_counted(char* ws, u32 off, u64 tag, ldsp cnt, u32 n, int l) {
    WAIT_VM(0);
    u32 old = 0;
    if (l == 0) old = __hip_atomic_fetch_add((LAS u32*)cnt, 1u, __ATOMIC_RELAXED, __HIP_MEMORY_SCOPE_WORKGROUP);
    if (l == 0 && old == n - 1) flag_st(ws, off, tag);
}

#define STAMP(i) asm volatile("; MARK " #i)
#define FSTAMP(i) do { } while (0)
#define FS1(i) do { } while (0)
#define FS2(i) do { } while (0)
#define FS3(i) do { } while (0)
#define FS4(i) do { } while (0)
#define STAMP_ARG
#define STAMP_PASS

struct Params {
    char* ws; const u16* u; const u16* wqkv; const u16* wphi; const u16* lar; const u16* aim; const u16* cre; const u16* cim;
    const u16* ldt; const u16* gw; const u16* dsk; const u16* wo; u16* out;
};

struct cplx { float r, i; };
DEV cplx cmul(cplx a, cplx b) { cplx c; c.r = a.r * b.r - a.i * b.i; c.i = a.r * b.i + a.i * b.r; return c; }
DEV cplx cexp_p(float zr2, float zrev, float p) {
    float e = __builtin_amdgcn_exp2f(zr2 * p);
    float a = __builtin_amdgcn_fractf(zrev * p);
    cplx c; c.r = e * __builtin_amdgcn_cosf(a); c.i = e * __builtin_amdgcn_sinf(a); return c;
}

template <int NW> DEV void wo_prefetch(const Params& p, ldsp lds, int ct, int w, int l) {
    const u32x4 rwo = mkdesc(p.wo, D * D * 2);
    u32 voff = (l >> 3) * 1024 + ct * 128 + ((((l & 7) >> 1) ^ ((l >> 4) & 3)) * 32) + (l & 1) * 16;
#pragma unroll
    for (int i = 0; i < 64 / NW; ++i) {
        int piece = i * NW + w;
        dma16(rwo, lds + 16384 + piece * 1024, voff, piece * 8192);
    }
}

DEV u32 touch_pages(const Params& p, int b, int l) {
    const int k = l & 15;
    const char* a = p.ws + WS_KVP + (b & 255) * 4096;
    if (k == 1) a = p.ws + WS_OP + (size_t)(b & 255) * 8192;
    if (k == 2) a = p.ws + WS_YT + (size_t)(b & 255) * 8192;
    if (k == 3) a = p.ws + WS_UT + (size_t)(b & 255) * 8192;
    if (k == 4) a = p.ws + WS_ZP + (b & 255) * 256;
    if (k == 5) a = p.ws + WS_G;
    if (k == 6) a = (const char*)p.out + (size_t)(b & 255) * 8192;
    if (k == 7) a = (const char*)p.wo + (size_t)(b & 255) * 2048;
    if (k == 8) a = (const char*)p.dsk;
    if (k == 9) a = (const char*)p.wphi;
    if (k == 10) a = p.ws + WS_OP + 0x1ff000;
    if (k == 11) a = p.ws + WS_YT + 0x1ff000;
    if (k == 12) a = p.ws + WS_UT + 0x1ff000;
    if (k == 13) a = (const char*)p.out + 0x1ff000;
    return *(const volatile GAS u32*)a;
}

DEV u64 role_conv(const Params& p, ldsp lds, char* ws, u64 e, int ba, int w, int l STAMP_ARG) {
    const int t = threadIdx.x, xcd = ba & 7, j = ba >> 3, g = l >> 4, r16 = l & 15;
    const int a = xcd >> 2, c = xcd & 3;
    rsrc_t ru = mkrsrc(p.u, L * D * 2), rws = mkrsrc(ws, WS_USED);
    const int ch0 = 2 * (32 * xcd + j);
    __builtin_amdgcn_s_setprio(PRIO_A0);
    const int tb = 16 * a + (j >> 1), cg = 2 * c + (j & 1);
    {
        u32 voff = (tb * 64 + (l >> 3)) * 1024 + cg * 128 + (l & 7) * 16;
        dma16(mkdesc(p.u, L * D * 2), lds + 70656 + w * 1024, voff, w * 8192);
    }
    const int gtok = 1024 * a + 8 * (32 * c + j) + w;
    u32x4 gx = ld128(ru, gtok * 1024 + l * 16);
    u32x4 gwv = *(const GAS u32x4*)(p.gw + l * 8);
    const int kch = (t >> 7) & 1, kn = t & 127;
    const int pidx = (ch0 + kch) * 128 + kn;
    float lar = blo(p.lar[pidx]), aim = blo(p.aim[pidx]), cre = blo(p.cre[pidx]), cim = blo(p.cim[pidx]), ldt = blo(p.ldt[ch0 + kch]);
    u32x4 idx = *(const GAS u32x4*)(g_idx.v + (t & 255) * 8);
    if (w == 7) { u32 x = touch_pages(p, ba, l); asm volatile("" :: "v"(x)); }
    SCHED_A();
    __builtin_amdgcn_s_setprio(PRIO_A1);
    WAIT_VM(0);
    const u64 tag = make_tag(e);
    bar();
    STAMP(1);
    {
        const int cw = w & 3, s = w >> 2;
        ldsp src = lds + 70656 + (32 * s + 8 * g + (r16 >> 2)) * 128 + 32 * cw + 8 * (l & 3);
        u32x4 v = cat(trread(src), trread(src + 4 * 128));
        st128c(rws, v, WS_UT + (cg * 64 + 16 * cw + r16) * 4096 + (tb * 64 + 32 * s + 8 * g) * 2);
    }
    {
        float s0 = 0.f, s1 = 0.f;
#pragma unroll
        for (int q = 0; q < 4; ++q) {
            u32 pr = pk2(blo(gx[q]) * blo(gwv[q]), bhi(gx[q]) * bhi(gwv[q]));
            s0 += blo(pr); s1 += bhi(pr);
        }
        s0 = row16_sum(s0 + s1);
        s1 = s0;
        rows_reduce2<false>(s0, s1);
        float g0 = rb(__builtin_amdgcn_rcpf(1.f + fexp(-rb(s0))));
        if (l == 0) __builtin_amdgcn_raw_buffer_store_b32(f2u(g0), rws, WS_G + gtok * 4, 0, AUX_SC1);
    }
    STAMP(2);
    {
        const float dt = rb(fexp(ldt));
        const float zr2 = -rb(fexp(lar)) * dt * 1.4426950408889634f, zrev = aim * dt * 0.15915494309189535f;
        ldsp T = lds + kch * 33792 + kn * 4;
        auto put = [&](int row, cplx v) { lds_w32(T + row * 528, pk2(v.r, v.i)); };
        const cplx w128 = cexp_p(zr2, zrev, 128.f);
        cplx G; G.r = 2.f * cre * dt; G.i = 2.f * cim * dt;
        if (w < 4) {
            cplx wp[8];
            wp[1] = cexp_p(zr2, zrev, 1.f); wp[2] = cmul(wp[1], wp[1]); wp[3] = cmul(wp[2], wp[1]); wp[4] = cmul(wp[2], wp[2]);
            wp[5] = cmul(wp[4], wp[1]); wp[6] = cmul(wp[4], wp[2]); wp[7] = cmul(wp[4], wp[3]);
#pragma unroll
            for (int aa = 0; aa < 4; ++aa) {
                if (aa == 1) { FS4(7); publish(ws, WS_FUT + cg * 256 + tb * 8, tag); FS4(8); }
                if (aa < 2) {
                    put(8 * aa, G);
#pragma unroll
                    for (int cc = 1; cc < 8; ++cc) put(8 * aa + cc, cmul(G, wp[cc]));
                } else {
                    put(16 + 4 * (aa - 2), G);
#pragma unroll
                    for (int cc = 1; cc < 4; ++cc) put(16 + 4 * (aa - 2) + cc, cmul(G, wp[2 * cc]));
                }
                if (aa < 3) G = cmul(G, w128);
            }
        } else {
            const cplx w4 = cexp_p(zr2, zrev, 4.f);
            G = cmul(G, cexp_p(zr2, zrev, 512.f));
#pragma unroll
            for (int aa = 4; aa <= 16; ++aa) {
                if (aa == 10) { FS4(7); publish(ws, WS_FUT + cg * 256 + tb * 8, tag); FS4(8); }
                if (aa < 8) {
                    put(24 + 2 * (aa - 4), G);
                    put(24 + 2 * (aa - 4) + 1, cmul(G, w4));
                } else put(32 + aa - 8, G);
                if (aa < 16) G = cmul(G, w128);
            }
            cplx B[9];
            B[1] = cmul(w4, w4); B[2] = cmul(B[1], B[1]); B[3] = cmul(B[2], B[1]); B[4] = cmul(B[2], B[2]);
            B[5] = cmul(B[4], B[1]); B[6] = cmul(B[4], B[2]); B[7] = cmul(B[4], B[3]); B[8] = cmul(B[4], B[4]);
            lds_w32(T + 48 * 528, 0x00003f80u);
#pragma unroll
            for (int b = 1; b < 16; ++b) {
                cplx v = b <= 8 ? B[b] : cmul(B[8], B[b - 8]);
                lds_w32(T + (48 + b) * 528, pk2(v.r, -v.i));
            }
        }
    }
    FS4(9);
    bar();
    FS4(10);
    if (w == 7) wait_flags(ws, WS_FUT + xcd * 256 + (l & 31) * 8, tag);
    if (w < 6) {
        const int ch = w >= 3, tile = w - 3 * ch;
        ldsp T = lds + ch * 33792 + r16 * 528 + g * 16;
        f32x4 k0 = {0, 0, 0, 0};
#pragma unroll
        for (int s = 0; s < 8; ++s) k0 = mfma32(lds_r128(T + tile * 16 * 528 + s * 64), lds_r128(T + 48 * 528 + s * 64), k0);
        ldsp KC = lds + 67584 + ch * 1536 + r16 * 2 + (tile * 16 + 4 * g) * 32;
        u32 a0 = pk2(k0[0], k0[1]), a1 = pk2(k0[2], k0[3]);
        *(LAS u16*)(KC) = (u16)a0; *(LAS u16*)(KC + 32) = (u16)(a0 >> 16);
        *(LAS u16*)(KC + 64) = (u16)a1; *(LAS u16*)(KC + 96) = (u16)(a1 >> 16);
    }
    bar();
    STAMP(3);
    const int hch = w >> 2, t8 = t & 255;
    const u32 uoff = WS_UT + (ch0 + hch) * 4096 + t8 * 16;
    u32x4 d0 = ld128c(rws, uoff), d1 = ld128c(rws, uoff + 4), d2 = ld128c(rws, uoff + 8);
    u32x2 f = ld64c(rws, WS_UT + (ch0 + hch) * 4096);
    SCHED_A();
    {
        ldsp KC = lds + 67584 + hch * 1536;
        u32x4 v;
#pragma unroll
        for (int q = 0; q < 4; ++q) {
            u32 lo = *(LAS u16*)(KC + (idx[q] & 0xffffu)), hi = *(LAS u16*)(KC + (idx[q] >> 16));
            v[q] = lo | (hi << 16);
        }
        lds_w128(lds + hch * 4608 + t8 * 16, v);
        if (t8 < 32) { u32x4 z = {0, 0, 0, 0}; lds_w128(lds + hch * 4608 + 4096 + t8 * 16, z); }
    }
    SCHED_A();
    STAMP(4);
    constexpr int UB = 9216, CS = 4160;
    {
        u32x4 v1, v3;
#pragma unroll
        for (int q = 0; q < 4; ++q) { v1[q] = __builtin_amdgcn_alignbit(d1[q], d0[q], 16); v3[q] = __builtin_amdgcn_alignbit(d2[q], d1[q], 16); }
        ldsp U = lds + UB + hch * 4 * CS + 32 + t8 * 16;
        lds_w128(U, d0); lds_w128(U + CS, v1); lds_w128(U + 2 * CS, d1); lds_w128(U + 3 * CS, v3);
        if (t8 < 4) {
            u32x4 z = {0, 0, 0, 0}, y = z;
            if (t8 == 1) y.w = f.x << 16;
            if (t8 == 2) y.w = f.x;
            if (t8 == 3) { y.z = f.x << 16; y.w = __builtin_amdgcn_alignbit(f.y, f.x, 16); }
            ldsp F = lds + UB + (hch * 4 + t8) * CS;
            lds_w128(F, z); lds_w128(F + 16, y);
        }
    }
    bar();
    STAMP(5);
    {
        const int mq = w & 3;
        const int s = (r16 + 1) & 3;
        ldsp A0 = lds + UB + (hch * 4 + s) * CS + 2 * (1 + r16 - s + 8 * g) + mq * 2 * 64;
        ldsp B0 = lds + hch * 4608 + 4064 - 3584 - 32 * r16 + 16 * g + mq * 2 * 64;
        f32x4 acc[8];
#pragma unroll
        for (int i = 0; i < 8; ++i) acc[i] = f32x4{0, 0, 0, 0};
#pragma unroll
        for (int mm = 0; mm < 2; ++mm) {
            u32x4 Af[8], Bf[8];
#pragma unroll
            for (int i = 0; i < 8; ++i) {
                Bf[i] = lds_r128(B0 + mm * 64 + (7 - i) * 512);
                Af[i] = cat(lds_r64(A0 + mm * 64 + i * 512), lds_r64(A0 + mm * 64 + i * 512 + 8));
            }
#pragma unroll
            for (int i = 0; i < 8; ++i)
#pragma unroll
                for (int cc = i; cc < 8; ++cc) acc[cc] = mfma32(Af[i], Bf[cc - i], acc[cc]);
            __builtin_amdgcn_sched_barrier(0);
        }
        FSTAMP(7);
        bar();
        FSTAMP(8);
        ldsp R = lds + hch * 24576 + l * 16;
#pragma unroll
        for (int cc = 0; cc < 8; ++cc)
            if ((cc >> 1) != mq) lds_w128(R + (cc * 3 + ((mq - (cc >> 1) - 1) & 3)) * 1024, __builtin_bit_cast(u32x4, acc[cc]));
        bar();
        FSTAMP(9);
#pragma unroll
        for (int q = 0; q < 4; ++q)
            if (q == mq) {
                u32 pk[2][2];
#pragma unroll
                for (int h = 0; h < 2; ++h) {
                    float v[4];
#pragma unroll
                    for (int r = 0; r < 4; ++r) v[r] = acc[2 * q + h][r];
#pragma unroll
                    for (int sl = 0; sl < 3; ++sl) {
                        u32x4 o = lds_r128(R + ((2 * q + h) * 3 + sl) * 1024);
#pragma unroll
                        for (int r = 0; r < 4; ++r) v[r] += u2f(o[r]);
                    }
                    pk[h][0] = pk2(v[0], v[1]); pk[h][1] = pk2(v[2], v[3]);
                }
                auto sx = __builtin_amdgcn_permlane16_swap(pk[0][0], pk[1][0], false, false);
                auto sy = __builtin_amdgcn_permlane16_swap(pk[0][1], pk[1][1], false, false);
                u32x4 v = {sx[0], sy[0], sx[1], sy[1]};
                st128c(rws, v, WS_YT + (ch0 + hch) * 4096 + (256 * (2 * q + (g & 1)) + 16 * r16 + 4 * (g & 2)) * 2);
            }
    }
    FSTAMP(10);
    publish(ws, WS_FY + xcd * 256 + j * 8, tag);
    STAMP(6);
    return tag;
}

typedef float f32x16 __attribute__((ext_vector_type(16)));
DEV f32x16 mfma32x32(u32x4 a, u32x4 b, f32x16 c) {
    return __builtin_amdgcn_mfma_f32_32x32x16_bf16(__builtin_bit_cast(bf16x8, a), __builtin_bit_cast(bf16x8, b), c, 0, 0, 0);
}
DEV float vmax3(float a, float b, float c) { float r; asm("v_max3_f32 %0, %1, %2, %3" : "=v"(r) : "v"(a), "v"(b), "v"(c)); return r; }
DEV float vmin3(float a, float b, float c) { float r; asm("v_min3_f32 %0, %1, %2, %3" : "=v"(r) : "v"(a), "v"(b), "v"(c)); return r; }

template <bool POS, bool NEG> DEV void feature_map(u32x4 x0, u32x4 x1, u32x4 wown, int l, u32 (&ph)[16]) {
    const bool odd = (l >> 2) & 1;
    u32x4 a0, a1;
#pragma unroll
    for (int q = 0; q < 4; ++q) { a0[q] = odd ? 0u : wown[q]; a1[q] = odd ? wown[q] : 0u; }
    f32x16 pr;
#pragma unroll
    for (int r = 0; r < 16; ++r) pr[r] = 0.f;
    pr = mfma32x32(a0, x0, pr);
    pr = mfma32x32(a1, x1, pr);
    float p[16];
#pragma unroll
    for (int r = 0; r < 16; r += 2) { u32 v = pk2(pr[r], pr[r + 1]); p[r] = blo(v); p[r + 1] = bhi(v); }
    constexpr float LG = 1.4426950408889634f;
#pragma unroll
    for (int sg = 0; sg < 2; ++sg) {
        if (sg == 0 ? !POS : !NEG) continue;
        float m = sg ? vmin3(p[0], p[1], p[2]) : vmax3(p[0], p[1], p[2]);
#pragma unroll
        for (int r = 3; r < 15; r += 2) m = sg ? vmin3(m, p[r], p[r + 1]) : vmax3(m, p[r], p[r + 1]);
        m = sg ? vmin3(m, p[15], p[15]) : vmax3(m, p[15], p[15]);
        const float sc = sg ? -LG : LG, c = -m * sc;
        float a[16];
#pragma unroll
        for (int r = 0; r < 16; ++r) a[r] = __builtin_fmaf(p[r], sc, c);
#pragma unroll
        for (int r = 0; r < 16; ++r) a[r] = __builtin_amdgcn_exp2f(a[r]);
        float sa[4];
#pragma unroll
        for (int q = 0; q < 4; ++q) sa[q] = (a[4 * q] + a[4 * q + 1]) + (a[4 * q + 2] + a[4 * q + 3]);
        const float ia = __builtin_amdgcn_rcpf((sa[0] + sa[1]) + (sa[2] + sa[3]));
#pragma unroll
        for (int r = 0; r < 8; ++r) ph[8 * sg + r] = pk2(a[2 * r] * ia, a[2 * r + 1] * ia);
    }
}

DEV void role_attn(const Params& p, ldsp lds, char* ws, u64 e, int bb, int w, int l STAMP_ARG) {
    const int xcd = bb & 7, j = bb >> 3, g = l >> 4, r16 = l & 15, h = l >> 5, w4 = w & 3;
    const int rb_ = 8 * (xcd >> 2) + (j >> 2), cb = 4 * (xcd & 3) + (j & 3);
    const int rot = (j + xcd) & 7;
    rsrc_t rws = mkrsrc(ws, WS_USED);
#define SCHED() __builtin_amdgcn_sched_barrier(0)
    constexpr int XCH = 32768, WPH = 49152, PH = 51200, PHS = 10240, VV = 71680, VS = 5120, FR = 0, CNT = 8192;
    const int tok = 32 * w4 + (l & 31);
    const u32 wph = WPH + (l & 1) * 512 + (4 * h + (r16 >> 2)) * 32 + (8 * (g & 1) + 4 * ((l & 3) >> 1)) * 2;
    u64 tag;
    if (w >= 4) {
        __builtin_amdgcn_s_setprio(PRIO_BH);
        const u32x4 ru = mkdesc(p.u, L * D * 2), rw = mkdesc(p.wqkv, D * 3 * D * 2);
        const u32 xv = (rb_ * 128 + w4 * 8 + (l >> 3)) * 1024 + (((l & 7) ^ (((w4 & 1) << 2) | ((l >> 4) & 3))) * 16);
        const u32 wv = (16 * w4 + (l >> 2)) * 3072 + cb * 64 + (l & 3) * 16;
        auto issue_x = [&](int s) {
#pragma unroll
            for (int i = 0; i < 4; ++i) dma16(ru, lds + (s % 3) * 16384 + (4 * i + w4) * 1024, xv, ((s + rot) & 7) * 128 + i * 32768);
        };
        auto issue_w = [&](int s, int sec) {
            dma16(rw, lds + 49152 + ((3 * s + sec) & 7) * 4096 + w4 * 1024, wv, ((s + rot) & 7) * (64 * 3072) + sec * 1024);
        };
        FS3(6);
        issue_w(0, 0); issue_w(0, 1); issue_w(0, 2); issue_x(0);
        FS3(7);
        __builtin_amdgcn_s_barrier();
        FS3(8);
        issue_w(1, 0); issue_w(1, 1); issue_w(1, 2); issue_x(1);
        FS3(9);
#pragma unroll
        for (int s = 0; s < 8; ++s) {
            if (s == 0) WAIT_VM(7); else if (s < 7) WAIT_VM(6); else WAIT_VM(0);
            if (s == 0) FS3(10);
            __builtin_amdgcn_s_barrier();
            if (s >= 1 && s + 1 < 8) issue_w(s + 1, 2);
            if (s + 2 < 8) { issue_x(s + 2); issue_w(s + 2, 0); issue_w(s + 2, 1); }
            if (s == 6 && w == 4) dma16(mkdesc(p.wphi, 32 * 256 * 2), lds + WPH, l * 16, cb * 1024);
            if (s == 7 && w == 4 && l < 2) lds_w32(lds + CNT + l * 4, 0u);
        }
        tag = make_tag(e);
        bar();
        ldsp X = lds + XCH + w4 * 4096 + l * 16;
        u32x4 wown = cat(trread(lds + wph), trread(lds + wph + 8 * 32));
        u32x4 q0 = lds_r128(X), q1 = lds_r128(X + 1024);
        bar();
        bar();
        u32 ph[16];
        feature_map<true, true>(q0, q1, wown, l, ph);
        u32x4 bq[2][2];
#pragma unroll
        for (int sg = 0; sg < 2; ++sg)
#pragma unroll
            for (int q = 0; q < 4; ++q) {
                auto sw = __builtin_amdgcn_permlane32_swap(ph[8 * sg + q], ph[8 * sg + q + 4], false, false);
                bq[0][sg][q] = sw[0]; bq[1][sg][q] = sw[1];
            }
        STAMP(3);
        __builtin_amdgcn_s_setprio(PRIO_BH2);
        bar();
#pragma unroll
        for (int hh = 0; hh < 2; ++hh) {
            f32x16 nd;
#pragma unroll
            for (int r = 0; r < 16; ++r) nd[r] = 0.f;
            nd = mfma32x32(lds_r128(lds + FR + (2 * hh) * 1024 + l * 16), bq[hh][0], nd);
            nd = mfma32x32(lds_r128(lds + FR + (2 * hh + 1) * 1024 + l * 16), bq[hh][1], nd);
            const float inv = __builtin_amdgcn_rcpf(rb(rb(nd[8]) + 1e-6f));
            u32x4 o;
#pragma unroll
            for (int q = 0; q < 4; ++q) { u32 n = pk2(nd[2 * q], nd[2 * q + 1]); o[q] = pk2(blo(n) * inv, bhi(n) * inv); }
            st128c(rws, o, WS_OP + (rb_ * 128 + tok) * 1024 + cb * 64 + hh * 32 + h * 16);
        }
        FS2(10);
        publish_counted(ws, WS_FO + rb_ * 256 + cb * 8, tag, lds + CNT + 4, 4, l);
        STAMP(5);
    } else {
        __builtin_amdgcn_s_setprio(PRIO_BC);
        f32x16 acc[3];
#pragma unroll
        for (int sec = 0; sec < 3; ++sec)
#pragma unroll
            for (int r = 0; r < 16; ++r) acc[sec][r] = 0.f;
        const u32 xa = tok * 128 + ((h ^ ((tok >> 1) & 7)) * 16);
        const u32 wa = 49152 + (8 * h + (r16 >> 2)) * 64 + (g & 1) * 32 + 8 * (l & 3);
        __builtin_amdgcn_s_barrier();
#pragma unroll
        for (int s = 0; s < 8; ++s) {
            __builtin_amdgcn_s_barrier();
            asm volatile("" ::: "memory");
            if (s == 0) FS1(6);
            if (s == 1) FS1(7);
            if (s == 2) FS1(8);
            if (s == 4) FS1(9);
            if (s == 6) FS1(10);
            u32x4 xf[4], wf[12];
            auto read_x = [&](int ks) { xf[ks] = lds_r128(lds + (s % 3) * 16384 + (xa ^ (32 * ks))); };
            auto read_w = [&](int i) {
                ldsp a = lds + wa + ((3 * s + i % 3) & 7) * 4096 + (i / 3) * 1024;
                wf[i] = cat(trread(a), trread(a + 256));
            };
            read_x(0); read_w(0); read_w(1); read_w(2);
            SCHED();
#pragma unroll
            for (int i = 0; i < 12; ++i) {
                if (i % 3 == 0 && i / 3 + 1 < 4) read_x(i / 3 + 1);
                if (i + 3 < 12) read_w(i + 3);
                SCHED();
                acc[i % 3] = mfma32x32(wf[i], xf[i / 3], acc[i % 3]);
                SCHED();
            }
        }
        u32x4 fr[3][2];
#pragma unroll
        for (int sec = 0; sec < 3; ++sec)
#pragma unroll
            for (int hh = 0; hh < 2; ++hh)
#pragma unroll
                for (int q = 0; q < 4; ++q) fr[sec][hh][q] = pk2(acc[sec][8 * hh + 2 * q], acc[sec][8 * hh + 2 * q + 1]);
        lds_w128(lds + XCH + w * 4096 + l * 16, fr[0][0]);
        lds_w128(lds + XCH + w * 4096 + 1024 + l * 16, fr[0][1]);
        tag = make_tag(e);
        bar();
        STAMP(1);
#pragma unroll
        for (int hh = 0; hh < 2; ++hh) {
            ldsp q = lds + VV + hh * VS + tok * 40 + 8 * h;
            lds_w64(q, u32x2{fr[2][hh].x, fr[2][hh].y});
            lds_w64(q + 16, u32x2{fr[2][hh].z, fr[2][hh].w});
        }
        {
            u32x4 wown = cat(trread(lds + wph), trread(lds + wph + 8 * 32));
            u32 ph[16];
            feature_map<true, true>(fr[1][0], fr[1][1], wown, l, ph);
            ldsp q = lds + PH + h * PHS + tok * 80;
#pragma unroll
            for (int i = 0; i < 4; ++i) lds_w128(q + i * 16, u32x4{ph[4 * i], ph[4 * i + 1], ph[4 * i + 2], ph[4 * i + 3]});
        }
        FS2(6);
        bar();
        FS2(7);
        const int hh = w >> 1, sg = w & 1;
        {
            f32x4 kv = {0, 0, 0, 0}, z = {0, 0, 0, 0};
            const u32x4 ones = {0x3f803f80u, 0x3f803f80u, 0x3f803f80u, 0x3f803f80u};
            ldsp pa = lds + PH + hh * PHS + (8 * g + (r16 >> 2)) * 80 + sg * 32 + 8 * (l & 3);
            ldsp pb = lds + VV + hh * VS + (8 * g + (r16 >> 2)) * 40 + 8 * (l & 3);
#pragma unroll
            for (int tg = 0; tg < 4; ++tg) {
                u32x4 A = cat(trread(pa + tg * 32 * 80), trread(pa + tg * 32 * 80 + 4 * 80));
                u32x4 B = cat(trread(pb + tg * 32 * 40), trread(pb + tg * 32 * 40 + 4 * 40));
                kv = mfma32(A, B, kv);
                z = mfma32(A, ones, z);
            }
            st128c(rws, __builtin_bit_cast(u32x4, kv), WS_KVP + ((cb * 16 + rb_) * 4 + w) * 1024 + l * 16);
            if (r16 == 0) st128c(rws, __builtin_bit_cast(u32x4, z), WS_ZP + ((cb * 16 + rb_) * 4 + w) * 64 + g * 16);
        }
        FS2(8);
        bar();
        publish_counted(ws, WS_FKV + cb * 256 + rb_ * 8, tag, lds + CNT, 4, l);
        STAMP(2);
        wait_flags(ws, WS_FKV + cb * 256 + r16 * 8, tag);
        STAMP(4);
        u32x4 part[16];
#pragma unroll
        for (int r = 0; r < 16; ++r) part[r] = ld128c(rws, WS_KVP + (cb * 16 * 4 + w) * 1024 + l * 16, r * 4096);
        u32x4 zp = ld128c(rws, WS_ZP + ((cb * 16 + (l >> 2)) * 4 + w) * 64 + (l & 3) * 16);
        float s[4] = {u2f(part[0].x), u2f(part[0].y), u2f(part[0].z), u2f(part[0].w)};
#pragma unroll
        for (int r = 1; r < 16; ++r)
#pragma unroll
            for (int q = 0; q < 4; ++q) s[q] += u2f(part[r][q]);
        float zs[4] = {u2f(zp.x), u2f(zp.y), u2f(zp.z), u2f(zp.w)};
#pragma unroll
        for (int q = 0; q < 4; ++q) {
            float x = zs[q];
            x += dpp_mov<0x124>(x);
            x += dpp_mov<0x128>(x);
            zs[q] = x;
        }
        rows_reduce2<false>(zs[0], zs[1]);
        rows_reduce2<false>(zs[2], zs[3]);
        FS2(9);
        ldsp F = lds + FR + w * 1024;
        lds_w64(F + (r16 + 32 * (g >> 1)) * 16 + 8 * (g & 1), u32x2{pk2(s[0], s[1]), pk2(s[2], s[3])});
        lds_w64(F + (16 + (l >> 2) + 32 * ((l & 3) >> 1)) * 16 + 8 * (l & 1), u32x2{pk2(zs[0], zs[1]), pk2(zs[2], zs[3])});
        bar();
    }
}

DEV void final_stage(const Params& p, ldsp lds, char* ws, u64 tag, int ba, int w, int l STAMP_ARG) {
    const int t = threadIdx.x, g = l >> 4, r16 = l & 15, cw = w & 3, th = w >> 2;
    const int tt64 = 4 * (ba & 7) + ((ba >> 3) & 3), ct = ba >> 5;
    rsrc_t ru = mkrsrc(p.u, L * D * 2), rws = mkrsrc(ws, WS_USED), rout = mkrsrc(p.out, L * D * 2);
    const int tok0 = tt64 * 64, chl = ct * 64 + 16 * cw + 4 * g, tok = tok0 + 32 * th + r16;
    constexpr int YL = 4096;
    bar();
    wo_prefetch<8>(p, lds, ct, w, l);
    u32x2 uu[2];
#pragma unroll
    for (int tt = 0; tt < 2; ++tt) uu[tt] = ld64(ru, (tok + 16 * tt) * 1024 + chl * 2);
    const u32x2 dsk = *(const GAS u32x2*)(p.dsk + chl);
    if (ba == 0 && w >= 4) {
        const int q = t - 256;
        wait_flags(ws, WS_FUT + (q >> 5) * 256 + (q & 31) * 8, tag);
        wait_flags(ws, WS_FKV + (q >> 4) * 256 + (q & 15) * 8, tag);
    }
    if (w == 0) {
        const int tg8 = tt64 * 8 + (l & 7), ga = tg8 >> 7, gc = (tg8 >> 5) & 3, gj = tg8 & 31;
        const int gtb = 16 * ga + (gj >> 1), gcg = 2 * gc + (gj & 1);
        wait_flags(ws, l < 32 ? WS_FY + ct * 256 + l * 8 : WS_FUT + gcg * 256 + gtb * 8, tag);
    }
    WAIT_VM(0);
    bar();
    if (ba == 0 && t == 0) flag_st(ws, WS_E, tag);
    STAMP(8);
    float gate[2];
    {
        u32x4 y = ld128c(rws, WS_YT + (ct * 64 + (t >> 3)) * 4096 + tok0 * 2 + (t & 7) * 16);
#pragma unroll
        for (int tt = 0; tt < 2; ++tt) gate[tt] = u2f(ld32c(rws, WS_G + (tok + 16 * tt) * 4));
        lds_w128(lds + YL + (t >> 3) * 128 + (t & 7) * 16, y);
    }
    u32x4 wf[16];
    {
        const int row = 16 * (g >> 1) + 4 * (g & 1) + (r16 >> 2);
        ldsp q = lds + 16384 + row * 128 + ((cw ^ ((row >> 1) & 3)) * 32) + 8 * (l & 3);
#pragma unroll
        for (int ks = 0; ks < 16; ++ks) wf[ks] = cat(trread(q + ks * 4096), trread(q + ks * 4096 + 1024));
    }
    bar();
    float ga[2][4], g1[2];
#pragma unroll
    for (int tt = 0; tt < 2; ++tt) {
        u32x2 y = trread(lds + YL + (16 * cw + 4 * g + (r16 >> 2)) * 128 + (32 * th + 16 * tt) * 2 + 8 * (l & 3));
        const float gg = gate[tt];
        g1[tt] = rb(1.f - gg);
        u32 t0 = pk2(blo(uu[tt].x) * blo(dsk.x), bhi(uu[tt].x) * bhi(dsk.x)), t1 = pk2(blo(uu[tt].y) * blo(dsk.y), bhi(uu[tt].y) * bhi(dsk.y));
        u32 a0 = pk2(blo(y.x) + blo(t0), bhi(y.x) + bhi(t0)), a1 = pk2(blo(y.y) + blo(t1), bhi(y.y) + bhi(t1));
        u32 ga0 = pk2(gg * blo(a0), gg * bhi(a0)), ga1 = pk2(gg * blo(a1), gg * bhi(a1));
        ga[tt][0] = blo(ga0); ga[tt][1] = bhi(ga0); ga[tt][2] = blo(ga1); ga[tt][3] = bhi(ga1);
        asm volatile("" : "+v"(ga[tt][0]), "+v"(ga[tt][1]), "+v"(ga[tt][2]), "+v"(ga[tt][3]), "+v"(g1[tt]));
    }
    STAMP(9);
    __builtin_amdgcn_s_setprio(PRIO_F);
    if (w == 0) wait_flags(ws, WS_FO + (tok0 >> 7) * 256 + r16 * 8, tag);
    bar();
    STAMP(10);
#pragma unroll
    for (int hf = 0; hf < 2; ++hf) {
        u32x4 ov[4];
#pragma unroll
        for (int i = 0; i < 4; ++i) ov[i] = ld128c(rws, WS_OP + (tok0 + 8 * w + 4 * hf) * 1024 + l * 16, i * 1024);
#pragma unroll
        for (int i = 0; i < 4; ++i) {
            const int tk = 8 * w + 4 * hf + i;
            lds_w128(lds + 16384 + tk * 1024 + ((l ^ (tk & 15)) * 16), ov[i]);
        }
    }
    bar();
    STAMP(11);
    f32x4 acc[2] = {f32x4{0, 0, 0, 0}, f32x4{0, 0, 0, 0}};
    {
        u32 ob[4];
#pragma unroll
        for (int k4 = 0; k4 < 4; ++k4) ob[k4] = 16384 + (32 * th + r16) * 1024 + ((((4 * k4) ^ (r16 & 12)) | (g ^ (r16 & 3))) * 16);
#pragma unroll
        for (int ks = 0; ks < 16; ++ks)
#pragma unroll
            for (int tt = 0; tt < 2; ++tt)
                acc[tt] = mfma32(wf[ks], lds_r128(lds + ob[ks & 3] + (ks >> 2) * 256 + tt * 16384), acc[tt]);
    }
#pragma unroll
    for (int tt = 0; tt < 2; ++tt) {
        u32x2 o = {pk2(__builtin_fmaf(g1[tt], acc[tt][0], ga[tt][0]), __builtin_fmaf(g1[tt], acc[tt][1], ga[tt][1])),
                   pk2(__builtin_fmaf(g1[tt], acc[tt][2], ga[tt][2]), __builtin_fmaf(g1[tt], acc[tt][3], ga[tt][3]))};
        __builtin_amdgcn_raw_buffer_store_b64(o, rout, (tok + 16 * tt) * 1024 + chl * 2, 0, 0);
    }
    STAMP(12);
}

__global__ __launch_bounds__(512, 4) void s4d_hedgehog_mix(Params p) {
    extern __shared__ __attribute__((aligned(16))) char smem[];
    ldsp lds = (ldsp)smem;
    char* ws = p.ws;
    const int b = blockIdx.x, l = threadIdx.x & 63, w = uni(threadIdx.x >> 6);
    const u64 sk_t0 = __builtin_amdgcn_s_memrealtime();
    if (b == ENC_REF && threadIdx.x == 0) __hip_atomic_store((GAS u64*)(p.ws + 128), sk_t0, __ATOMIC_RELAXED, __HIP_MEMORY_SCOPE_AGENT);
    STAMP(0);
    asm volatile("" :: "s"(p.ws), "s"(p.u), "s"(p.wqkv), "s"(p.ldt), "s"(p.gw));
    const u64 e = flag_ld(ws, WS_E);
    if (b >= 256) {
        const u64 tag = role_conv(p, lds, ws, e, b - 256, w, l STAMP_PASS);
        final_stage(p, lds, ws, tag, b - 256, w, l STAMP_PASS);
    } else {
        role_attn(p, lds, ws, e, b, w, l STAMP_PASS);
    }
    if (b == ENC_SKEW && w == 0) {
        const u64 r = __hip_atomic_load((GAS u64*)(p.ws + 128), __ATOMIC_RELAXED, __HIP_MEMORY_SCOPE_AGENT);
        long long d = (long long)(sk_t0 - r) + 200;
        if (d < 0) d = 0;
        if (d > 2000) d = 2000;
        const u64 now = __builtin_amdgcn_s_memrealtime();
        while (__builtin_amdgcn_s_memrealtime() - now < (u64)(8 * d)) __builtin_amdgcn_s_sleep(4);
    }
}

#if defined(__HIP_DEVICE_COMPILE__)
#pragma clang attribute pop
#endif

extern "C" void kernel_launch(void* const* d_in, const int* in_sizes, int n_in, void* d_out, int out_size, void* d_ws, size_t ws_size,
                              hipStream_t stream) {
    Params p;
    p.u = (const u16*)d_in[0]; p.lar = (const u16*)d_in[1]; p.aim = (const u16*)d_in[2]; p.cre = (const u16*)d_in[3];
    p.cim = (const u16*)d_in[4]; p.ldt = (const u16*)d_in[5]; p.dsk = (const u16*)d_in[6]; p.wqkv = (const u16*)d_in[8];
    p.wphi = (const u16*)d_in[9]; p.wo = (const u16*)d_in[10]; p.gw = (const u16*)d_in[11]; p.out = (u16*)d_out; p.ws = (char*)d_ws;
    (void)hipFuncSetAttribute((const void*)s4d_hedgehog_mix, hipFuncAttributeMaxDynamicSharedMemorySize, LDS_BYTES);
    s4d_hedgehog_mix<<<512, 512, LDS_BYTES, stream>>>(p);
}
```

```cpp
#define POST_K 1
#define POST_T 64
#define POST_L 0
#define GAP_POST 1
#include <hip/hip_runtime.h>
#include <hip/hip_bf16.h>
#include <stdint.h>

#if defined(__HIP_DEVICE_COMPILE__)
#pragma clang attribute push(__attribute__((target("no-packed-fp32-ops"))), apply_to = function)
#endif

typedef unsigned short u16;
typedef unsigned u32;
typedef unsigned long long u64;
typedef float f32x2 __attribute__((ext_vector_type(2)));
typedef float f32x4 __attribute__((ext_vector_type(4)));
typedef u32 u32x2 __attribute__((ext_vector_type(2)));
typedef u32 u32x4 __attribute__((ext_vector_type(4)));
typedef short s16x4 __attribute__((ext_vector_type(4)));
typedef __bf16 bf16x2 __attribute__((ext_vector_type(2)));
typedef __bf16 bf16x8 __attribute__((ext_vector_type(8)));
#define LAS __attribute__((address_space(3)))
#define GAS __attribute__((address_space(1)))
typedef LAS char* ldsp;
typedef __amdgpu_buffer_rsrc_t rsrc_t;
#define DEV __device__ __forceinline__

constexpr int L = 2048, D = 512;
constexpr int LDS_BYTES = 81920;
constexpr u64 TAG_STEP = 0x9E3779B97F4A7C15ull;
constexpr u32 SPIN_CAP = 40000;

constexpr u32 WS_E = 0, WS_TMO = 64;
constexpr u32 WS_FUT = 4096, WS_FKV = WS_FUT + 4096, WS_FY = WS_FKV + 4096, WS_FO = WS_FY + 4096;
constexpr u32 WS_G = 0x28000;
constexpr u32 WS_UT = 0x40000;
constexpr u32 WS_YT = WS_UT + 0x200000;
constexpr u32 WS_OP = WS_YT + 0x200000;
constexpr u32 WS_KVP = WS_OP + 0x200000;
constexpr u32 WS_ZP = WS_KVP + 0x100000;
constexpr u32 WS_DBG = WS_ZP + 0x10000;
constexpr u32 WS_USED = WS_DBG + 0x400000;

constexpr int AUX_SC1 = 16;

struct IdxTab { u16 v[2048]; };
constexpr IdxTab make_idx() {
    IdxTab t{};
    for (int i = 0; i < 2048; ++i) {
        int j = 2047 - i, p = j;
        int sh = j >= 1024 ? 3 : (j >= 512 ? 2 : (j >= 256 ? 1 : 0));
        if (sh) {
            int q = j >> sh, rem = j & ((1 << sh) - 1), half = 1 << (sh - 1);
            if (rem > half || (rem == half && (q & 1))) ++q;
            p = q << sh;
        }
        int row = 0, col = (p >> 3) & 15;
        if (p < 256) row = (p & 7) + 8 * (p >> 7);
        else if (p < 512) row = 16 + ((p - 256) >> 7) * 4 + ((p & 7) >> 1);
        else if (p < 1024) row = 24 + ((p - 512) >> 7) * 2 + ((p & 7) >> 2);
        else row = 32 + ((p - 1024) >> 7);
        t.v[i] = (u16)((row * 16 + col) * 2);
    }
    return t;
}
__device__ const IdxTab g_idx = make_idx();

DEV float u2f(u32 x) { return __builtin_bit_cast(float, x); }
DEV u32 f2u(float x) { return __builtin_bit_cast(u32, x); }
DEV float fexp(float x) { return __builtin_amdgcn_exp2f(x * 1.4426950408889634f); }
template <int CTRL> DEV float dpp_mov(float x) { return u2f(__builtin_amdgcn_update_dpp(0u, f2u(x), CTRL, 0xf, 0xf, true)); }
DEV float row16_sum(float x) {
    x += dpp_mov<0xB1>(x);
    x += dpp_mov<0x4E>(x);
    x += dpp_mov<0x141>(x);
    x += dpp_mov<0x140>(x);
    return x;
}
DEV u32 pk2(float lo, float hi) { f32x2 v = {lo, hi}; return __builtin_bit_cast(u32, __builtin_convertvector(v, bf16x2)); }
DEV float blo(u32 x) { return u2f(x << 16); }
DEV float bhi(u32 x) { return u2f(x & 0xffff0000u); }
DEV float rb(float f) { return blo(pk2(f, 0.f)); }
DEV f32x4 mfma32(u32x4 a, u32x4 b, f32x4 c) {
    return __builtin_amdgcn_mfma_f32_16x16x32_bf16(__builtin_bit_cast(bf16x8, a), __builtin_bit_cast(bf16x8, b), c, 0, 0, 0);
}
DEV f32x4 mfma16(u32x2 a, u32x2 b, f32x4 c) {
    return __builtin_amdgcn_mfma_f32_16x16x16bf16_1k(__builtin_bit_cast(s16x4, a), __builtin_bit_cast(s16x4, b), c, 0, 0, 0);
}
DEV u32x2 trread(ldsp p) { return __builtin_bit_cast(u32x2, __builtin_amdgcn_ds_read_tr16_b64_v4i16((LAS s16x4*)p)); }
DEV u32x4 lds_r128(ldsp p) { return *(LAS u32x4*)p; }
DEV u32x2 lds_r64(ldsp p) { return *(volatile LAS u32x2*)p; }
DEV void lds_w128(ldsp p, u32x4 v) { *(LAS u32x4*)p = v; }
DEV void lds_w64(ldsp p, u32x2 v) { *(LAS u32x2*)p = v; }
DEV void lds_w32(ldsp p, u32 v) { *(LAS u32*)p = v; }
DEV u32x4 cat(u32x2 a, u32x2 b) { u32x4 r = {a.x, a.y, b.x, b.y}; return r; }
DEV rsrc_t mkrsrc(const void* p, u32 bytes) { return __builtin_amdgcn_make_buffer_rsrc((void*)p, 0, bytes, 0x00020000); }
DEV u32x4 mkdesc(const void* p, u32 bytes) {
    u64 a = (u64)p;
    u32x4 d = {(u32)a, (u32)(a >> 32) & 0xffffu, bytes, 0x00020000u};
    return d;
}
DEV void dma16(u32x4 desc, ldsp dst, u32 voff, u32 soff) {
    asm volatile("s_mov_b32 m0, %0\n\ts_nop 0\n\tbuffer_load_dwordx4 %1, %2, %3 offen lds"
                 :: "s"((u32)(uintptr_t)dst), "v"(voff), "s"(desc), "s"(soff) : "memory");
}
DEV u32x4 ld128(rsrc_t r, u32 voff, u32 soff = 0) { return __builtin_amdgcn_raw_buffer_load_b128(r, voff, soff, 0); }
DEV u32x2 ld64(rsrc_t r, u32 voff, u32 soff = 0) { return __builtin_amdgcn_raw_buffer_load_b64(r, voff, soff, 0); }
DEV u32 ld16(rsrc_t r, u32 voff, u32 soff = 0) { return __builtin_amdgcn_raw_buffer_load_b16(r, voff, soff, 0); }
DEV u32x4 ld128c(rsrc_t r, u32 voff, u32 soff = 0) { return __builtin_amdgcn_raw_buffer_load_b128(r, voff, soff, AUX_SC1); }
DEV u32x2 ld64c(rsrc_t r, u32 voff, u32 soff = 0) { return __builtin_amdgcn_raw_buffer_load_b64(r, voff, soff, AUX_SC1); }
DEV u32 ld32c(rsrc_t r, u32 voff, u32 soff = 0) { return __builtin_amdgcn_raw_buffer_load_b32(r, voff, soff, AUX_SC1); }
DEV void st128c(rsrc_t r, u32x4 v, u32 voff, u32 soff = 0) { __builtin_amdgcn_raw_buffer_store_b128(v, r, voff, soff, AUX_SC1); }
DEV void st64c(rsrc_t r, u32x2 v, u32 voff, u32 soff = 0) { __builtin_amdgcn_raw_buffer_store_b64(v, r, voff, soff, AUX_SC1); }
#define WAIT_VM(n) asm volatile("s_waitcnt vmcnt(" #n ")" ::: "memory")
#define SCHED_A() __builtin_amdgcn_sched_barrier(0)
#define WAIT_LGKM0() asm volatile("s_waitcnt lgkmcnt(0)" ::: "memory")
DEV void bar() { asm volatile("s_waitcnt lgkmcnt(0)" ::: "memory"); __builtin_amdgcn_s_barrier(); asm volatile("" ::: "memory"); }
#define PRIO_A0 3
#define PRIO_A1 1
#define PRIO_BH2 3
#define PRIO_F 3
#define PRIO_BC 2
#define PRIO_BH 0
DEV u32 uni(u32 x) { return __builtin_amdgcn_readfirstlane(x); }

DEV float swap16_lo(float a, float b, float& other) {
    auto s = __builtin_amdgcn_permlane16_swap(f2u(a), f2u(b), false, false);
    other = u2f(s[1]);
    return u2f(s[0]);
}
DEV float swap32_lo(float a, float b, float& other) {
    auto s = __builtin_amdgcn_permlane32_swap(f2u(a), f2u(b), false, false);
    other = u2f(s[1]);
    return u2f(s[0]);
}
template <bool MAX> DEV void rows_reduce2(float& a, float& b) {
    float o, t = swap16_lo(a, b, o);
    t = MAX ? __builtin_fmaxf(t, o) : t + o;
    float u = swap32_lo(t, t, o);
    u = MAX ? __builtin_fmaxf(u, o) : u + o;
    a = swap16_lo(u, u, b);
}

typedef GAS u64 gu64;
DEV u64 make_tag(u64 e) {
    u32 lo = (u32)e, hi = (u32)(e >> 32);
    asm volatile("" : "+v"(lo), "+v"(hi));
    return (((u64)uni(hi) << 32) | uni(lo)) + TAG_STEP;
}
DEV u64 flag_ld(char* ws, u32 off) { return __hip_atomic_load((gu64*)(ws + off), __ATOMIC_RELAXED, __HIP_MEMORY_SCOPE_AGENT); }
DEV void flag_st(char* ws, u32 off, u64 v) { __hip_atomic_store((gu64*)(ws + off), v, __ATOMIC_RELAXED, __HIP_MEMORY_SCOPE_AGENT); }
DEV void wait_flags(char* ws, u32 off, u64 tag) {
#pragma unroll 1
    for (u32 spins = 0; spins < SPIN_CAP; ++spins) {
        u64 v = flag_ld(ws, off);
        if (__builtin_amdgcn_ballot_w64(v != tag) == 0) { asm volatile("" ::: "memory"); return; }
        __builtin_amdgcn_s_sleep(1);
    }
    flag_st(ws, WS_TMO, tag);
}
DEV void publish(char* ws, u32 off, u64 tag) {
    WAIT_VM(0);
    bar();
    if (threadIdx.x == 0) flag_st(ws, off, tag);
}

DEV void publish_counted(char* ws, u32 off, u64 tag, ldsp cnt, u32 n, int l) {
    WAIT_VM(0);
    u32 old = 0;
    if (l == 0) old = __hip_atomic_fetch_add((LAS u32*)cnt, 1u, __ATOMIC_RELAXED, __HIP_MEMORY_SCOPE_WORKGROUP);
    if (l == 0 && old == n - 1) flag_st(ws, off, tag);
}

#define STAMP(i) asm volatile("; MARK " #i)
#define FSTAMP(i) do { } while (0)
#define FS1(i) do { } while (0)
#define FS2(i) do { } while (0)
#define FS3(i) do { } while (0)
#define FS4(i) do { } while (0)
#define STAMP_ARG
#define STAMP_PASS

struct Params {
    char* ws; const u16* u; const u16* wqkv; const u16* wphi; const u16* lar; const u16* aim; const u16* cre; const u16* cim;
    const u16* ldt; const u16* gw; const u16* dsk; const u16* wo; u16* out;
};

struct cplx { float r, i; };
DEV cplx cmul(cplx a, cplx b) { cplx c; c.r = a.r * b.r - a.i * b.i; c.i = a.r * b.i + a.i * b.r; return c; }
DEV cplx cexp_p(float zr2, float zrev, float p) {
    float e = __builtin_amdgcn_exp2f(zr2 * p);
    float a = __builtin_amdgcn_fractf(zrev * p);
    cplx c; c.r = e * __builtin_amdgcn_cosf(a); c.i = e * __builtin_amdgcn_sinf(a); return c;
}

template <int NW> DEV void wo_prefetch(const Params& p, ldsp lds, int ct, int w, int l) {
    const u32x4 rwo = mkdesc(p.wo, D * D * 2);
    u32 voff = (l >> 3) * 1024 + ct * 128 + ((((l & 7) >> 1) ^ ((l >> 4) & 3)) * 32) + (l & 1) * 16;
#pragma unroll
    for (int i = 0; i < 64 / NW; ++i) {
        int piece = i * NW + w;
        dma16(rwo, lds + 16384 + piece * 1024, voff, piece * 8192);
    }
}

DEV u32 touch_pages(const Params& p, int b, int l) {
    const int k = l & 15;
    const char* a = p.ws + WS_KVP + (b & 255) * 4096;
    if (k == 1) a = p.ws + WS_OP + (size_t)(b & 255) * 8192;
    if (k == 2) a = p.ws + WS_YT + (size_t)(b & 255) * 8192;
    if (k == 3) a = p.ws + WS_UT + (size_t)(b & 255) * 8192;
    if (k == 4) a = p.ws + WS_ZP + (b & 255) * 256;
    if (k == 5) a = p.ws + WS_G;
    if (k == 6) a = (const char*)p.out + (size_t)(b & 255) * 8192;
    if (k == 7) a = (const char*)p.wo + (size_t)(b & 255) * 2048;
    if (k == 8) a = (const char*)p.dsk;
    if (k == 9) a = (const char*)p.wphi;
    if (k == 10) a = p.ws + WS_OP + 0x1ff000;
    if (k == 11) a = p.ws + WS_YT + 0x1ff000;
    if (k == 12) a = p.ws + WS_UT + 0x1ff000;
    if (k == 13) a = (const char*)p.out + 0x1ff000;
    return *(const volatile GAS u32*)a;
}

DEV u64 role_conv(const Params& p, ldsp lds, char* ws, u64 e, int ba, int w, int l STAMP_ARG) {
    const int t = threadIdx.x, xcd = ba & 7, j = ba >> 3, g = l >> 4, r16 = l & 15;
    const int a = xcd >> 2, c = xcd & 3;
    rsrc_t ru = mkrsrc(p.u, L * D * 2), rws = mkrsrc(ws, WS_USED);
    const int ch0 = 2 * (32 * xcd + j);
    __builtin_amdgcn_s_setprio(PRIO_A0);
    const int tb = 16 * a + (j >> 1), cg = 2 * c + (j & 1);
    {
        u32 voff = (tb * 64 + (l >> 3)) * 1024 + cg * 128 + (l & 7) * 16;
        dma16(mkdesc(p.u, L * D * 2), lds + 70656 + w * 1024, voff, w * 8192);
    }
    const int gtok = 1024 * a + 8 * (32 * c + j) + w;
    u32x4 gx = ld128(ru, gtok * 1024 + l * 16);
    u32x4 gwv = *(const GAS u32x4*)(p.gw + l * 8);
    const int kch = (t >> 7) & 1, kn = t & 127;
    const int pidx = (ch0 + kch) * 128 + kn;
    float lar = blo(p.lar[pidx]), aim = blo(p.aim[pidx]), cre = blo(p.cre[pidx]), cim = blo(p.cim[pidx]), ldt = blo(p.ldt[ch0 + kch]);
    u32x4 idx = *(const GAS u32x4*)(g_idx.v + (t & 255) * 8);
    if (w == 7) { u32 x = touch_pages(p, ba, l); asm volatile("" :: "v"(x)); }
    SCHED_A();
    __builtin_amdgcn_s_setprio(PRIO_A1);
    WAIT_VM(0);
    const u64 tag = make_tag(e);
    bar();
    STAMP(1);
    {
        const int cw = w & 3, s = w >> 2;
        ldsp src = lds + 70656 + (32 * s + 8 * g + (r16 >> 2)) * 128 + 32 * cw + 8 * (l & 3);
        u32x4 v = cat(trread(src), trread(src + 4 * 128));
        st128c(rws, v, WS_UT + (cg * 64 + 16 * cw + r16) * 4096 + (tb * 64 + 32 * s + 8 * g) * 2);
    }
    {
        float s0 = 0.f, s1 = 0.f;
#pragma unroll
        for (int q = 0; q < 4; ++q) {
            u32 pr = pk2(blo(gx[q]) * blo(gwv[q]), bhi(gx[q]) * bhi(gwv[q]));
            s0 += blo(pr); s1 += bhi(pr);
        }
        s0 = row16_sum(s0 + s1);
        s1 = s0;
        rows_reduce2<false>(s0, s1);
        float g0 = rb(__builtin_amdgcn_rcpf(1.f + fexp(-rb(s0))));
        if (l == 0) __builtin_amdgcn_raw_buffer_store_b32(f2u(g0), rws, WS_G + gtok * 4, 0, AUX_SC1);
    }
    STAMP(2);
    {
        const float dt = rb(fexp(ldt));
        const float zr2 = -rb(fexp(lar)) * dt * 1.4426950408889634f, zrev = aim * dt * 0.15915494309189535f;
        ldsp T = lds + kch * 33792 + kn * 4;
        auto put = [&](int row, cplx v) { lds_w32(T + row * 528, pk2(v.r, v.i)); };
        const cplx w128 = cexp_p(zr2, zrev, 128.f);
        cplx G; G.r = 2.f * cre * dt; G.i = 2.f * cim * dt;
        if (w < 4) {
            cplx wp[8];
            wp[1] = cexp_p(zr2, zrev, 1.f); wp[2] = cmul(wp[1], wp[1]); wp[3] = cmul(wp[2], wp[1]); wp[4] = cmul(wp[2], wp[2]);
            wp[5] = cmul(wp[4], wp[1]); wp[6] = cmul(wp[4], wp[2]); wp[7] = cmul(wp[4], wp[3]);
#pragma unroll
            for (int aa = 0; aa < 4; ++aa) {
                if (aa == 1) { FS4(7); publish(ws, WS_FUT + cg * 256 + tb * 8, tag); FS4(8); }
                if (aa < 2) {
                    put(8 * aa, G);
#pragma unroll
                    for (int cc = 1; cc < 8; ++cc) put(8 * aa + cc, cmul(G, wp[cc]));
                } else {
                    put(16 + 4 * (aa - 2), G);
#pragma unroll
                    for (int cc = 1; cc < 4; ++cc) put(16 + 4 * (aa - 2) + cc, cmul(G, wp[2 * cc]));
                }
                if (aa < 3) G = cmul(G, w128);
            }
        } else {
            const cplx w4 = cexp_p(zr2, zrev, 4.f);
            G = cmul(G, cexp_p(zr2, zrev, 512.f));
#pragma unroll
            for (int aa = 4; aa <= 16; ++aa) {
                if (aa == 10) { FS4(7); publish(ws, WS_FUT + cg * 256 + tb * 8, tag); FS4(8); }
                if (aa < 8) {
                    put(24 + 2 * (aa - 4), G);
                    put(24 + 2 * (aa - 4) + 1, cmul(G, w4));
                } else put(32 + aa - 8, G);
                if (aa < 16) G = cmul(G, w128);
            }
            cplx B[9];
            B[1] = cmul(w4, w4); B[2] = cmul(B[1], B[1]); B[3] = cmul(B[2], B[1]); B[4] = cmul(B[2], B[2]);
            B[5] = cmul(B[4], B[1]); B[6] = cmul(B[4], B[2]); B[7] = cmul(B[4], B[3]); B[8] = cmul(B[4], B[4]);
            lds_w32(T + 48 * 528, 0x00003f80u);
#pragma unroll
            for (int b = 1; b < 16; ++b) {
                cplx v = b <= 8 ? B[b] : cmul(B[8], B[b - 8]);
                lds_w32(T + (48 + b) * 528, pk2(v.r, -v.i));
            }
        }
    }
    FS4(9);
    bar();
    FS4(10);
    if (w == 7) wait_flags(ws, WS_FUT + xcd * 256 + (l & 31) * 8, tag);
    if (w < 6) {
        const int ch = w >= 3, tile = w - 3 * ch;
        ldsp T = lds + ch * 33792 + r16 * 528 + g * 16;
        f32x4 k0 = {0, 0, 0, 0};
#pragma unroll
        for (int s = 0; s < 8; ++s) k0 = mfma32(lds_r128(T + tile * 16 * 528 + s * 64), lds_r128(T + 48 * 528 + s * 64), k0);
        ldsp KC = lds + 67584 + ch * 1536 + r16 * 2 + (tile * 16 + 4 * g) * 32;
        u32 a0 = pk2(k0[0], k0[1]), a1 = pk2(k0[2], k0[3]);
        *(LAS u16*)(KC) = (u16)a0; *(LAS u16*)(KC + 32) = (u16)(a0 >> 16);
        *(LAS u16*)(KC + 64) = (u16)a1; *(LAS u16*)(KC + 96) = (u16)(a1 >> 16);
    }
    bar();
    STAMP(3);
    const int hch = w >> 2, t8 = t & 255;
    const u32 uoff = WS_UT + (ch0 + hch) * 4096 + t8 * 16;
    u32x4 d0 = ld128c(rws, uoff), d1 = ld128c(rws, uoff + 4), d2 = ld128c(rws, uoff + 8);
    u32x2 f = ld64c(rws, WS_UT + (ch0 + hch) * 4096);
    SCHED_A();
    {
        ldsp KC = lds + 67584 + hch * 1536;
        u32x4 v;
#pragma unroll
        for (int q = 0; q < 4; ++q) {
            u32 lo = *(LAS u16*)(KC + (idx[q] & 0xffffu)), hi = *(LAS u16*)(KC + (idx[q] >> 16));
            v[q] = lo | (hi << 16);
        }
        lds_w128(lds + hch * 4608 + t8 * 16, v);
        if (t8 < 32) { u32x4 z = {0, 0, 0, 0}; lds_w128(lds + hch * 4608 + 4096 + t8 * 16, z); }
    }
    SCHED_A();
    STAMP(4);
    constexpr int UB = 9216, CS = 4160;
    {
        u32x4 v1, v3;
#pragma unroll
        for (int q = 0; q < 4; ++q) { v1[q] = __builtin_amdgcn_alignbit(d1[q], d0[q], 16); v3[q] = __builtin_amdgcn_alignbit(d2[q], d1[q], 16); }
        ldsp U = lds + UB + hch * 4 * CS + 32 + t8 * 16;
        lds_w128(U, d0); lds_w128(U + CS, v1); lds_w128(U + 2 * CS, d1); lds_w128(U + 3 * CS, v3);
        if (t8 < 4) {
            u32x4 z = {0, 0, 0, 0}, y = z;
            if (t8 == 1) y.w = f.x << 16;
            if (t8 == 2) y.w = f.x;
            if (t8 == 3) { y.z = f.x << 16; y.w = __builtin_amdgcn_alignbit(f.y, f.x, 16); }
            ldsp F = lds + UB + (hch * 4 + t8) * CS;
            lds_w128(F, z); lds_w128(F + 16, y);
        }
    }
    bar();
    STAMP(5);
    {
        const int mq = w & 3;
        const int s = (r16 + 1) & 3;
        ldsp A0 = lds + UB + (hch * 4 + s) * CS + 2 * (1 + r16 - s + 8 * g) + mq * 2 * 64;
        ldsp B0 = lds + hch * 4608 + 4064 - 3584 - 32 * r16 + 16 * g + mq * 2 * 64;
        f32x4 acc[8];
#pragma unroll
        for (int i = 0; i < 8; ++i) acc[i] = f32x4{0, 0, 0, 0};
#pragma unroll
        for (int mm = 0; mm < 2; ++mm) {
            u32x4 Af[8], Bf[8];
#pragma unroll
            for (int i = 0; i < 8; ++i) {
                Bf[i] = lds_r128(B0 + mm * 64 + (7 - i) * 512);
                Af[i] = cat(lds_r64(A0 + mm * 64 + i * 512), lds_r64(A0 + mm * 64 + i * 512 + 8));
            }
#pragma unroll
            for (int i = 0; i < 8; ++i)
#pragma unroll
                for (int cc = i; cc < 8; ++cc) acc[cc] = mfma32(Af[i], Bf[cc - i], acc[cc]);
            __builtin_amdgcn_sched_barrier(0);
        }
        FSTAMP(7);
        bar();
        FSTAMP(8);
        ldsp R = lds + hch * 24576 + l * 16;
#pragma unroll
        for (int cc = 0; cc < 8; ++cc)
            if ((cc >> 1) != mq) lds_w128(R + (cc * 3 + ((mq - (cc >> 1) - 1) & 3)) * 1024, __builtin_bit_cast(u32x4, acc[cc]));
        bar();
        FSTAMP(9);
#pragma unroll
        for (int q = 0; q < 4; ++q)
            if (q == mq) {
                u32 pk[2][2];
#pragma unroll
                for (int h = 0; h < 2; ++h) {
                    float v[4];
#pragma unroll
                    for (int r = 0; r < 4; ++r) v[r] = acc[2 * q + h][r];
#pragma unroll
                    for (int sl = 0; sl < 3; ++sl) {
                        u32x4 o = lds_r128(R + ((2 * q + h) * 3 + sl) * 1024);
#pragma unroll
                        for (int r = 0; r < 4; ++r) v[r] += u2f(o[r]);
                    }
                    pk[h][0] = pk2(v[0], v[1]); pk[h][1] = pk2(v[2], v[3]);
                }
                auto sx = __builtin_amdgcn_permlane16_swap(pk[0][0], pk[1][0], false, false);
                auto sy = __builtin_amdgcn_permlane16_swap(pk[0][1], pk[1][1], false, false);
                u32x4 v = {sx[0], sy[0], sx[1], sy[1]};
                st128c(rws, v, WS_YT + (ch0 + hch) * 4096 + (256 * (2 * q + (g & 1)) + 16 * r16 + 4 * (g & 2)) * 2);
            }
    }
    FSTAMP(10);
    publish(ws, WS_FY + xcd * 256 + j * 8, tag);
    STAMP(6);
    return tag;
}

typedef float f32x16 __attribute__((ext_vector_type(16)));
DEV f32x16 mfma32x32(u32x4 a, u32x4 b, f32x16 c) {
    return __builtin_amdgcn_mfma_f32_32x32x16_bf16(__builtin_bit_cast(bf16x8, a), __builtin_bit_cast(bf16x8, b), c, 0, 0, 0);
}
DEV float vmax3(float a, float b, float c) { float r; asm("v_max3_f32 %0, %1, %2, %3" : "=v"(r) : "v"(a), "v"(b), "v"(c)); return r; }
DEV float vmin3(float a, float b, float c) { float r; asm("v_min3_f32 %0, %1, %2, %3" : "=v"(r) : "v"(a), "v"(b), "v"(c)); return r; }

template <bool POS, bool NEG> DEV void feature_map(u32x4 x0, u32x4 x1, u32x4 wown, int l, u32 (&ph)[16]) {
    const bool odd = (l >> 2) & 1;
    u32x4 a0, a1;
#pragma unroll
    for (int q = 0; q < 4; ++q) { a0[q] = odd ? 0u : wown[q]; a1[q] = odd ? wown[q] : 0u; }
    f32x16 pr;
#pragma unroll
    for (int r = 0; r < 16; ++r) pr[r] = 0.f;
    pr = mfma32x32(a0, x0, pr);
    pr = mfma32x32(a1, x1, pr);
    float p[16];
#pragma unroll
    for (int r = 0; r < 16; r += 2) { u32 v = pk2(pr[r], pr[r + 1]); p[r] = blo(v); p[r + 1] = bhi(v); }
    constexpr float LG = 1.4426950408889634f;
#pragma unroll
    for (int sg = 0; sg < 2; ++sg) {
        if (sg == 0 ? !POS : !NEG) continue;
        float m = sg ? vmin3(p[0], p[1], p[2]) : vmax3(p[0], p[1], p[2]);
#pragma unroll
        for (int r = 3; r < 15; r += 2) m = sg ? vmin3(m, p[r], p[r + 1]) : vmax3(m, p[r], p[r + 1]);
        m = sg ? vmin3(m, p[15], p[15]) : vmax3(m, p[15], p[15]);
        const float sc = sg ? -LG : LG, c = -m * sc;
        float a[16];
#pragma unroll
        for (int r = 0; r < 16; ++r) a[r] = __builtin_fmaf(p[r], sc, c);
#pragma unroll
        for (int r = 0; r < 16; ++r) a[r] = __builtin_amdgcn_exp2f(a[r]);
        float sa[4];
#pragma unroll
        for (int q = 0; q < 4; ++q) sa[q] = (a[4 * q] + a[4 * q + 1]) + (a[4 * q + 2] + a[4 * q + 3]);
        const float ia = __builtin_amdgcn_rcpf((sa[0] + sa[1]) + (sa[2] + sa[3]));
#pragma unroll
        for (int r = 0; r < 8; ++r) ph[8 * sg + r] = pk2(a[2 * r] * ia, a[2 * r + 1] * ia);
    }
}

DEV void role_attn(const Params& p, ldsp lds, char* ws, u64 e, int bb, int w, int l STAMP_ARG) {
    const int xcd = bb & 7, j = bb >> 3, g = l >> 4, r16 = l & 15, h = l >> 5, w4 = w & 3;
    const int rb_ = 8 * (xcd >> 2) + (j >> 2), cb = 4 * (xcd & 3) + (j & 3);
    const int rot = (j + xcd) & 7;
    rsrc_t rws = mkrsrc(ws, WS_USED);
#define SCHED() __builtin_amdgcn_sched_barrier(0)
    constexpr int XCH = 32768, WPH = 49152, PH = 51200, PHS = 10240, VV = 71680, VS = 5120, FR = 0, CNT = 8192;
    const int tok = 32 * w4 + (l & 31);
    const u32 wph = WPH + (l & 1) * 512 + (4 * h + (r16 >> 2)) * 32 + (8 * (g & 1) + 4 * ((l & 3) >> 1)) * 2;
    u64 tag;
    if (w >= 4) {
        __builtin_amdgcn_s_setprio(PRIO_BH);
        const u32x4 ru = mkdesc(p.u, L * D * 2), rw = mkdesc(p.wqkv, D * 3 * D * 2);
        const u32 xv = (rb_ * 128 + w4 * 8 + (l >> 3)) * 1024 + (((l & 7) ^ (((w4 & 1) << 2) | ((l >> 4) & 3))) * 16);
        const u32 wv = (16 * w4 + (l >> 2)) * 3072 + cb * 64 + (l & 3) * 16;
        auto issue_x = [&](int s) {
#pragma unroll
            for (int i = 0; i < 4; ++i) dma16(ru, lds + (s % 3) * 16384 + (4 * i + w4) * 1024, xv, ((s + rot) & 7) * 128 + i * 32768);
        };
        auto issue_w = [&](int s, int sec) {
            dma16(rw, lds + 49152 + ((3 * s + sec) & 7) * 4096 + w4 * 1024, wv, ((s + rot) & 7) * (64 * 3072) + sec * 1024);
        };
        FS3(6);
        issue_w(0, 0); issue_w(0, 1); issue_w(0, 2); issue_x(0);
        FS3(7);
        __builtin_amdgcn_s_barrier();
        FS3(8);
        issue_w(1, 0); issue_w(1, 1); issue_w(1, 2); issue_x(1);
        FS3(9);
#pragma unroll
        for (int s = 0; s < 8; ++s) {
            if (s == 0) WAIT_VM(7); else if (s < 7) WAIT_VM(6); else WAIT_VM(0);
            if (s == 0) FS3(10);
            __builtin_amdgcn_s_barrier();
            if (s >= 1 && s + 1 < 8) issue_w(s + 1, 2);
            if (s + 2 < 8) { issue_x(s + 2); issue_w(s + 2, 0); issue_w(s + 2, 1); }
            if (s == 6 && w == 4) dma16(mkdesc(p.wphi, 32 * 256 * 2), lds + WPH, l * 16, cb * 1024);
            if (s == 7 && w == 4 && l < 2) lds_w32(lds + CNT + l * 4, 0u);
        }
        tag = make_tag(e);
        bar();
        ldsp X = lds + XCH + w4 * 4096 + l * 16;
        u32x4 wown = cat(trread(lds + wph), trread(lds + wph + 8 * 32));
        u32x4 q0 = lds_r128(X), q1 = lds_r128(X + 1024);
        bar();
        bar();
        u32 ph[16];
        feature_map<true, true>(q0, q1, wown, l, ph);
        u32x4 bq[2][2];
#pragma unroll
        for (int sg = 0; sg < 2; ++sg)
#pragma unroll
            for (int q = 0; q < 4; ++q) {
                auto sw = __builtin_amdgcn_permlane32_swap(ph[8 * sg + q], ph[8 * sg + q + 4], false, false);
                bq[0][sg][q] = sw[0]; bq[1][sg][q] = sw[1];
            }
        STAMP(3);
        __builtin_amdgcn_s_setprio(PRIO_BH2);
        bar();
#pragma unroll
        for (int hh = 0; hh < 2; ++hh) {
            f32x16 nd;
#pragma unroll
            for (int r = 0; r < 16; ++r) nd[r] = 0.f;
            nd = mfma32x32(lds_r128(lds + FR + (2 * hh) * 1024 + l * 16), bq[hh][0], nd);
            nd = mfma32x32(lds_r128(lds + FR + (2 * hh + 1) * 1024 + l * 16), bq[hh][1], nd);
            const float inv = __builtin_amdgcn_rcpf(rb(rb(nd[8]) + 1e-6f));
            u32x4 o;
#pragma unroll
            for (int q = 0; q < 4; ++q) { u32 n = pk2(nd[2 * q], nd[2 * q + 1]); o[q] = pk2(blo(n) * inv, bhi(n) * inv); }
            st128c(rws, o, WS_OP + (rb_ * 128 + tok) * 1024 + cb * 64 + hh * 32 + h * 16);
        }
        FS2(10);
        publish_counted(ws, WS_FO + rb_ * 256 + cb * 8, tag, lds + CNT + 4, 4, l);
        STAMP(5);
    } else {
        __builtin_amdgcn_s_setprio(PRIO_BC);
        f32x16 acc[3];
#pragma unroll
        for (int sec = 0; sec < 3; ++sec)
#pragma unroll
            for (int r = 0; r < 16; ++r) acc[sec][r] = 0.f;
        const u32 xa = tok * 128 + ((h ^ ((tok >> 1) & 7)) * 16);
        const u32 wa = 49152 + (8 * h + (r16 >> 2)) * 64 + (g & 1) * 32 + 8 * (l & 3);
        __builtin_amdgcn_s_barrier();
#pragma unroll
        for (int s = 0; s < 8; ++s) {
            __builtin_amdgcn_s_barrier();
            asm volatile("" ::: "memory");
            if (s == 0) FS1(6);
            if (s == 1) FS1(7);
            if (s == 2) FS1(8);
            if (s == 4) FS1(9);
            if (s == 6) FS1(10);
            u32x4 xf[4], wf[12];
            auto read_x = [&](int ks) { xf[ks] = lds_r128(lds + (s % 3) * 16384 + (xa ^ (32 * ks))); };
            auto read_w = [&](int i) {
                ldsp a = lds + wa + ((3 * s + i % 3) & 7) * 4096 + (i / 3) * 1024;
                wf[i] = cat(trread(a), trread(a + 256));
            };
            read_x(0); read_w(0); read_w(1); read_w(2);
            SCHED();
#pragma unroll
            for (int i = 0; i < 12; ++i) {
                if (i % 3 == 0 && i / 3 + 1 < 4) read_x(i / 3 + 1);
                if (i + 3 < 12) read_w(i + 3);
                SCHED();
                acc[i % 3] = mfma32x32(wf[i], xf[i / 3], acc[i % 3]);
                SCHED();
            }
        }
        u32x4 fr[3][2];
#pragma unroll
        for (int sec = 0; sec < 3; ++sec)
#pragma unroll
            for (int hh = 0; hh < 2; ++hh)
#pragma unroll
                for (int q = 0; q < 4; ++q) fr[sec][hh][q] = pk2(acc[sec][8 * hh + 2 * q], acc[sec][8 * hh + 2 * q + 1]);
        lds_w128(lds + XCH + w * 4096 + l * 16, fr[0][0]);
        lds_w128(lds + XCH + w * 4096 + 1024 + l * 16, fr[0][1]);
        tag = make_tag(e);
        bar();
        STAMP(1);
#pragma unroll
        for (int hh = 0; hh < 2; ++hh) {
            ldsp q = lds + VV + hh * VS + tok * 40 + 8 * h;
            lds_w64(q, u32x2{fr[2][hh].x, fr[2][hh].y});
            lds_w64(q + 16, u32x2{fr[2][hh].z, fr[2][hh].w});
        }
        {
            u32x4 wown = cat(trread(lds + wph), trread(lds + wph + 8 * 32));
            u32 ph[16];
            feature_map<true, true>(fr[1][0], fr[1][1], wown, l, ph);
            ldsp q = lds + PH + h * PHS + tok * 80;
#pragma unroll
            for (int i = 0; i < 4; ++i) lds_w128(q + i * 16, u32x4{ph[4 * i], ph[4 * i + 1], ph[4 * i + 2], ph[4 * i + 3]});
        }
        FS2(6);
        bar();
        FS2(7);
        const int hh = w >> 1, sg = w & 1;
        {
            f32x4 kv = {0, 0, 0, 0}, z = {0, 0, 0, 0};
            const u32x4 ones = {0x3f803f80u, 0x3f803f80u, 0x3f803f80u, 0x3f803f80u};
            ldsp pa = lds + PH + hh * PHS + (8 * g + (r16 >> 2)) * 80 + sg * 32 + 8 * (l & 3);
            ldsp pb = lds + VV + hh * VS + (8 * g + (r16 >> 2)) * 40 + 8 * (l & 3);
#pragma unroll
            for (int tg = 0; tg < 4; ++tg) {
                u32x4 A = cat(trread(pa + tg * 32 * 80), trread(pa + tg * 32 * 80 + 4 * 80));
                u32x4 B = cat(trread(pb + tg * 32 * 40), trread(pb + tg * 32 * 40 + 4 * 40));
                kv = mfma32(A, B, kv);
                z = mfma32(A, ones, z);
            }
            st128c(rws, __builtin_bit_cast(u32x4, kv), WS_KVP + ((cb * 16 + rb_) * 4 + w) * 1024 + l * 16);
            if (r16 == 0) st128c(rws, __builtin_bit_cast(u32x4, z), WS_ZP + ((cb * 16 + rb_) * 4 + w) * 64 + g * 16);
        }
        FS2(8);
        bar();
        publish_counted(ws, WS_FKV + cb * 256 + rb_ * 8, tag, lds + CNT, 4, l);
        STAMP(2);
        wait_flags(ws, WS_FKV + cb * 256 + r16 * 8, tag);
        STAMP(4);
        u32x4 part[16];
#pragma unroll
        for (int r = 0; r < 16; ++r) part[r] = ld128c(rws, WS_KVP + (cb * 16 * 4 + w) * 1024 + l * 16, r * 4096);
        u32x4 zp = ld128c(rws, WS_ZP + ((cb * 16 + (l >> 2)) * 4 + w) * 64 + (l & 3) * 16);
        float s[4] = {u2f(part[0].x), u2f(part[0].y), u2f(part[0].z), u2f(part[0].w)};
#pragma unroll
        for (int r = 1; r < 16; ++r)
#pragma unroll
            for (int q = 0; q < 4; ++q) s[q] += u2f(part[r][q]);
        float zs[4] = {u2f(zp.x), u2f(zp.y), u2f(zp.z), u2f(zp.w)};
#pragma unroll
        for (int q = 0; q < 4; ++q) {
            float x = zs[q];
            x += dpp_mov<0x124>(x);
            x += dpp_mov<0x128>(x);
            zs[q] = x;
        }
        rows_reduce2<false>(zs[0], zs[1]);
        rows_reduce2<false>(zs[2], zs[3]);
        FS2(9);
        ldsp F = lds + FR + w * 1024;
        lds_w64(F + (r16 + 32 * (g >> 1)) * 16 + 8 * (g & 1), u32x2{pk2(s[0], s[1]), pk2(s[2], s[3])});
        lds_w64(F + (16 + (l >> 2) + 32 * ((l & 3) >> 1)) * 16 + 8 * (l & 1), u32x2{pk2(zs[0], zs[1]), pk2(zs[2], zs[3])});
        bar();
    }
}

DEV void final_stage(const Params& p, ldsp lds, char* ws, u64 tag, int ba, int w, int l STAMP_ARG) {
    const int t = threadIdx.x, g = l >> 4, r16 = l & 15, cw = w & 3, th = w >> 2;
    const int tt64 = 4 * (ba & 7) + ((ba >> 3) & 3), ct = ba >> 5;
    rsrc_t ru = mkrsrc(p.u, L * D * 2), rws = mkrsrc(ws, WS_USED), rout = mkrsrc(p.out, L * D * 2);
    const int tok0 = tt64 * 64, chl = ct * 64 + 16 * cw + 4 * g, tok = tok0 + 32 * th + r16;
    constexpr int YL = 4096;
    bar();
    wo_prefetch<8>(p, lds, ct, w, l);
    u32x2 uu[2];
#pragma unroll
    for (int tt = 0; tt < 2; ++tt) uu[tt] = ld64(ru, (tok + 16 * tt) * 1024 + chl * 2);
    const u32x2 dsk = *(const GAS u32x2*)(p.dsk + chl);
    if (ba == 0 && w >= 4) {
        const int q = t - 256;
        wait_flags(ws, WS_FUT + (q >> 5) * 256 + (q & 31) * 8, tag);
        wait_flags(ws, WS_FKV + (q >> 4) * 256 + (q & 15) * 8, tag);
    }
    if (w == 0) {
        const int tg8 = tt64 * 8 + (l & 7), ga = tg8 >> 7, gc = (tg8 >> 5) & 3, gj = tg8 & 31;
        const int gtb = 16 * ga + (gj >> 1), gcg = 2 * gc + (gj & 1);
        wait_flags(ws, l < 32 ? WS_FY + ct * 256 + l * 8 : WS_FUT + gcg * 256 + gtb * 8, tag);
    }
    WAIT_VM(0);
    bar();
    if (ba == 0 && t == 0) flag_st(ws, WS_E, tag);
    STAMP(8);
    float gate[2];
    {
        u32x4 y = ld128c(rws, WS_YT + (ct * 64 + (t >> 3)) * 4096 + tok0 * 2 + (t & 7) * 16);
#pragma unroll
        for (int tt = 0; tt < 2; ++tt) gate[tt] = u2f(ld32c(rws, WS_G + (tok + 16 * tt) * 4));
        lds_w128(lds + YL + (t >> 3) * 128 + (t & 7) * 16, y);
    }
    u32x4 wf[16];
    {
        const int row = 16 * (g >> 1) + 4 * (g & 1) + (r16 >> 2);
        ldsp q = lds + 16384 + row * 128 + ((cw ^ ((row >> 1) & 3)) * 32) + 8 * (l & 3);
#pragma unroll
        for (int ks = 0; ks < 16; ++ks) wf[ks] = cat(trread(q + ks * 4096), trread(q + ks * 4096 + 1024));
    }
    bar();
    float ga[2][4], g1[2];
#pragma unroll
    for (int tt = 0; tt < 2; ++tt) {
        u32x2 y = trread(lds + YL + (16 * cw + 4 * g + (r16 >> 2)) * 128 + (32 * th + 16 * tt) * 2 + 8 * (l & 3));
        const float gg = gate[tt];
        g1[tt] = rb(1.f - gg);
        u32 t0 = pk2(blo(uu[tt].x) * blo(dsk.x), bhi(uu[tt].x) * bhi(dsk.x)), t1 = pk2(blo(uu[tt].y) * blo(dsk.y), bhi(uu[tt].y) * bhi(dsk.y));
        u32 a0 = pk2(blo(y.x) + blo(t0), bhi(y.x) + bhi(t0)), a1 = pk2(blo(y.y) + blo(t1), bhi(y.y) + bhi(t1));
        u32 ga0 = pk2(gg * blo(a0), gg * bhi(a0)), ga1 = pk2(gg * blo(a1), gg * bhi(a1));
        ga[tt][0] = blo(ga0); ga[tt][1] = bhi(ga0); ga[tt][2] = blo(ga1); ga[tt][3] = bhi(ga1);
        asm volatile("" : "+v"(ga[tt][0]), "+v"(ga[tt][1]), "+v"(ga[tt][2]), "+v"(ga[tt][3]), "+v"(g1[tt]));
    }
    STAMP(9);
    __builtin_amdgcn_s_setprio(PRIO_F);
    if (w == 0) wait_flags(ws, WS_FO + (tok0 >> 7) * 256 + r16 * 8, tag);
    bar();
    STAMP(10);
#pragma unroll
    for (int hf = 0; hf < 2; ++hf) {
        u32x4 ov[4];
#pragma unroll
        for (int i = 0; i < 4; ++i) ov[i] = ld128c(rws, WS_OP + (tok0 + 8 * w + 4 * hf) * 1024 + l * 16, i * 1024);
#pragma unroll
        for (int i = 0; i < 4; ++i) {
            const int tk = 8 * w + 4 * hf + i;
            lds_w128(lds + 16384 + tk * 1024 + ((l ^ (tk & 15)) * 16), ov[i]);
        }
    }
    bar();
    STAMP(11);
    f32x4 acc[2] = {f32x4{0, 0, 0, 0}, f32x4{0, 0, 0, 0}};
    {
        u32 ob[4];
#pragma unroll
        for (int k4 = 0; k4 < 4; ++k4) ob[k4] = 16384 + (32 * th + r16) * 1024 + ((((4 * k4) ^ (r16 & 12)) | (g ^ (r16 & 3))) * 16);
#pragma unroll
        for (int ks = 0; ks < 16; ++ks)
#pragma unroll
            for (int tt = 0; tt < 2; ++tt)
                acc[tt] = mfma32(wf[ks], lds_r128(lds + ob[ks & 3] + (ks >> 2) * 256 + tt * 16384), acc[tt]);
    }
#pragma unroll
    for (int tt = 0; tt < 2; ++tt) {
        u32x2 o = {pk2(__builtin_fmaf(g1[tt], acc[tt][0], ga[tt][0]), __builtin_fmaf(g1[tt], acc[tt][1], ga[tt][1])),
                   pk2(__builtin_fmaf(g1[tt], acc[tt][2], ga[tt][2]), __builtin_fmaf(g1[tt], acc[tt][3], ga[tt][3]))};
        __builtin_amdgcn_raw_buffer_store_b64(o, rout, (tok + 16 * tt) * 1024 + chl * 2, 0, 0);
    }
    STAMP(12);
}

__global__ __launch_bounds__(512, 4) void s4d_hedgehog_mix(Params p) {
    extern __shared__ __attribute__((aligned(16))) char smem[];
    ldsp lds = (ldsp)smem;
    char* ws = p.ws;
    const int b = blockIdx.x, l = threadIdx.x & 63, w = uni(threadIdx.x >> 6);
    STAMP(0);
    asm volatile("" :: "s"(p.ws), "s"(p.u), "s"(p.wqkv), "s"(p.ldt), "s"(p.gw));
    const u64 e = flag_ld(ws, WS_E);
    if (b >= 256) {
        const u64 tag = role_conv(p, lds, ws, e, b - 256, w, l STAMP_PASS);
        final_stage(p, lds, ws, tag, b - 256, w, l STAMP_PASS);
    } else {
        role_attn(p, lds, ws, e, b, w, l STAMP_PASS);
    }
    if (b >= 256 && threadIdx.x == 0) __hip_atomic_store((GAS u64*)(p.ws + 0x3200000) + (b - 256), (u64)__builtin_amdgcn_s_memrealtime(), __ATOMIC_RELAXED, __HIP_MEMORY_SCOPE_AGENT);
}

__global__ void k_tiny(char* ws) {
    const u64 now = __builtin_amdgcn_s_memrealtime();
    u64 mx = 0;
    for (int i = threadIdx.x; i < 256; i += 64) { u64 r = __hip_atomic_load((GAS u64*)(ws + 0x3200000) + i, __ATOMIC_RELAXED, __HIP_MEMORY_SCOPE_AGENT); mx = r > mx ? r : mx; }
    for (int o = 32; o; o >>= 1) { u64 other = ((u64)__builtin_amdgcn_ds_bpermute(((threadIdx.x ^ o) << 2), (int)(mx >> 32)) << 32) | (u32)__builtin_amdgcn_ds_bpermute(((threadIdx.x ^ o) << 2), (int)(u32)mx); mx = other > mx ? other : mx; }
    long long d = (long long)(now - mx);
    if (d < 0) d = 0;
    if (d > 3000) d = 3000;
    while (__builtin_amdgcn_s_memrealtime() - now < (u64)(8 * d)) __builtin_amdgcn_s_sleep(4);
}
#if defined(__HIP_DEVICE_COMPILE__)
#pragma clang attribute pop
#endif

extern "C" void kernel_launch(void* const* d_in, const int* in_sizes, int n_in, void* d_out, int out_size, void* d_ws, size_t ws_size,
                              hipStream_t stream) {
    Params p;
    p.u = (const u16*)d_in[0]; p.lar = (const u16*)d_in[1]; p.aim = (const u16*)d_in[2]; p.cre = (const u16*)d_in[3];
    p.cim = (const u16*)d_in[4]; p.ldt = (const u16*)d_in[5]; p.dsk = (const u16*)d_in[6]; p.wqkv = (const u16*)d_in[8];
    p.wphi = (const u16*)d_in[9]; p.wo = (const u16*)d_in[10]; p.gw = (const u16*)d_in[11]; p.out = (u16*)d_out; p.ws = (char*)d_ws;
    (void)hipFuncSetAttribute((const void*)s4d_hedgehog_mix, hipFuncAttributeMaxDynamicSharedMemorySize, LDS_BYTES);
    s4d_hedgehog_mix<<<512, 512, LDS_BYTES, stream>>>(p);
    k_tiny<<<POST_K, 64, 0, stream>>>(p.ws);
}
```

```cpp
#define GAP_POST 1
#define POST_K 512
#define POST_T 512
#define POST_L 81920
#include <hip/hip_runtime.h>
#include <hip/hip_bf16.h>
#include <stdint.h>

#if defined(__HIP_DEVICE_COMPILE__)
#pragma clang attribute push(__attribute__((target("no-packed-fp32-ops"))), apply_to = function)
#endif

typedef unsigned short u16;
typedef unsigned u32;
typedef unsigned long long u64;
typedef float f32x2 __attribute__((ext_vector_type(2)));
typedef float f32x4 __attribute__((ext_vector_type(4)));
typedef u32 u32x2 __attribute__((ext_vector_type(2)));
typedef u32 u32x4 __attribute__((ext_vector_type(4)));
typedef short s16x4 __attribute__((ext_vector_type(4)));
typedef __bf16 bf16x2 __attribute__((ext_vector_type(2)));
typedef __bf16 bf16x8 __attribute__((ext_vector_type(8)));
#define LAS __attribute__((address_space(3)))
#define GAS __attribute__((address_space(1)))
typedef LAS char* ldsp;
typedef __amdgpu_buffer_rsrc_t rsrc_t;
#define DEV __device__ __forceinline__

constexpr int L = 2048, D = 512;
constexpr int LDS_BYTES = 81920;
constexpr u64 TAG_STEP = 0x9E3779B97F4A7C15ull;
constexpr u32 SPIN_CAP = 40000;

constexpr u32 WS_E = 0, WS_TMO = 64;
constexpr u32 WS_FUT = 4096, WS_FKV = WS_FUT + 4096, WS_FY = WS_FKV + 4096, WS_FO = WS_FY + 4096;
constexpr u32 WS_G = 0x28000;
constexpr u32 WS_UT = 0x40000;
constexpr u32 WS_YT = WS_UT + 0x200000;
constexpr u32 WS_OP = WS_YT + 0x200000;
constexpr u32 WS_KVP = WS_OP + 0x200000;
constexpr u32 WS_ZP = WS_KVP + 0x100000;
constexpr u32 WS_DBG = WS_ZP + 0x10000;
constexpr u32 WS_USED = WS_DBG + 0x400000;

constexpr int AUX_SC1 = 16;

struct IdxTab { u16 v[2048]; };
constexpr IdxTab make_idx() {
    IdxTab t{};
    for (int i = 0; i < 2048; ++i) {
        int j = 2047 - i, p = j;
        int sh = j >= 1024 ? 3 : (j >= 512 ? 2 : (j >= 256 ? 1 : 0));
        if (sh) {
            int q = j >> sh, rem = j & ((1 << sh) - 1), half = 1 << (sh - 1);
            if (rem > half || (rem == half && (q & 1))) ++q;
            p = q << sh;
        }
        int row = 0, col = (p >> 3) & 15;
        if (p < 256) row = (p & 7) + 8 * (p >> 7);
        else if (p < 512) row = 16 + ((p - 256) >> 7) * 4 + ((p & 7) >> 1);
        else if (p < 1024) row = 24 + ((p - 512) >> 7) * 2 + ((p & 7) >> 2);
        else row = 32 + ((p - 1024) >> 7);
        t.v[i] = (u16)((row * 16 + col) * 2);
    }
    return t;
}
__device__ const IdxTab g_idx = make_idx();

DEV float u2f(u32 x) { return __builtin_bit_cast(float, x); }
DEV u32 f2u(float x) { return __builtin_bit_cast(u32, x); }
DEV float fexp(float x) { return __builtin_amdgcn_exp2f(x * 1.4426950408889634f); }
template <int CTRL> DEV float dpp_mov(float x) { return u2f(__builtin_amdgcn_update_dpp(0u, f2u(x), CTRL, 0xf, 0xf, true)); }
DEV float row16_sum(float x) {
    x += dpp_mov<0xB1>(x);
    x += dpp_mov<0x4E>(x);
    x += dpp_mov<0x141>(x);
    x += dpp_mov<0x140>(x);
    return x;
}
DEV u32 pk2(float lo, float hi) { f32x2 v = {lo, hi}; return __builtin_bit_cast(u32, __builtin_convertvector(v, bf16x2)); }
DEV float blo(u32 x) { return u2f(x << 16); }
DEV float bhi(u32 x) { return u2f(x & 0xffff0000u); }
DEV float rb(float f) { return blo(pk2(f, 0.f)); }
DEV f32x4 mfma32(u32x4 a, u32x4 b, f32x4 c) {
    return __builtin_amdgcn_mfma_f32_16x16x32_bf16(__builtin_bit_cast(bf16x8, a), __builtin_bit_cast(bf16x8, b), c, 0, 0, 0);
}
DEV f32x4 mfma16(u32x2 a, u32x2 b, f32x4 c) {
    return __builtin_amdgcn_mfma_f32_16x16x16bf16_1k(__builtin_bit_cast(s16x4, a), __builtin_bit_cast(s16x4, b), c, 0, 0, 0);
}
DEV u32x2 trread(ldsp p) { return __builtin_bit_cast(u32x2, __builtin_amdgcn_ds_read_tr16_b64_v4i16((LAS s16x4*)p)); }
DEV u32x4 lds_r128(ldsp p) { return *(LAS u32x4*)p; }
DEV u32x2 lds_r64(ldsp p) { return *(volatile LAS u32x2*)p; }
DEV void lds_w128(ldsp p, u32x4 v) { *(LAS u32x4*)p = v; }
DEV void lds_w64(ldsp p, u32x2 v) { *(LAS u32x2*)p = v; }
DEV void lds_w32(ldsp p, u32 v) { *(LAS u32*)p = v; }
DEV u32x4 cat(u32x2 a, u32x2 b) { u32x4 r = {a.x, a.y, b.x, b.y}; return r; }
DEV rsrc_t mkrsrc(const void* p, u32 bytes) { return __builtin_amdgcn_make_buffer_rsrc((void*)p, 0, bytes, 0x00020000); }
DEV u32x4 mkdesc(const void* p, u32 bytes) {
    u64 a = (u64)p;
    u32x4 d = {(u32)a, (u32)(a >> 32) & 0xffffu, bytes, 0x00020000u};
    return d;
}
DEV void dma16(u32x4 desc, ldsp dst, u32 voff, u32 soff) {
    asm volatile("s_mov_b32 m0, %0\n\ts_nop 0\n\tbuffer_load_dwordx4 %1, %2, %3 offen lds"
                 :: "s"((u32)(uintptr_t)dst), "v"(voff), "s"(desc), "s"(soff) : "memory");
}
DEV u32x4 ld128(rsrc_t r, u32 voff, u32 soff = 0) { return __builtin_amdgcn_raw_buffer_load_b128(r, voff, soff, 0); }
DEV u32x2 ld64(rsrc_t r, u32 voff, u32 soff = 0) { return __builtin_amdgcn_raw_buffer_load_b64(r, voff, soff, 0); }
DEV u32 ld16(rsrc_t r, u32 voff, u32 soff = 0) { return __builtin_amdgcn_raw_buffer_load_b16(r, voff, soff, 0); }
DEV u32x4 ld128c(rsrc_t r, u32 voff, u32 soff = 0) { return __builtin_amdgcn_raw_buffer_load_b128(r, voff, soff, AUX_SC1); }
DEV u32x2 ld64c(rsrc_t r, u32 voff, u32 soff = 0) { return __builtin_amdgcn_raw_buffer_load_b64(r, voff, soff, AUX_SC1); }
DEV u32 ld32c(rsrc_t r, u32 voff, u32 soff = 0) { return __builtin_amdgcn_raw_buffer_load_b32(r, voff, soff, AUX_SC1); }
DEV void st128c(rsrc_t r, u32x4 v, u32 voff, u32 soff = 0) { __builtin_amdgcn_raw_buffer_store_b128(v, r, voff, soff, AUX_SC1); }
DEV void st64c(rsrc_t r, u32x2 v, u32 voff, u32 soff = 0) { __builtin_amdgcn_raw_buffer_store_b64(v, r, voff, soff, AUX_SC1); }
#define WAIT_VM(n) asm volatile("s_waitcnt vmcnt(" #n ")" ::: "memory")
#define SCHED_A() __builtin_amdgcn_sched_barrier(0)
#define WAIT_LGKM0() asm volatile("s_waitcnt lgkmcnt(0)" ::: "memory")
DEV void bar() { asm volatile("s_waitcnt lgkmcnt(0)" ::: "memory"); __builtin_amdgcn_s_barrier(); asm volatile("" ::: "memory"); }
#define PRIO_A0 3
#define PRIO_A1 1
#define PRIO_BH2 3
#define PRIO_F 3
#define PRIO_BC 2
#define PRIO_BH 0
DEV u32 uni(u32 x) { return __builtin_amdgcn_readfirstlane(x); }

DEV float swap16_lo(float a, float b, float& other) {
    auto s = __builtin_amdgcn_permlane16_swap(f2u(a), f2u(b), false, false);
    other = u2f(s[1]);
    return u2f(s[0]);
}
DEV float swap32_lo(float a, float b, float& other) {
    auto s = __builtin_amdgcn_permlane32_swap(f2u(a), f2u(b), false, false);
    other = u2f(s[1]);
    return u2f(s[0]);
}
template <bool MAX> DEV void rows_reduce2(float& a, float& b) {
    float o, t = swap16_lo(a, b, o);
    t = MAX ? __builtin_fmaxf(t, o) : t + o;
    float u = swap32_lo(t, t, o);
    u = MAX ? __builtin_fmaxf(u, o) : u + o;
    a = swap16_lo(u, u, b);
}

typedef GAS u64 gu64;
DEV u64 make_tag(u64 e) {
    u32 lo = (u32)e, hi = (u32)(e >> 32);
    asm volatile("" : "+v"(lo), "+v"(hi));
    return (((u64)uni(hi) << 32) | uni(lo)) + TAG_STEP;
}
DEV u64 flag_ld(char* ws, u32 off) { return __hip_atomic_load((gu64*)(ws + off), __ATOMIC_RELAXED, __HIP_MEMORY_SCOPE_AGENT); }
DEV void flag_st(char* ws, u32 off, u64 v) { __hip_atomic_store((gu64*)(ws + off), v, __ATOMIC_RELAXED, __HIP_MEMORY_SCOPE_AGENT); }
DEV void wait_flags(char* ws, u32 off, u64 tag) {
#pragma unroll 1
    for (u32 spins = 0; spins < SPIN_CAP; ++spins) {
        u64 v = flag_ld(ws, off);
        if (__builtin_amdgcn_ballot_w64(v != tag) == 0) { asm volatile("" ::: "memory"); return; }
        __builtin_amdgcn_s_sleep(1);
    }
    flag_st(ws, WS_TMO, tag);
}
DEV void publish(char* ws, u32 off, u64 tag) {
    WAIT_VM(0);
    bar();
    if (threadIdx.x == 0) flag_st(ws, off, tag);
}

DEV void publish_counted(char* ws, u32 off, u64 tag, ldsp cnt, u32 n, int l) {
    WAIT_VM(0);
    u32 old = 0;
    if (l == 0) old = __hip_atomic_fetch_add((LAS u32*)cnt, 1u, __ATOMIC_RELAXED, __HIP_MEMORY_SCOPE_WORKGROUP);
    if (l == 0 && old == n - 1) flag_st(ws, off, tag);
}

#define STAMP(i) asm volatile("; MARK " #i)
#define FSTAMP(i) do { } while (0)
#define FS1(i) do { } while (0)
#define FS2(i) do { } while (0)
#define FS3(i) do { } while (0)
#define FS4(i) do { } while (0)
#define STAMP_ARG
#define STAMP_PASS

struct Params {
    char* ws; const u16* u; const u16* wqkv; const u16* wphi; const u16* lar; const u16* aim; const u16* cre; const u16* cim;
    const u16* ldt; const u16* gw; const u16* dsk; const u16* wo; u16* out;
};

struct cplx { float r, i; };
DEV cplx cmul(cplx a, cplx b) { cplx c; c.r = a.r * b.r - a.i * b.i; c.i = a.r * b.i + a.i * b.r; return c; }
DEV cplx cexp_p(float zr2, float zrev, float p) {
    float e = __builtin_amdgcn_exp2f(zr2 * p);
    float a = __builtin_amdgcn_fractf(zrev * p);
    cplx c; c.r = e * __builtin_amdgcn_cosf(a); c.i = e * __builtin_amdgcn_sinf(a); return c;
}

template <int NW> DEV void wo_prefetch(const Params& p, ldsp lds, int ct, int w, int l) {
    const u32x4 rwo = mkdesc(p.wo, D * D * 2);
    u32 voff = (l >> 3) * 1024 + ct * 128 + ((((l & 7) >> 1) ^ ((l >> 4) & 3)) * 32) + (l & 1) * 16;
#pragma unroll
    for (int i = 0; i < 64 / NW; ++i) {
        int piece = i * NW + w;
        dma16(rwo, lds + 16384 + piece * 1024, voff, piece * 8192);
    }
}

DEV u32 touch_pages(const Params& p, int b, int l) {
    const int k = l & 15;
    const char* a = p.ws + WS_KVP + (b & 255) * 4096;
    if (k == 1) a = p.ws + WS_OP + (size_t)(b & 255) * 8192;
    if (k == 2) a = p.ws + WS_YT + (size_t)(b & 255) * 8192;
    if (k == 3) a = p.ws + WS_UT + (size_t)(b & 255) * 8192;
    if (k == 4) a = p.ws + WS_ZP + (b & 255) * 256;
    if (k == 5) a = p.ws + WS_G;
    if (k == 6) a = (const char*)p.out + (size_t)(b & 255) * 8192;
    if (k == 7) a = (const char*)p.wo + (size_t)(b & 255) * 2048;
    if (k == 8) a = (const char*)p.dsk;
    if (k == 9) a = (const char*)p.wphi;
    if (k == 10) a = p.ws + WS_OP + 0x1ff000;
    if (k == 11) a = p.ws + WS_YT + 0x1ff000;
    if (k == 12) a = p.ws + WS_UT + 0x1ff000;
    if (k == 13) a = (const char*)p.out + 0x1ff000;
    return *(const volatile GAS u32*)a;
}

DEV u64 role_conv(const Params& p, ldsp lds, char* ws, u64 e, int ba, int w, int l STAMP_ARG) {
    const int t = threadIdx.x, xcd = ba & 7, j = ba >> 3, g = l >> 4, r16 = l & 15;
    const int a = xcd >> 2, c = xcd & 3;
    rsrc_t ru = mkrsrc(p.u, L * D * 2), rws = mkrsrc(ws, WS_USED);
    const int ch0 = 2 * (32 * xcd + j);
    __builtin_amdgcn_s_setprio(PRIO_A0);
    const int tb = 16 * a + (j >> 1), cg = 2 * c + (j & 1);
    {
        u32 voff = (tb * 64 + (l >> 3)) * 1024 + cg * 128 + (l & 7) * 16;
        dma16(mkdesc(p.u, L * D * 2), lds + 70656 + w * 1024, voff, w * 8192);
    }
    const int gtok = 1024 * a + 8 * (32 * c + j) + w;
    u32x4 gx = ld128(ru, gtok * 1024 + l * 16);
    u32x4 gwv = *(const GAS u32x4*)(p.gw + l * 8);
    const int kch = (t >> 7) & 1, kn = t & 127;
    const int pidx = (ch0 + kch) * 128 + kn;
    float lar = blo(p.lar[pidx]), aim = blo(p.aim[pidx]), cre = blo(p.cre[pidx]), cim = blo(p.cim[pidx]), ldt = blo(p.ldt[ch0 + kch]);
    u32x4 idx = *(const GAS u32x4*)(g_idx.v + (t & 255) * 8);
    if (w == 7) { u32 x = touch_pages(p, ba, l); asm volatile("" :: "v"(x)); }
    SCHED_A();
    __builtin_amdgcn_s_setprio(PRIO_A1);
    WAIT_VM(0);
    const u64 tag = make_tag(e);
    bar();
    STAMP(1);
    {
        const int cw = w & 3, s = w >> 2;
        ldsp src = lds + 70656 + (32 * s + 8 * g + (r16 >> 2)) * 128 + 32 * cw + 8 * (l & 3);
        u32x4 v = cat(trread(src), trread(src + 4 * 128));
        st128c(rws, v, WS_UT + (cg * 64 + 16 * cw + r16) * 4096 + (tb * 64 + 32 * s + 8 * g) * 2);
    }
    {
        float s0 = 0.f, s1 = 0.f;
#pragma unroll
        for (int q = 0; q < 4; ++q) {
            u32 pr = pk2(blo(gx[q]) * blo(gwv[q]), bhi(gx[q]) * bhi(gwv[q]));
            s0 += blo(pr); s1 += bhi(pr);
        }
        s0 = row16_sum(s0 + s1);
        s1 = s0;
        rows_reduce2<false>(s0, s1);
        float g0 = rb(__builtin_amdgcn_rcpf(1.f + fexp(-rb(s0))));
        if (l == 0) __builtin_amdgcn_raw_buffer_store_b32(f2u(g0), rws, WS_G + gtok * 4, 0, AUX_SC1);
    }
    STAMP(2);
    {
        const float dt = rb(fexp(ldt));
        const float zr2 = -rb(fexp(lar)) * dt * 1.4426950408889634f, zrev = aim * dt * 0.15915494309189535f;
        ldsp T = lds + kch * 33792 + kn * 4;
        auto put = [&](int row, cplx v) { lds_w32(T + row * 528, pk2(v.r, v.i)); };
        const cplx w128 = cexp_p(zr2, zrev, 128.f);
        cplx G; G.r = 2.f * cre * dt; G.i = 2.f * cim * dt;
        if (w < 4) {
            cplx wp[8];
            wp[1] = cexp_p(zr2, zrev, 1.f); wp[2] = cmul(wp[1], wp[1]); wp[3] = cmul(wp[2], wp[1]); wp[4] = cmul(wp[2], wp[2]);
            wp[5] = cmul(wp[4], wp[1]); wp[6] = cmul(wp[4], wp[2]); wp[7] = cmul(wp[4], wp[3]);
#pragma unroll
            for (int aa = 0; aa < 4; ++aa) {
                if (aa == 1) { FS4(7); publish(ws, WS_FUT + cg * 256 + tb * 8, tag); FS4(8); }
                if (aa < 2) {
                    put(8 * aa, G);
#pragma unroll
                    for (int cc = 1; cc < 8; ++cc) put(8 * aa + cc, cmul(G, wp[cc]));
                } else {
                    put(16 + 4 * (aa - 2), G);
#pragma unroll
                    for (int cc = 1; cc < 4; ++cc) put(16 + 4 * (aa - 2) + cc, cmul(G, wp[2 * cc]));
                }
                if (aa < 3) G = cmul(G, w128);
            }
        } else {
            const cplx w4 = cexp_p(zr2, zrev, 4.f);
            G = cmul(G, cexp_p(zr2, zrev, 512.f));
#pragma unroll
            for (int aa = 4; aa <= 16; ++aa) {
                if (aa == 10) { FS4(7); publish(ws, WS_FUT + cg * 256 + tb * 8, tag); FS4(8); }
                if (aa < 8) {
                    put(24 + 2 * (aa - 4), G);
                    put(24 + 2 * (aa - 4) + 1, cmul(G, w4));
                } else put(32 + aa - 8, G);
                if (aa < 16) G = cmul(G, w128);
            }
            cplx B[9];
            B[1] = cmul(w4, w4); B[2] = cmul(B[1], B[1]); B[3] = cmul(B[2], B[1]); B[4] = cmul(B[2], B[2]);
            B[5] = cmul(B[4], B[1]); B[6] = cmul(B[4], B[2]); B[7] = cmul(B[4], B[3]); B[8] = cmul(B[4], B[4]);
            lds_w32(T + 48 * 528, 0x00003f80u);
#pragma unroll
            for (int b = 1; b < 16; ++b) {
                cplx v = b <= 8 ? B[b] : cmul(B[8], B[b - 8]);
                lds_w32(T + (48 + b) * 528, pk2(v.r, -v.i));
            }
        }
    }
    FS4(9);
    bar();
    FS4(10);
    if (w == 7) wait_flags(ws, WS_FUT + xcd * 256 + (l & 31) * 8, tag);
    if (w < 6) {
        const int ch = w >= 3, tile = w - 3 * ch;
        ldsp T = lds + ch * 33792 + r16 * 528 + g * 16;
        f32x4 k0 = {0, 0, 0, 0};
#pragma unroll
        for (int s = 0; s < 8; ++s) k0 = mfma32(lds_r128(T + tile * 16 * 528 + s * 64), lds_r128(T + 48 * 528 + s * 64), k0);
        ldsp KC = lds + 67584 + ch * 1536 + r16 * 2 + (tile * 16 + 4 * g) * 32;
        u32 a0 = pk2(k0[0], k0[1]), a1 = pk2(k0[2], k0[3]);
        *(LAS u16*)(KC) = (u16)a0; *(LAS u16*)(KC + 32) = (u16)(a0 >> 16);
        *(LAS u16*)(KC + 64) = (u16)a1; *(LAS u16*)(KC + 96) = (u16)(a1 >> 16);
    }
    bar();
    STAMP(3);
    const int hch = w >> 2, t8 = t & 255;
    const u32 uoff = WS_UT + (ch0 + hch) * 4096 + t8 * 16;
    u32x4 d0 = ld128c(rws, uoff), d1 = ld128c(rws, uoff + 4), d2 = ld128c(rws, uoff + 8);
    u32x2 f = ld64c(rws, WS_UT + (ch0 + hch) * 4096);
    SCHED_A();
    {
        ldsp KC = lds + 67584 + hch * 1536;
        u32x4 v;
#pragma unroll
        for (int q = 0; q < 4; ++q) {
            u32 lo = *(LAS u16*)(KC + (idx[q] & 0xffffu)), hi = *(LAS u16*)(KC + (idx[q] >> 16));
            v[q] = lo | (hi << 16);
        }
        lds_w128(lds + hch * 4608 + t8 * 16, v);
        if (t8 < 32) { u32x4 z = {0, 0, 0, 0}; lds_w128(lds + hch * 4608 + 4096 + t8 * 16, z); }
    }
    SCHED_A();
    STAMP(4);
    constexpr int UB = 9216, CS = 4160;
    {
        u32x4 v1, v3;
#pragma unroll
        for (int q = 0; q < 4; ++q) { v1[q] = __builtin_amdgcn_alignbit(d1[q], d0[q], 16); v3[q] = __builtin_amdgcn_alignbit(d2[q], d1[q], 16); }
        ldsp U = lds + UB + hch * 4 * CS + 32 + t8 * 16;
        lds_w128(U, d0); lds_w128(U + CS, v1); lds_w128(U + 2 * CS, d1); lds_w128(U + 3 * CS, v3);
        if (t8 < 4) {
            u32x4 z = {0, 0, 0, 0}, y = z;
            if (t8 == 1) y.w = f.x << 16;
            if (t8 == 2) y.w = f.x;
            if (t8 == 3) { y.z = f.x << 16; y.w = __builtin_amdgcn_alignbit(f.y, f.x, 16); }
            ldsp F = lds + UB + (hch * 4 + t8) * CS;
            lds_w128(F, z); lds_w128(F + 16, y);
        }
    }
    bar();
    STAMP(5);
    {
        const int mq = w & 3;
        const int s = (r16 + 1) & 3;
        ldsp A0 = lds + UB + (hch * 4 + s) * CS + 2 * (1 + r16 - s + 8 * g) + mq * 2 * 64;
        ldsp B0 = lds + hch * 4608 + 4064 - 3584 - 32 * r16 + 16 * g + mq * 2 * 64;
        f32x4 acc[8];
#pragma unroll
        for (int i = 0; i < 8; ++i) acc[i] = f32x4{0, 0, 0, 0};
#pragma unroll
        for (int mm = 0; mm < 2; ++mm) {
            u32x4 Af[8], Bf[8];
#pragma unroll
            for (int i = 0; i < 8; ++i) {
                Bf[i] = lds_r128(B0 + mm * 64 + (7 - i) * 512);
                Af[i] = cat(lds_r64(A0 + mm * 64 + i * 512), lds_r64(A0 + mm * 64 + i * 512 + 8));
            }
#pragma unroll
            for (int i = 0; i < 8; ++i)
#pragma unroll
                for (int cc = i; cc < 8; ++cc) acc[cc] = mfma32(Af[i], Bf[cc - i], acc[cc]);
            __builtin_amdgcn_sched_barrier(0);
        }
        FSTAMP(7);
        bar();
        FSTAMP(8);
        ldsp R = lds + hch * 24576 + l * 16;
#pragma unroll
        for (int cc = 0; cc < 8; ++cc)
            if ((cc >> 1) != mq) lds_w128(R + (cc * 3 + ((mq - (cc >> 1) - 1) & 3)) * 1024, __builtin_bit_cast(u32x4, acc[cc]));
        bar();
        FSTAMP(9);
#pragma unroll
        for (int q = 0; q < 4; ++q)
            if (q == mq) {
                u32 pk[2][2];
#pragma unroll
                for (int h = 0; h < 2; ++h) {
                    float v[4];
#pragma unroll
                    for (int r = 0; r < 4; ++r) v[r] = acc[2 * q + h][r];
#pragma unroll
                    for (int sl = 0; sl < 3; ++sl) {
                        u32x4 o = lds_r128(R + ((2 * q + h) * 3 + sl) * 1024);
#pragma unroll
                        for (int r = 0; r < 4; ++r) v[r] += u2f(o[r]);
                    }
                    pk[h][0] = pk2(v[0], v[1]); pk[h][1] = pk2(v[2], v[3]);
                }
                auto sx = __builtin_amdgcn_permlane16_swap(pk[0][0], pk[1][0], false, false);
                auto sy = __builtin_amdgcn_permlane16_swap(pk[0][1], pk[1][1], false, false);
                u32x4 v = {sx[0], sy[0], sx[1], sy[1]};
                st128c(rws, v, WS_YT + (ch0 + hch) * 4096 + (256 * (2 * q + (g & 1)) + 16 * r16 + 4 * (g & 2)) * 2);
            }
    }
    FSTAMP(10);
    publish(ws, WS_FY + xcd * 256 + j * 8, tag);
    STAMP(6);
    return tag;
}

typedef float f32x16 __attribute__((ext_vector_type(16)));
DEV f32x16 mfma32x32(u32x4 a, u32x4 b, f32x16 c) {
    return __builtin_amdgcn_mfma_f32_32x32x16_bf16(__builtin_bit_cast(bf16x8, a), __builtin_bit_cast(bf16x8, b), c, 0, 0, 0);
}
DEV float vmax3(float a, float b, float c) { float r; asm("v_max3_f32 %0, %1, %2, %3" : "=v"(r) : "v"(a), "v"(b), "v"(c)); return r; }
DEV float vmin3(float a, float b, float c) { float r; asm("v_min3_f32 %0, %1, %2, %3" : "=v"(r) : "v"(a), "v"(b), "v"(c)); return r; }

template <bool POS, bool NEG> DEV void feature_map(u32x4 x0, u32x4 x1, u32x4 wown, int l, u32 (&ph)[16]) {
    const bool odd = (l >> 2) & 1;
    u32x4 a0, a1;
#pragma unroll
    for (int q = 0; q < 4; ++q) { a0[q] = odd ? 0u : wown[q]; a1[q] = odd ? wown[q] : 0u; }
    f32x16 pr;
#pragma unroll
    for (int r = 0; r < 16; ++r) pr[r] = 0.f;
    pr = mfma32x32(a0, x0, pr);
    pr = mfma32x32(a1, x1, pr);
    float p[16];
#pragma unroll
    for (int r = 0; r < 16; r += 2) { u32 v = pk2(pr[r], pr[r + 1]); p[r] = blo(v); p[r + 1] = bhi(v); }
    constexpr float LG = 1.4426950408889634f;
#pragma unroll
    for (int sg = 0; sg < 2; ++sg) {
        if (sg == 0 ? !POS : !NEG) continue;
        float m = sg ? vmin3(p[0], p[1], p[2]) : vmax3(p[0], p[1], p[2]);
#pragma unroll
        for (int r = 3; r < 15; r += 2) m = sg ? vmin3(m, p[r], p[r + 1]) : vmax3(m, p[r], p[r + 1]);
        m = sg ? vmin3(m, p[15], p[15]) : vmax3(m, p[15], p[15]);
        const float sc = sg ? -LG : LG, c = -m * sc;
        float a[16];
#pragma unroll
        for (int r = 0; r < 16; ++r) a[r] = __builtin_fmaf(p[r], sc, c);
#pragma unroll
        for (int r = 0; r < 16; ++r) a[r] = __builtin_amdgcn_exp2f(a[r]);
        float sa[4];
#pragma unroll
        for (int q = 0; q < 4; ++q) sa[q] = (a[4 * q] + a[4 * q + 1]) + (a[4 * q + 2] + a[4 * q + 3]);
        const float ia = __builtin_amdgcn_rcpf((sa[0] + sa[1]) + (sa[2] + sa[3]));
#pragma unroll
        for (int r = 0; r < 8; ++r) ph[8 * sg + r] = pk2(a[2 * r] * ia, a[2 * r + 1] * ia);
    }
}

DEV void role_attn(const Params& p, ldsp lds, char* ws, u64 e, int bb, int w, int l STAMP_ARG) {
    const int xcd = bb & 7, j = bb >> 3, g = l >> 4, r16 = l & 15, h = l >> 5, w4 = w & 3;
    const int rb_ = 8 * (xcd >> 2) + (j >> 2), cb = 4 * (xcd & 3) + (j & 3);
    const int rot = (j + xcd) & 7;
    rsrc_t rws = mkrsrc(ws, WS_USED);
#define SCHED() __builtin_amdgcn_sched_barrier(0)
    constexpr int XCH = 32768, WPH = 49152, PH = 51200, PHS = 10240, VV = 71680, VS = 5120, FR = 0, CNT = 8192;
    const int tok = 32 * w4 + (l & 31);
    const u32 wph = WPH + (l & 1) * 512 + (4 * h + (r16 >> 2)) * 32 + (8 * (g & 1) + 4 * ((l & 3) >> 1)) * 2;
    u64 tag;
    if (w >= 4) {
        __builtin_amdgcn_s_setprio(PRIO_BH);
        const u32x4 ru = mkdesc(p.u, L * D * 2), rw = mkdesc(p.wqkv, D * 3 * D * 2);
        const u32 xv = (rb_ * 128 + w4 * 8 + (l >> 3)) * 1024 + (((l & 7) ^ (((w4 & 1) << 2) | ((l >> 4) & 3))) * 16);
        const u32 wv = (16 * w4 + (l >> 2)) * 3072 + cb * 64 + (l & 3) * 16;
        auto issue_x = [&](int s) {
#pragma unroll
            for (int i = 0; i < 4; ++i) dma16(ru, lds + (s % 3) * 16384 + (4 * i + w4) * 1024, xv, ((s + rot) & 7) * 128 + i * 32768);
        };
        auto issue_w = [&](int s, int sec) {
            dma16(rw, lds + 49152 + ((3 * s + sec) & 7) * 4096 + w4 * 1024, wv, ((s + rot) & 7) * (64 * 3072) + sec * 1024);
        };
        FS3(6);
        issue_w(0, 0); issue_w(0, 1); issue_w(0, 2); issue_x(0);
        FS3(7);
        __builtin_amdgcn_s_barrier();
        FS3(8);
        issue_w(1, 0); issue_w(1, 1); issue_w(1, 2); issue_x(1);
        FS3(9);
#pragma unroll
        for (int s = 0; s < 8; ++s) {
            if (s == 0) WAIT_VM(7); else if (s < 7) WAIT_VM(6); else WAIT_VM(0);
            if (s == 0) FS3(10);
            __builtin_amdgcn_s_barrier();
            if (s >= 1 && s + 1 < 8) issue_w(s + 1, 2);
            if (s + 2 < 8) { issue_x(s + 2); issue_w(s + 2, 0); issue_w(s + 2, 1); }
            if (s == 6 && w == 4) dma16(mkdesc(p.wphi, 32 * 256 * 2), lds + WPH, l * 16, cb * 1024);
            if (s == 7 && w == 4 && l < 2) lds_w32(lds + CNT + l * 4, 0u);
        }
        tag = make_tag(e);
        bar();
        ldsp X = lds + XCH + w4 * 4096 + l * 16;
        u32x4 wown = cat(trread(lds + wph), trread(lds + wph + 8 * 32));
        u32x4 q0 = lds_r128(X), q1 = lds_r128(X + 1024);
        bar();
        bar();
        u32 ph[16];
        feature_map<true, true>(q0, q1, wown, l, ph);
        u32x4 bq[2][2];
#pragma unroll
        for (int sg = 0; sg < 2; ++sg)
#pragma unroll
            for (int q = 0; q < 4; ++q) {
                auto sw = __builtin_amdgcn_permlane32_swap(ph[8 * sg + q], ph[8 * sg + q + 4], false, false);
                bq[0][sg][q] = sw[0]; bq[1][sg][q] = sw[1];
            }
        STAMP(3);
        __builtin_amdgcn_s_setprio(PRIO_BH2);
        bar();
#pragma unroll
        for (int hh = 0; hh < 2; ++hh) {
            f32x16 nd;
#pragma unroll
            for (int r = 0; r < 16; ++r) nd[r] = 0.f;
            nd = mfma32x32(lds_r128(lds + FR + (2 * hh) * 1024 + l * 16), bq[hh][0], nd);
            nd = mfma32x32(lds_r128(lds + FR + (2 * hh + 1) * 1024 + l * 16), bq[hh][1], nd);
            const float inv = __builtin_amdgcn_rcpf(rb(rb(nd[8]) + 1e-6f));
            u32x4 o;
#pragma unroll
            for (int q = 0; q < 4; ++q) { u32 n = pk2(nd[2 * q], nd[2 * q + 1]); o[q] = pk2(blo(n) * inv, bhi(n) * inv); }
            st128c(rws, o, WS_OP + (rb_ * 128 + tok) * 1024 + cb * 64 + hh * 32 + h * 16);
        }
        FS2(10);
        publish_counted(ws, WS_FO + rb_ * 256 + cb * 8, tag, lds + CNT + 4, 4, l);
        STAMP(5);
    } else {
        __builtin_amdgcn_s_setprio(PRIO_BC);
        f32x16 acc[3];
#pragma unroll
        for (int sec = 0; sec < 3; ++sec)
#pragma unroll
            for (int r = 0; r < 16; ++r) acc[sec][r] = 0.f;
        const u32 xa = tok * 128 + ((h ^ ((tok >> 1) & 7)) * 16);
        const u32 wa = 49152 + (8 * h + (r16 >> 2)) * 64 + (g & 1) * 32 + 8 * (l & 3);
        __builtin_amdgcn_s_barrier();
#pragma unroll
        for (int s = 0; s < 8; ++s) {
            __builtin_amdgcn_s_barrier();
            asm volatile("" ::: "memory");
            if (s == 0) FS1(6);
            if (s == 1) FS1(7);
            if (s == 2) FS1(8);
            if (s == 4) FS1(9);
            if (s == 6) FS1(10);
            u32x4 xf[4], wf[12];
            auto read_x = [&](int ks) { xf[ks] = lds_r128(lds + (s % 3) * 16384 + (xa ^ (32 * ks))); };
            auto read_w = [&](int i) {
                ldsp a = lds + wa + ((3 * s + i % 3) & 7) * 4096 + (i / 3) * 1024;
                wf[i] = cat(trread(a), trread(a + 256));
            };
            read_x(0); read_w(0); read_w(1); read_w(2);
            SCHED();
#pragma unroll
            for (int i = 0; i < 12; ++i) {
                if (i % 3 == 0 && i / 3 + 1 < 4) read_x(i / 3 + 1);
                if (i + 3 < 12) read_w(i + 3);
                SCHED();
                acc[i % 3] = mfma32x32(wf[i], xf[i / 3], acc[i % 3]);
                SCHED();
            }
        }
        u32x4 fr[3][2];
#pragma unroll
        for (int sec = 0; sec < 3; ++sec)
#pragma unroll
            for (int hh = 0; hh < 2; ++hh)
#pragma unroll
                for (int q = 0; q < 4; ++q) fr[sec][hh][q] = pk2(acc[sec][8 * hh + 2 * q], acc[sec][8 * hh + 2 * q + 1]);
        lds_w128(lds + XCH + w * 4096 + l * 16, fr[0][0]);
        lds_w128(lds + XCH + w * 4096 + 1024 + l * 16, fr[0][1]);
        tag = make_tag(e);
        bar();
        STAMP(1);
#pragma unroll
        for (int hh = 0; hh < 2; ++hh) {
            ldsp q = lds + VV + hh * VS + tok * 40 + 8 * h;
            lds_w64(q, u32x2{fr[2][hh].x, fr[2][hh].y});
            lds_w64(q + 16, u32x2{fr[2][hh].z, fr[2][hh].w});
        }
        {
            u32x4 wown = cat(trread(lds + wph), trread(lds + wph + 8 * 32));
            u32 ph[16];
            feature_map<true, true>(fr[1][0], fr[1][1], wown, l, ph);
            ldsp q = lds + PH + h * PHS + tok * 80;
#pragma unroll
            for (int i = 0; i < 4; ++i) lds_w128(q + i * 16, u32x4{ph[4 * i], ph[4 * i + 1], ph[4 * i + 2], ph[4 * i + 3]});
        }
        FS2(6);
        bar();
        FS2(7);
        const int hh = w >> 1, sg = w & 1;
        {
            f32x4 kv = {0, 0, 0, 0}, z = {0, 0, 0, 0};
            const u32x4 ones = {0x3f803f80u, 0x3f803f80u, 0x3f803f80u, 0x3f803f80u};
            ldsp pa = lds + PH + hh * PHS + (8 * g + (r16 >> 2)) * 80 + sg * 32 + 8 * (l & 3);
            ldsp pb = lds + VV + hh * VS + (8 * g + (r16 >> 2)) * 40 + 8 * (l & 3);
#pragma unroll
            for (int tg = 0; tg < 4; ++tg) {
                u32x4 A = cat(trread(pa + tg * 32 * 80), trread(pa + tg * 32 * 80 + 4 * 80));
                u32x4 B = cat(trread(pb + tg * 32 * 40), trread(pb + tg * 32 * 40 + 4 * 40));
                kv = mfma32(A, B, kv);
                z = mfma32(A, ones, z);
            }
            st128c(rws, __builtin_bit_cast(u32x4, kv), WS_KVP + ((cb * 16 + rb_) * 4 + w) * 1024 + l * 16);
            if (r16 == 0) st128c(rws, __builtin_bit_cast(u32x4, z), WS_ZP + ((cb * 16 + rb_) * 4 + w) * 64 + g * 16);
        }
        FS2(8);
        bar();
        publish_counted(ws, WS_FKV + cb * 256 + rb_ * 8, tag, lds + CNT, 4, l);
        STAMP(2);
        wait_flags(ws, WS_FKV + cb * 256 + r16 * 8, tag);
        STAMP(4);
        u32x4 part[16];
#pragma unroll
        for (int r = 0; r < 16; ++r) part[r] = ld128c(rws, WS_KVP + (cb * 16 * 4 + w) * 1024 + l * 16, r * 4096);
        u32x4 zp = ld128c(rws, WS_ZP + ((cb * 16 + (l >> 2)) * 4 + w) * 64 + (l & 3) * 16);
        float s[4] = {u2f(part[0].x), u2f(part[0].y), u2f(part[0].z), u2f(part[0].w)};
#pragma unroll
        for (int r = 1; r < 16; ++r)
#pragma unroll
            for (int q = 0; q < 4; ++q) s[q] += u2f(part[r][q]);
        float zs[4] = {u2f(zp.x), u2f(zp.y), u2f(zp.z), u2f(zp.w)};
#pragma unroll
        for (int q = 0; q < 4; ++q) {
            float x = zs[q];
            x += dpp_mov<0x124>(x);
            x += dpp_mov<0x128>(x);
            zs[q] = x;
        }
        rows_reduce2<false>(zs[0], zs[1]);
        rows_reduce2<false>(zs[2], zs[3]);
        FS2(9);
        ldsp F = lds + FR + w * 1024;
        lds_w64(F + (r16 + 32 * (g >> 1)) * 16 + 8 * (g & 1), u32x2{pk2(s[0], s[1]), pk2(s[2], s[3])});
        lds_w64(F + (16 + (l >> 2) + 32 * ((l & 3) >> 1)) * 16 + 8 * (l & 1), u32x2{pk2(zs[0], zs[1]), pk2(zs[2], zs[3])});
        bar();
    }
}

DEV void final_stage(const Params& p, ldsp lds, char* ws, u64 tag, int ba, int w, int l STAMP_ARG) {
    const int t = threadIdx.x, g = l >> 4, r16 = l & 15, cw = w & 3, th = w >> 2;
    const int tt64 = 4 * (ba & 7) + ((ba >> 3) & 3), ct = ba >> 5;
    rsrc_t ru = mkrsrc(p.u, L * D * 2), rws = mkrsrc(ws, WS_USED), rout = mkrsrc(p.out, L * D * 2);
    const int tok0 = tt64 * 64, chl = ct * 64 + 16 * cw + 4 * g, tok = tok0 + 32 * th + r16;
    constexpr int YL = 4096;
    bar();
    wo_prefetch<8>(p, lds, ct, w, l);
    u32x2 uu[2];
#pragma unroll
    for (int tt = 0; tt < 2; ++tt) uu[tt] = ld64(ru, (tok + 16 * tt) * 1024 + chl * 2);
    const u32x2 dsk = *(const GAS u32x2*)(p.dsk + chl);
    if (ba == 0 && w >= 4) {
        const int q = t - 256;
        wait_flags(ws, WS_FUT + (q >> 5) * 256 + (q & 31) * 8, tag);
        wait_flags(ws, WS_FKV + (q >> 4) * 256 + (q & 15) * 8, tag);
    }
    if (w == 0) {
        const int tg8 = tt64 * 8 + (l & 7), ga = tg8 >> 7, gc = (tg8 >> 5) & 3, gj = tg8 & 31;
        const int gtb = 16 * ga + (gj >> 1), gcg = 2 * gc + (gj & 1);
        wait_flags(ws, l < 32 ? WS_FY + ct * 256 + l * 8 : WS_FUT + gcg * 256 + gtb * 8, tag);
    }
    WAIT_VM(0);
    bar();
    if (ba == 0 && t == 0) flag_st(ws, WS_E, tag);
    STAMP(8);
    float gate[2];
    {
        u32x4 y = ld128c(rws, WS_YT + (ct * 64 + (t >> 3)) * 4096 + tok0 * 2 + (t & 7) * 16);
#pragma unroll
        for (int tt = 0; tt < 2; ++tt) gate[tt] = u2f(ld32c(rws, WS_G + (tok + 16 * tt) * 4));
        lds_w128(lds + YL + (t >> 3) * 128 + (t & 7) * 16, y);
    }
    u32x4 wf[16];
    {
        const int row = 16 * (g >> 1) + 4 * (g & 1) + (r16 >> 2);
        ldsp q = lds + 16384 + row * 128 + ((cw ^ ((row >> 1) & 3)) * 32) + 8 * (l & 3);
#pragma unroll
        for (int ks = 0; ks < 16; ++ks) wf[ks] = cat(trread(q + ks * 4096), trread(q + ks * 4096 + 1024));
    }
    bar();
    float ga[2][4], g1[2];
#pragma unroll
    for (int tt = 0; tt < 2; ++tt) {
        u32x2 y = trread(lds + YL + (16 * cw + 4 * g + (r16 >> 2)) * 128 + (32 * th + 16 * tt) * 2 + 8 * (l & 3));
        const float gg = gate[tt];
        g1[tt] = rb(1.f - gg);
        u32 t0 = pk2(blo(uu[tt].x) * blo(dsk.x), bhi(uu[tt].x) * bhi(dsk.x)), t1 = pk2(blo(uu[tt].y) * blo(dsk.y), bhi(uu[tt].y) * bhi(dsk.y));
        u32 a0 = pk2(blo(y.x) + blo(t0), bhi(y.x) + bhi(t0)), a1 = pk2(blo(y.y) + blo(t1), bhi(y.y) + bhi(t1));
        u32 ga0 = pk2(gg * blo(a0), gg * bhi(a0)), ga1 = pk2(gg * blo(a1), gg * bhi(a1));
        ga[tt][0] = blo(ga0); ga[tt][1] = bhi(ga0); ga[tt][2] = blo(ga1); ga[tt][3] = bhi(ga1);
        asm volatile("" : "+v"(ga[tt][0]), "+v"(ga[tt][1]), "+v"(ga[tt][2]), "+v"(ga[tt][3]), "+v"(g1[tt]));
    }
    STAMP(9);
    __builtin_amdgcn_s_setprio(PRIO_F);
    if (w == 0) wait_flags(ws, WS_FO + (tok0 >> 7) * 256 + r16 * 8, tag);
    bar();
    STAMP(10);
#pragma unroll
    for (int hf = 0; hf < 2; ++hf) {
        u32x4 ov[4];
#pragma unroll
        for (int i = 0; i < 4; ++i) ov[i] = ld128c(rws, WS_OP + (tok0 + 8 * w + 4 * hf) * 1024 + l * 16, i * 1024);
#pragma unroll
        for (int i = 0; i < 4; ++i) {
            const int tk = 8 * w + 4 * hf + i;
            lds_w128(lds + 16384 + tk * 1024 + ((l ^ (tk & 15)) * 16), ov[i]);
        }
    }
    bar();
    STAMP(11);
    f32x4 acc[2] = {f32x4{0, 0, 0, 0}, f32x4{0, 0, 0, 0}};
    {
        u32 ob[4];
#pragma unroll
        for (int k4 = 0; k4 < 4; ++k4) ob[k4] = 16384 + (32 * th + r16) * 1024 + ((((4 * k4) ^ (r16 & 12)) | (g ^ (r16 & 3))) * 16);
#pragma unroll
        for (int ks = 0; ks < 16; ++ks)
#pragma unroll
            for (int tt = 0; tt < 2; ++tt)
                acc[tt] = mfma32(wf[ks], lds_r128(lds + ob[ks & 3] + (ks >> 2) * 256 + tt * 16384), acc[tt]);
    }
#pragma unroll
    for (int tt = 0; tt < 2; ++tt) {
        u32x2 o = {pk2(__builtin_fmaf(g1[tt], acc[tt][0], ga[tt][0]), __builtin_fmaf(g1[tt], acc[tt][1], ga[tt][1])),
                   pk2(__builtin_fmaf(g1[tt], acc[tt][2], ga[tt][2]), __builtin_fmaf(g1[tt], acc[tt][3], ga[tt][3]))};
        __builtin_amdgcn_raw_buffer_store_b64(o, rout, (tok + 16 * tt) * 1024 + chl * 2, 0, 0);
    }
    STAMP(12);
}

__global__ __launch_bounds__(512, 4) void s4d_hedgehog_mix(Params p) {
    extern __shared__ __attribute__((aligned(16))) char smem[];
    ldsp lds = (ldsp)smem;
    char* ws = p.ws;
    const int b = blockIdx.x, l = threadIdx.x & 63, w = uni(threadIdx.x >> 6);
    STAMP(0);
    asm volatile("" :: "s"(p.ws), "s"(p.u), "s"(p.wqkv), "s"(p.ldt), "s"(p.gw));
    const u64 e = flag_ld(ws, WS_E);
    if (b >= 256) {
        const u64 tag = role_conv(p, lds, ws, e, b - 256, w, l STAMP_PASS);
        final_stage(p, lds, ws, tag, b - 256, w, l STAMP_PASS);
    } else {
        role_attn(p, lds, ws, e, b, w, l STAMP_PASS);
    }
    if (b >= 256 && threadIdx.x == 0) __hip_atomic_store((GAS u64*)(p.ws + 0x3200000) + (b - 256), (u64)__builtin_amdgcn_s_memrealtime(), __ATOMIC_RELAXED, __HIP_MEMORY_SCOPE_AGENT);
}

struct TinyArgs { char* ws; char* pad[12]; };
__global__ void k_tiny(TinyArgs ta) {
    char* ws = ta.ws;
    const u64 now = __builtin_amdgcn_s_memrealtime();
    if (blockIdx.x != 0 || threadIdx.x >= 64) return;
    u64 mx = 0;
    for (int i = threadIdx.x; i < 256; i += 64) { u64 r = __hip_atomic_load((GAS u64*)(ws + 0x3200000) + i, __ATOMIC_RELAXED, __HIP_MEMORY_SCOPE_AGENT); mx = r > mx ? r : mx; }
    for (int o = 32; o; o >>= 1) { u64 other = ((u64)__builtin_amdgcn_ds_bpermute(((threadIdx.x ^ o) << 2), (int)(mx >> 32)) << 32) | (u32)__builtin_amdgcn_ds_bpermute(((threadIdx.x ^ o) << 2), (int)(u32)mx); mx = other > mx ? other : mx; }
    long long d = (long long)(now - mx);
    if (d < 0) d = 0;
    if (d > 3000) d = 3000;
    while (__builtin_amdgcn_s_memrealtime() - now < (u64)(8 * d)) __builtin_amdgcn_s_sleep(4);
}
#if defined(__HIP_DEVICE_COMPILE__)
#pragma clang attribute pop
#endif

extern "C" void kernel_launch(void* const* d_in, const int* in_sizes, int n_in, void* d_out, int out_size, void* d_ws, size_t ws_size,
                              hipStream_t stream) {
    Params p;
    p.u = (const u16*)d_in[0]; p.lar = (const u16*)d_in[1]; p.aim = (const u16*)d_in[2]; p.cre = (const u16*)d_in[3];
    p.cim = (const u16*)d_in[4]; p.ldt = (const u16*)d_in[5]; p.dsk = (const u16*)d_in[6]; p.wqkv = (const u16*)d_in[8];
    p.wphi = (const u16*)d_in[9]; p.wo = (const u16*)d_in[10]; p.gw = (const u16*)d_in[11]; p.out = (u16*)d_out; p.ws = (char*)d_ws;
    (void)hipFuncSetAttribute((const void*)s4d_hedgehog_mix, hipFuncAttributeMaxDynamicSharedMemorySize, LDS_BYTES);
    s4d_hedgehog_mix<<<512, 512, LDS_BYTES, stream>>>(p);
    (void)hipFuncSetAttribute((const void*)k_tiny, hipFuncAttributeMaxDynamicSharedMemorySize, LDS_BYTES);
    { TinyArgs ta; ta.ws = p.ws; for (int i = 0; i < 12; ++i) ta.pad[i] = p.ws; k_tiny<<<POST_K, POST_T, POST_L, stream>>>(ta); }
}
```

```cpp
#include <hip/hip_runtime.h>
#include <hip/hip_bf16.h>
#include <stdint.h>

#if defined(__HIP_DEVICE_COMPILE__)
#pragma clang attribute push(__attribute__((target("no-packed-fp32-ops"))), apply_to = function)
#endif

typedef unsigned short u16;
typedef unsigned u32;
typedef unsigned long long u64;
typedef float f32x2 __attribute__((ext_vector_type(2)));
typedef float f32x4 __attribute__((ext_vector_type(4)));
typedef u32 u32x2 __attribute__((ext_vector_type(2)));
typedef u32 u32x4 __attribute__((ext_vector_type(4)));
typedef short s16x4 __attribute__((ext_vector_type(4)));
typedef __bf16 bf16x2 __attribute__((ext_vector_type(2)));
typedef __bf16 bf16x8 __attribute__((ext_vector_type(8)));
#define LAS __attribute__((address_space(3)))
#define GAS __attribute__((address_space(1)))
typedef LAS char* ldsp;
typedef __amdgpu_buffer_rsrc_t rsrc_t;
#define DEV __device__ __forceinline__

constexpr int L = 2048, D = 512;
constexpr int LDS_BYTES = 81920;
constexpr u64 TAG_STEP = 0x9E3779B97F4A7C15ull;
constexpr u32 SPIN_CAP = 40000;

constexpr u32 WS_E = 0, WS_TMO = 64;
constexpr u32 WS_FUT = 4096, WS_FKV = WS_FUT + 4096, WS_FY = WS_FKV + 4096, WS_FO = WS_FY + 4096;
constexpr u32 WS_G = 0x28000;
constexpr u32 WS_UT = 0x40000;
constexpr u32 WS_YT = WS_UT + 0x200000;
constexpr u32 WS_OP = WS_YT + 0x200000;
constexpr u32 WS_KVP = WS_OP + 0x200000;
constexpr u32 WS_ZP = WS_KVP + 0x100000;
constexpr u32 WS_DBG = WS_ZP + 0x10000;
constexpr u32 WS_USED = WS_DBG + 0x400000;

constexpr int AUX_SC1 = 16;

struct IdxTab { u16 v[2048]; };
constexpr IdxTab make_idx() {
    IdxTab t{};
    for (int i = 0; i < 2048; ++i) {
        int j = 2047 - i, p = j;
        int sh = j >= 1024 ? 3 : (j >= 512 ? 2 : (j >= 256 ? 1 : 0));
        if (sh) {
            int q = j >> sh, rem = j & ((1 << sh) - 1), half = 1 << (sh - 1);
            if (rem > half || (rem == half && (q & 1))) ++q;
            p = q << sh;
        }
        int row = 0, col = (p >> 3) & 15;
        if (p < 256) row = (p & 7) + 8 * (p >> 7);
        else if (p < 512) row = 16 + ((p - 256) >> 7) * 4 + ((p & 7) >> 1);
        else if (p < 1024) row = 24 + ((p - 512) >> 7) * 2 + ((p & 7) >> 2);
        else row = 32 + ((p - 1024) >> 7);
        t.v[i] = (u16)((row * 16 + col) * 2);
    }
    return t;
}
__device__ const IdxTab g_idx = make_idx();

DEV float u2f(u32 x) { return __builtin_bit_cast(float, x); }
DEV u32 f2u(float x) { return __builtin_bit_cast(u32, x); }
DEV float fexp(float x) { return __builtin_amdgcn_exp2f(x * 1.4426950408889634f); }
template <int CTRL> DEV float dpp_mov(float x) { return u2f(__builtin_amdgcn_update_dpp(0u, f2u(x), CTRL, 0xf, 0xf, true)); }
DEV float row16_sum(float x) {
    x += dpp_mov<0xB1>(x);
    x += dpp_mov<0x4E>(x);
    x += dpp_mov<0x141>(x);
    x += dpp_mov<0x140>(x);
    return x;
}
DEV u32 pk2(float lo, float hi) { f32x2 v = {lo, hi}; return __builtin_bit_cast(u32, __builtin_convertvector(v, bf16x2)); }
DEV float blo(u32 x) { return u2f(x << 16); }
DEV float bhi(u32 x) { return u2f(x & 0xffff0000u); }
DEV float rb(float f) { return blo(pk2(f, 0.f)); }
DEV f32x4 mfma32(u32x4 a, u32x4 b, f32x4 c) {
    return __builtin_amdgcn_mfma_f32_16x16x32_bf16(__builtin_bit_cast(bf16x8, a), __builtin_bit_cast(bf16x8, b), c, 0, 0, 0);
}
DEV f32x4 mfma16(u32x2 a, u32x2 b, f32x4 c) {
    return __builtin_amdgcn_mfma_f32_16x16x16bf16_1k(__builtin_bit_cast(s16x4, a), __builtin_bit_cast(s16x4, b), c, 0, 0, 0);
}
DEV u32x2 trread(ldsp p) { return __builtin_bit_cast(u32x2, __builtin_amdgcn_ds_read_tr16_b64_v4i16((LAS s16x4*)p)); }
DEV u32x4 lds_r128(ldsp p) { return *(LAS u32x4*)p; }
DEV u32x2 lds_r64(ldsp p) { return *(volatile LAS u32x2*)p; }
DEV void lds_w128(ldsp p, u32x4 v) { *(LAS u32x4*)p = v; }
DEV void lds_w64(ldsp p, u32x2 v) { *(LAS u32x2*)p = v; }
DEV void lds_w32(ldsp p, u32 v) { *(LAS u32*)p = v; }
DEV u32x4 cat(u32x2 a, u32x2 b) { u32x4 r = {a.x, a.y, b.x, b.y}; return r; }
DEV rsrc_t mkrsrc(const void* p, u32 bytes) { return __builtin_amdgcn_make_buffer_rsrc((void*)p, 0, bytes, 0x00020000); }
DEV u32x4 mkdesc(const void* p, u32 bytes) {
    u64 a = (u64)p;
    u32x4 d = {(u32)a, (u32)(a >> 32) & 0xffffu, bytes, 0x00020000u};
    return d;
}
DEV void dma16(u32x4 desc, ldsp dst, u32 voff, u32 soff) {
    asm volatile("s_mov_b32 m0, %0\n\ts_nop 0\n\tbuffer_load_dwordx4 %1, %2, %3 offen lds"
                 :: "s"((u32)(uintptr_t)dst), "v"(voff), "s"(desc), "s"(soff) : "memory");
}
DEV u32x4 ld128(rsrc_t r, u32 voff, u32 soff = 0) { return __builtin_amdgcn_raw_buffer_load_b128(r, voff, soff, 0); }
DEV u32x2 ld64(rsrc_t r, u32 voff, u32 soff = 0) { return __builtin_amdgcn_raw_buffer_load_b64(r, voff, soff, 0); }
DEV u32 ld16(rsrc_t r, u32 voff, u32 soff = 0) { return __builtin_amdgcn_raw_buffer_load_b16(r, voff, soff, 0); }
DEV u32x4 ld128c(rsrc_t r, u32 voff, u32 soff = 0) { return __builtin_amdgcn_raw_buffer_load_b128(r, voff, soff, AUX_SC1); }
DEV u32x2 ld64c(rsrc_t r, u32 voff, u32 soff = 0) { return __builtin_amdgcn_raw_buffer_load_b64(r, voff, soff, AUX_SC1); }
DEV u32 ld32c(rsrc_t r, u32 voff, u32 soff = 0) { return __builtin_amdgcn_raw_buffer_load_b32(r, voff, soff, AUX_SC1); }
DEV void st128c(rsrc_t r, u32x4 v, u32 voff, u32 soff = 0) { __builtin_amdgcn_raw_buffer_store_b128(v, r, voff, soff, AUX_SC1); }
DEV void st64c(rsrc_t r, u32x2 v, u32 voff, u32 soff = 0) { __builtin_amdgcn_raw_buffer_store_b64(v, r, voff, soff, AUX_SC1); }
#define WAIT_VM(n) asm volatile("s_waitcnt vmcnt(" #n ")" ::: "memory")
#define SCHED_A() __builtin_amdgcn_sched_barrier(0)
#define WAIT_LGKM0() asm volatile("s_waitcnt lgkmcnt(0)" ::: "memory")
DEV void bar() { asm volatile("s_waitcnt lgkmcnt(0)" ::: "memory"); __builtin_amdgcn_s_barrier(); asm volatile("" ::: "memory"); }
#define PRIO_A0 3
#define PRIO_A1 1
#define PRIO_BH2 3
#define OUT_AUX 0
#define PRIO_F 3
#define PRIO_BC 2
#define PRIO_BH 0
DEV u32 uni(u32 x) { return __builtin_amdgcn_readfirstlane(x); }

DEV float swap16_lo(float a, float b, float& other) {
    auto s = __builtin_amdgcn_permlane16_swap(f2u(a), f2u(b), false, false);
    other = u2f(s[1]);
    return u2f(s[0]);
}
DEV float swap32_lo(float a, float b, float& other) {
    auto s = __builtin_amdgcn_permlane32_swap(f2u(a), f2u(b), false, false);
    other = u2f(s[1]);
    return u2f(s[0]);
}
template <bool MAX> DEV void rows_reduce2(float& a, float& b) {
    float o, t = swap16_lo(a, b, o);
    t = MAX ? __builtin_fmaxf(t, o) : t + o;
    float u = swap32_lo(t, t, o);
    u = MAX ? __builtin_fmaxf(u, o) : u + o;
    a = swap16_lo(u, u, b);
}

typedef GAS u64 gu64;
DEV u64 make_tag(u64 e) {
    u32 lo = (u32)e, hi = (u32)(e >> 32);
    asm volatile("" : "+v"(lo), "+v"(hi));
    return (((u64)uni(hi) << 32) | uni(lo)) + TAG_STEP;
}
DEV u64 flag_ld(char* ws, u32 off) { return __hip_atomic_load((gu64*)(ws + off), __ATOMIC_RELAXED, __HIP_MEMORY_SCOPE_AGENT); }
DEV void flag_st(char* ws, u32 off, u64 v) { __hip_atomic_store((gu64*)(ws + off), v, __ATOMIC_RELAXED, __HIP_MEMORY_SCOPE_AGENT); }
DEV void wait_flags(char* ws, u32 off, u64 tag) {
#pragma unroll 1
    for (u32 spins = 0; spins < SPIN_CAP; ++spins) {
        u64 v = flag_ld(ws, off);
        if (__builtin_amdgcn_ballot_w64(v != tag) == 0) { asm volatile("" ::: "memory"); return; }
        __builtin_amdgcn_s_sleep(1);
    }
    flag_st(ws, WS_TMO, tag);
}
DEV void publish(char* ws, u32 off, u64 tag) {
    WAIT_VM(0);
    bar();
    if (threadIdx.x == 0) flag_st(ws, off, tag);
}

DEV void publish_counted(char* ws, u32 off, u64 tag, ldsp cnt, u32 n, int l) {
    WAIT_VM(0);
    u32 old = 0;
    if (l == 0) old = __hip_atomic_fetch_add((LAS u32*)cnt, 1u, __ATOMIC_RELAXED, __HIP_MEMORY_SCOPE_WORKGROUP);
    if (l == 0 && old == n - 1) flag_st(ws, off, tag);
}

#define STAMP(i) asm volatile("; MARK " #i)
#define FSTAMP(i) do { } while (0)
#define FS1(i) do { } while (0)
#define FS2(i) do { } while (0)
#define FS3(i) do { } while (0)
#define FS4(i) do { } while (0)
#define STAMP_ARG
#define STAMP_PASS

struct Params {
    char* ws; const u16* u; const u16* wqkv; const u16* wphi; const u16* lar; const u16* aim; const u16* cre; const u16* cim;
    const u16* ldt; const u16* gw; const u16* dsk; const u16* wo; u16* out;
};

struct cplx { float r, i; };
DEV cplx cmul(cplx a, cplx b) { cplx c; c.r = a.r * b.r - a.i * b.i; c.i = a.r * b.i + a.i * b.r; return c; }
DEV cplx cexp_p(float zr2, float zrev, float p) {
    float e = __builtin_amdgcn_exp2f(zr2 * p);
    float a = __builtin_amdgcn_fractf(zrev * p);
    cplx c; c.r = e * __builtin_amdgcn_cosf(a); c.i = e * __builtin_amdgcn_sinf(a); return c;
}

template <int NW> DEV void wo_prefetch(const Params& p, ldsp lds, int ct, int w, int l) {
    const u32x4 rwo = mkdesc(p.wo, D * D * 2);
    u32 voff = (l >> 3) * 1024 + ct * 128 + ((((l & 7) >> 1) ^ ((l >> 4) & 3)) * 32) + (l & 1) * 16;
#pragma unroll
    for (int i = 0; i < 64 / NW; ++i) {
        int piece = i * NW + w;
        dma16(rwo, lds + 16384 + piece * 1024, voff, piece * 8192);
    }
}

DEV u64 role_conv(const Params& p, ldsp lds, char* ws, u64 e, int ba, int w, int l STAMP_ARG) {
    const int t = threadIdx.x, xcd = ba & 7, j = ba >> 3, g = l >> 4, r16 = l & 15;
    const int a = xcd >> 2, c = xcd & 3;
    rsrc_t ru = mkrsrc(p.u, L * D * 2), rws = mkrsrc(ws, WS_USED);
    const int ch0 = 2 * (32 * xcd + j);
    __builtin_amdgcn_s_setprio(PRIO_A0);
    const int tb = 16 * a + (j >> 1), cg = 2 * c + (j & 1);
    {
        u32 voff = (tb * 64 + (l >> 3)) * 1024 + cg * 128 + (l & 7) * 16;
        dma16(mkdesc(p.u, L * D * 2), lds + 70656 + w * 1024, voff, w * 8192);
    }
    const int gtok = 1024 * a + 8 * (32 * c + j) + w;
    u32x4 gx = ld128(ru, gtok * 1024 + l * 16);
    u32x4 gwv = *(const GAS u32x4*)(p.gw + l * 8);
    const int kch = (t >> 7) & 1, kn = t & 127;
    const int pidx = (ch0 + kch) * 128 + kn;
    float lar = blo(p.lar[pidx]), aim = blo(p.aim[pidx]), cre = blo(p.cre[pidx]), cim = blo(p.cim[pidx]), ldt = blo(p.ldt[ch0 + kch]);
    u32x4 idx = *(const GAS u32x4*)(g_idx.v + (t & 255) * 8);
    SCHED_A();
    __builtin_amdgcn_s_setprio(PRIO_A1);
    WAIT_VM(0);
    const u64 tag = make_tag(e);
    bar();
    STAMP(1);
    {
        const int cw = w & 3, s = w >> 2;
        ldsp src = lds + 70656 + (32 * s + 8 * g + (r16 >> 2)) * 128 + 32 * cw + 8 * (l & 3);
        u32x4 v = cat(trread(src), trread(src + 4 * 128));
        st128c(rws, v, WS_UT + (cg * 64 + 16 * cw + r16) * 4096 + (tb * 64 + 32 * s + 8 * g) * 2);
    }
    {
        float s0 = 0.f, s1 = 0.f;
#pragma unroll
        for (int q = 0; q < 4; ++q) {
            u32 pr = pk2(blo(gx[q]) * blo(gwv[q]), bhi(gx[q]) * bhi(gwv[q]));
            s0 += blo(pr); s1 += bhi(pr);
        }
        s0 = row16_sum(s0 + s1);
        s1 = s0;
        rows_reduce2<false>(s0, s1);
        float g0 = rb(__builtin_amdgcn_rcpf(1.f + fexp(-rb(s0))));
        if (l == 0) __builtin_amdgcn_raw_buffer_store_b32(f2u(g0), rws, WS_G + gtok * 4, 0, AUX_SC1);
    }
    STAMP(2);
    {
        const float dt = rb(fexp(ldt));
        const float zr2 = -rb(fexp(lar)) * dt * 1.4426950408889634f, zrev = aim * dt * 0.15915494309189535f;
        ldsp T = lds + kch * 33792 + kn * 4;
        auto put = [&](int row, cplx v) { lds_w32(T + row * 528, pk2(v.r, v.i)); };
        const cplx w128 = cexp_p(zr2, zrev, 128.f);
        cplx G; G.r = 2.f * cre * dt; G.i = 2.f * cim * dt;
        if (w < 4) {
            cplx wp[8];
            wp[1] = cexp_p(zr2, zrev, 1.f); wp[2] = cmul(wp[1], wp[1]); wp[3] = cmul(wp[2], wp[1]); wp[4] = cmul(wp[2], wp[2]);
            wp[5] = cmul(wp[4], wp[1]); wp[6] = cmul(wp[4], wp[2]); wp[7] = cmul(wp[4], wp[3]);
#pragma unroll
            for (int aa = 0; aa < 4; ++aa) {
                if (aa == 1) { FS4(7); publish(ws, WS_FUT + cg * 256 + tb * 8, tag); FS4(8); }
                if (aa < 2) {
                    put(8 * aa, G);
#pragma unroll
                    for (int cc = 1; cc < 8; ++cc) put(8 * aa + cc, cmul(G, wp[cc]));
                } else {
                    put(16 + 4 * (aa - 2), G);
#pragma unroll
                    for (int cc = 1; cc < 4; ++cc) put(16 + 4 * (aa - 2) + cc, cmul(G, wp[2 * cc]));
                }
                if (aa < 3) G = cmul(G, w128);
            }
        } else {
            const cplx w4 = cexp_p(zr2, zrev, 4.f);
            G = cmul(G, cexp_p(zr2, zrev, 512.f));
#pragma unroll
            for (int aa = 4; aa <= 16; ++aa) {
                if (aa == 10) { FS4(7); publish(ws, WS_FUT + cg * 256 + tb * 8, tag); FS4(8); }
                if (aa < 8) {
                    put(24 + 2 * (aa - 4), G);
                    put(24 + 2 * (aa - 4) + 1, cmul(G, w4));
                } else put(32 + aa - 8, G);
                if (aa < 16) G = cmul(G, w128);
            }
            cplx B[9];
            B[1] = cmul(w4, w4); B[2] = cmul(B[1], B[1]); B[3] = cmul(B[2], B[1]); B[4] = cmul(B[2], B[2]);
            B[5] = cmul(B[4], B[1]); B[6] = cmul(B[4], B[2]); B[7] = cmul(B[4], B[3]); B[8] = cmul(B[4], B[4]);
            lds_w32(T + 48 * 528, 0x00003f80u);
#pragma unroll
            for (int b = 1; b < 16; ++b) {
                cplx v = b <= 8 ? B[b] : cmul(B[8], B[b - 8]);
                lds_w32(T + (48 + b) * 528, pk2(v.r, -v.i));
            }
        }
    }
    FS4(9);
    bar();
    FS4(10);
    if (w == 7) wait_flags(ws, WS_FUT + xcd * 256 + (l & 31) * 8, tag);
    if (w < 6) {
        const int ch = w >= 3, tile = w - 3 * ch;
        ldsp T = lds + ch * 33792 + r16 * 528 + g * 16;
        f32x4 k0 = {0, 0, 0, 0};
#pragma unroll
        for (int s = 0; s < 8; ++s) k0 = mfma32(lds_r128(T + tile * 16 * 528 + s * 64), lds_r128(T + 48 * 528 + s * 64), k0);
        ldsp KC = lds + 67584 + ch * 1536 + r16 * 2 + (tile * 16 + 4 * g) * 32;
        u32 a0 = pk2(k0[0], k0[1]), a1 = pk2(k0[2], k0[3]);
        *(LAS u16*)(KC) = (u16)a0; *(LAS u16*)(KC + 32) = (u16)(a0 >> 16);
        *(LAS u16*)(KC + 64) = (u16)a1; *(LAS u16*)(KC + 96) = (u16)(a1 >> 16);
    }
    bar();
    STAMP(3);
    const int hch = w >> 2, t8 = t & 255;
    const u32 uoff = WS_UT + (ch0 + hch) * 4096 + t8 * 16;
    u32x4 d0 = ld128c(rws, uoff), d1 = ld128c(rws, uoff + 4), d2 = ld128c(rws, uoff + 8);
    u32x2 f = ld64c(rws, WS_UT + (ch0 + hch) * 4096);
    SCHED_A();
    {
        ldsp KC = lds + 67584 + hch * 1536;
        u32x4 v;
#pragma unroll
        for (int q = 0; q < 4; ++q) {
            u32 lo = *(LAS u16*)(KC + (idx[q] & 0xffffu)), hi = *(LAS u16*)(KC + (idx[q] >> 16));
            v[q] = lo | (hi << 16);
        }
        lds_w128(lds + hch * 4608 + t8 * 16, v);
        if (t8 < 32) { u32x4 z = {0, 0, 0, 0}; lds_w128(lds + hch * 4608 + 4096 + t8 * 16, z); }
    }
    SCHED_A();
    STAMP(4);
    constexpr int UB = 9216, CS = 4160;
    {
        u32x4 v1, v3;
#pragma unroll
        for (int q = 0; q < 4; ++q) { v1[q] = __builtin_amdgcn_alignbit(d1[q], d0[q], 16); v3[q] = __builtin_amdgcn_alignbit(d2[q], d1[q], 16); }
        ldsp U = lds + UB + hch * 4 * CS + 32 + t8 * 16;
        lds_w128(U, d0); lds_w128(U + CS, v1); lds_w128(U + 2 * CS, d1); lds_w128(U + 3 * CS, v3);
        if (t8 < 4) {
            u32x4 z = {0, 0, 0, 0}, y = z;
            if (t8 == 1) y.w = f.x << 16;
            if (t8 == 2) y.w = f.x;
            if (t8 == 3) { y.z = f.x << 16; y.w = __builtin_amdgcn_alignbit(f.y, f.x, 16); }
            ldsp F = lds + UB + (hch * 4 + t8) * CS;
            lds_w128(F, z); lds_w128(F + 16, y);
        }
    }
    bar();
    STAMP(5);
    {
        const int mq = w & 3;
        const int s = (r16 + 1) & 3;
        ldsp A0 = lds + UB + (hch * 4 + s) * CS + 2 * (1 + r16 - s + 8 * g) + mq * 2 * 64;
        ldsp B0 = lds + hch * 4608 + 4064 - 3584 - 32 * r16 + 16 * g + mq * 2 * 64;
        f32x4 acc[8];
#pragma unroll
        for (int i = 0; i < 8; ++i) acc[i] = f32x4{0, 0, 0, 0};
#pragma unroll
        for (int mm = 0; mm < 2; ++mm) {
            u32x4 Af[8], Bf[8];
#pragma unroll
            for (int i = 0; i < 8; ++i) {
                Bf[i] = lds_r128(B0 + mm * 64 + (7 - i) * 512);
                Af[i] = cat(lds_r64(A0 + mm * 64 + i * 512), lds_r64(A0 + mm * 64 + i * 512 + 8));
            }
#pragma unroll
            for (int i = 0; i < 8; ++i)
#pragma unroll
                for (int cc = i; cc < 8; ++cc) acc[cc] = mfma32(Af[i], Bf[cc - i], acc[cc]);
            __builtin_amdgcn_sched_barrier(0);
        }
        FSTAMP(7);
        bar();
        FSTAMP(8);
        ldsp R = lds + hch * 24576 + l * 16;
#pragma unroll
        for (int cc = 0; cc < 8; ++cc)
            if ((cc >> 1) != mq) lds_w128(R + (cc * 3 + ((mq - (cc >> 1) - 1) & 3)) * 1024, __builtin_bit_cast(u32x4, acc[cc]));
        bar();
        FSTAMP(9);
#pragma unroll
        for (int q = 0; q < 4; ++q)
            if (q == mq) {
                u32 pk[2][2];
#pragma unroll
                for (int h = 0; h < 2; ++h) {
                    float v[4];
#pragma unroll
                    for (int r = 0; r < 4; ++r) v[r] = acc[2 * q + h][r];
#pragma unroll
                    for (int sl = 0; sl < 3; ++sl) {
                        u32x4 o = lds_r128(R + ((2 * q + h) * 3 + sl) * 1024);
#pragma unroll
                        for (int r = 0; r < 4; ++r) v[r] += u2f(o[r]);
                    }
                    pk[h][0] = pk2(v[0], v[1]); pk[h][1] = pk2(v[2], v[3]);
                }
                auto sx = __builtin_amdgcn_permlane16_swap(pk[0][0], pk[1][0], false, false);
                auto sy = __builtin_amdgcn_permlane16_swap(pk[0][1], pk[1][1], false, false);
                u32x4 v = {sx[0], sy[0], sx[1], sy[1]};
                st128c(rws, v, WS_YT + (ch0 + hch) * 4096 + (256 * (2 * q + (g & 1)) + 16 * r16 + 4 * (g & 2)) * 2);
            }
    }
    FSTAMP(10);
    bar();
    wo_prefetch<8>(p, lds, ba >> 5, w, l);
    WAIT_VM(8);
    bar();
    if (t == 0) flag_st(ws, WS_FY + xcd * 256 + j * 8, tag);
    STAMP(6);
    return tag;
}

typedef float f32x16 __attribute__((ext_vector_type(16)));
DEV f32x16 mfma32x32(u32x4 a, u32x4 b, f32x16 c) {
    return __builtin_amdgcn_mfma_f32_32x32x16_bf16(__builtin_bit_cast(bf16x8, a), __builtin_bit_cast(bf16x8, b), c, 0, 0, 0);
}
DEV float vmax3(float a, float b, float c) { float r; asm("v_max3_f32 %0, %1, %2, %3" : "=v"(r) : "v"(a), "v"(b), "v"(c)); return r; }
DEV float vmin3(float a, float b, float c) { float r; asm("v_min3_f32 %0, %1, %2, %3" : "=v"(r) : "v"(a), "v"(b), "v"(c)); return r; }

template <bool POS, bool NEG> DEV void feature_map(u32x4 x0, u32x4 x1, u32x4 wown, int l, u32 (&ph)[16]) {
    const bool odd = (l >> 2) & 1;
    u32x4 a0, a1;
#pragma unroll
    for (int q = 0; q < 4; ++q) { a0[q] = odd ? 0u : wown[q]; a1[q] = odd ? wown[q] : 0u; }
    f32x16 pr;
#pragma unroll
    for (int r = 0; r < 16; ++r) pr[r] = 0.f;
    pr = mfma32x32(a0, x0, pr);
    pr = mfma32x32(a1, x1, pr);
    float p[16];
#pragma unroll
    for (int r = 0; r < 16; r += 2) { u32 v = pk2(pr[r], pr[r + 1]); p[r] = blo(v); p[r + 1] = bhi(v); }
    constexpr float LG = 1.4426950408889634f;
#pragma unroll
    for (int sg = 0; sg < 2; ++sg) {
        if (sg == 0 ? !POS : !NEG) continue;
        float m = sg ? vmin3(p[0], p[1], p[2]) : vmax3(p[0], p[1], p[2]);
#pragma unroll
        for (int r = 3; r < 15; r += 2) m = sg ? vmin3(m, p[r], p[r + 1]) : vmax3(m, p[r], p[r + 1]);
        m = sg ? vmin3(m, p[15], p[15]) : vmax3(m, p[15], p[15]);
        const float sc = sg ? -LG : LG, c = -m * sc;
        float a[16];
#pragma unroll
        for (int r = 0; r < 16; ++r) a[r] = __builtin_fmaf(p[r], sc, c);
#pragma unroll
        for (int r = 0; r < 16; ++r) a[r] = __builtin_amdgcn_exp2f(a[r]);
        float sa[4];
#pragma unroll
        for (int q = 0; q < 4; ++q) sa[q] = (a[4 * q] + a[4 * q + 1]) + (a[4 * q + 2] + a[4 * q + 3]);
        const float ia = __builtin_amdgcn_rcpf((sa[0] + sa[1]) + (sa[2] + sa[3]));
#pragma unroll
        for (int r = 0; r < 8; ++r) ph[8 * sg + r] = pk2(a[2 * r] * ia, a[2 * r + 1] * ia);
    }
}

DEV void role_attn(const Params& p, ldsp lds, char* ws, u64 e, int bb, int w, int l STAMP_ARG) {
    const int xcd = bb & 7, j = bb >> 3, g = l >> 4, r16 = l & 15, h = l >> 5, w4 = w & 3;
    const int rb_ = 8 * (xcd >> 2) + (j >> 2), cb = 4 * (xcd & 3) + (j & 3);
    const int rot = (j + xcd) & 7;
    rsrc_t rws = mkrsrc(ws, WS_USED);
#define SCHED() __builtin_amdgcn_sched_barrier(0)
    constexpr int XCH = 32768, WPH = 49152, PH = 51200, PHS = 10240, VV = 71680, VS = 5120, FR = 0, CNT = 8192;
    const int tok = 32 * w4 + (l & 31);
    const u32 wph = WPH + (l & 1) * 512 + (4 * h + (r16 >> 2)) * 32 + (8 * (g & 1) + 4 * ((l & 3) >> 1)) * 2;
    u64 tag;
    if (w >= 4) {
        __builtin_amdgcn_s_setprio(PRIO_BH);
        const u32x4 ru = mkdesc(p.u, L * D * 2), rw = mkdesc(p.wqkv, D * 3 * D * 2);
        const u32 xv = (rb_ * 128 + w4 * 8 + (l >> 3)) * 1024 + (((l & 7) ^ (((w4 & 1) << 2) | ((l >> 4) & 3))) * 16);
        const u32 wv = (16 * w4 + (l >> 2)) * 3072 + cb * 64 + (l & 3) * 16;
        auto issue_x = [&](int s) {
#pragma unroll
            for (int i = 0; i < 4; ++i) dma16(ru, lds + (s % 3) * 16384 + (4 * i + w4) * 1024, xv, ((s + rot) & 7) * 128 + i * 32768);
        };
        auto issue_w = [&](int s, int sec) {
            dma16(rw, lds + 49152 + ((3 * s + sec) & 7) * 4096 + w4 * 1024, wv, ((s + rot) & 7) * (64 * 3072) + sec * 1024);
        };
        FS3(6);
        issue_w(0, 0); issue_w(0, 1); issue_w(0, 2); issue_x(0);
        FS3(7);
        __builtin_amdgcn_s_barrier();
        FS3(8);
        issue_w(1, 0); issue_w(1, 1); issue_w(1, 2); issue_x(1);
        FS3(9);
#pragma unroll
        for (int s = 0; s < 8; ++s) {
            if (s == 0) WAIT_VM(7); else if (s < 7) WAIT_VM(6); else WAIT_VM(0);
            if (s == 0) FS3(10);
            __builtin_amdgcn_s_barrier();
            if (s >= 1 && s + 1 < 8) issue_w(s + 1, 2);
            if (s + 2 < 8) { issue_x(s + 2); issue_w(s + 2, 0); issue_w(s + 2, 1); }
            if (s == 6 && w == 4) dma16(mkdesc(p.wphi, 32 * 256 * 2), lds + WPH, l * 16, cb * 1024);
            if (s == 7 && w == 4 && l < 2) lds_w32(lds + CNT + l * 4, 0u);
        }
        tag = make_tag(e);
        bar();
        ldsp X = lds + XCH + w4 * 4096 + l * 16;
        u32x4 wown = cat(trread(lds + wph), trread(lds + wph + 8 * 32));
        u32x4 q0 = lds_r128(X), q1 = lds_r128(X + 1024);
        bar();
        bar();
        u32 ph[16];
        feature_map<true, true>(q0, q1, wown, l, ph);
        u32x4 bq[2][2];
#pragma unroll
        for (int sg = 0; sg < 2; ++sg)
#pragma unroll
            for (int q = 0; q < 4; ++q) {
                auto sw = __builtin_amdgcn_permlane32_swap(ph[8 * sg + q], ph[8 * sg + q + 4], false, false);
                bq[0][sg][q] = sw[0]; bq[1][sg][q] = sw[1];
            }
        STAMP(3);
        __builtin_amdgcn_s_setprio(PRIO_BH2);
        bar();
#pragma unroll
        for (int hh = 0; hh < 2; ++hh) {
            f32x16 nd;
#pragma unroll
            for (int r = 0; r < 16; ++r) nd[r] = 0.f;
            nd = mfma32x32(lds_r128(lds + FR + (2 * hh) * 1024 + l * 16), bq[hh][0], nd);
            nd = mfma32x32(lds_r128(lds + FR + (2 * hh + 1) * 1024 + l * 16), bq[hh][1], nd);
            const float inv = __builtin_amdgcn_rcpf(rb(rb(nd[8]) + 1e-6f));
            u32x4 o;
#pragma unroll
            for (int q = 0; q < 4; ++q) { u32 n = pk2(nd[2 * q], nd[2 * q + 1]); o[q] = pk2(blo(n) * inv, bhi(n) * inv); }
            st128c(rws, o, WS_OP + (rb_ * 128 + tok) * 1024 + cb * 64 + hh * 32 + h * 16);
        }
        FS2(10);
        publish_counted(ws, WS_FO + rb_ * 256 + cb * 8, tag, lds + CNT + 4, 4, l);
        STAMP(5);
    } else {
        __builtin_amdgcn_s_setprio(PRIO_BC);
        f32x16 acc[3];
#pragma unroll
        for (int sec = 0; sec < 3; ++sec)
#pragma unroll
            for (int r = 0; r < 16; ++r) acc[sec][r] = 0.f;
        const u32 xa = tok * 128 + ((h ^ ((tok >> 1) & 7)) * 16);
        const u32 wa = 49152 + (8 * h + (r16 >> 2)) * 64 + (g & 1) * 32 + 8 * (l & 3);
        __builtin_amdgcn_s_barrier();
#pragma unroll
        for (int s = 0; s < 8; ++s) {
            __builtin_amdgcn_s_barrier();
            asm volatile("" ::: "memory");
            if (s == 0) FS1(6);
            if (s == 1) FS1(7);
            if (s == 2) FS1(8);
            if (s == 4) FS1(9);
            if (s == 6) FS1(10);
            u32x4 xf[4], wf[12];
            auto read_x = [&](int ks) { xf[ks] = lds_r128(lds + (s % 3) * 16384 + (xa ^ (32 * ks))); };
            auto read_w = [&](int i) {
                ldsp a = lds + wa + ((3 * s + i % 3) & 7) * 4096 + (i / 3) * 1024;
                wf[i] = cat(trread(a), trread(a + 256));
            };
            read_x(0); read_w(0); read_w(1); read_w(2);
            SCHED();
#pragma unroll
            for (int i = 0; i < 12; ++i) {
                if (i % 3 == 0 && i / 3 + 1 < 4) read_x(i / 3 + 1);
                if (i + 3 < 12) read_w(i + 3);
                SCHED();
                acc[i % 3] = mfma32x32(wf[i], xf[i / 3], acc[i % 3]);
                SCHED();
            }
        }
        u32x4 fr[3][2];
#pragma unroll
        for (int sec = 0; sec < 3; ++sec)
#pragma unroll
            for (int hh = 0; hh < 2; ++hh)
#pragma unroll
                for (int q = 0; q < 4; ++q) fr[sec][hh][q] = pk2(acc[sec][8 * hh + 2 * q], acc[sec][8 * hh + 2 * q + 1]);
        lds_w128(lds + XCH + w * 4096 + l * 16, fr[0][0]);
        lds_w128(lds + XCH + w * 4096 + 1024 + l * 16, fr[0][1]);
        tag = make_tag(e);
        bar();
        STAMP(1);
#pragma unroll
        for (int hh = 0; hh < 2; ++hh) {
            ldsp q = lds + VV + hh * VS + tok * 40 + 8 * h;
            lds_w64(q, u32x2{fr[2][hh].x, fr[2][hh].y});
            lds_w64(q + 16, u32x2{fr[2][hh].z, fr[2][hh].w});
        }
        {
            u32x4 wown = cat(trread(lds + wph), trread(lds + wph + 8 * 32));
            u32 ph[16];
            feature_map<true, true>(fr[1][0], fr[1][1], wown, l, ph);
            ldsp q = lds + PH + h * PHS + tok * 80;
#pragma unroll
            for (int i = 0; i < 4; ++i) lds_w128(q + i * 16, u32x4{ph[4 * i], ph[4 * i + 1], ph[4 * i + 2], ph[4 * i + 3]});
        }
        FS2(6);
        bar();
        FS2(7);
        const int hh = w >> 1, sg = w & 1;
        {
            f32x4 kv = {0, 0, 0, 0}, z = {0, 0, 0, 0};
            const u32x4 ones = {0x3f803f80u, 0x3f803f80u, 0x3f803f80u, 0x3f803f80u};
            ldsp pa = lds + PH + hh * PHS + (8 * g + (r16 >> 2)) * 80 + sg * 32 + 8 * (l & 3);
            ldsp pb = lds + VV + hh * VS + (8 * g + (r16 >> 2)) * 40 + 8 * (l & 3);
#pragma unroll
            for (int tg = 0; tg < 4; ++tg) {
                u32x4 A = cat(trread(pa + tg * 32 * 80), trread(pa + tg * 32 * 80 + 4 * 80));
                u32x4 B = cat(trread(pb + tg * 32 * 40), trread(pb + tg * 32 * 40 + 4 * 40));
                kv = mfma32(A, B, kv);
                z = mfma32(A, ones, z);
            }
            st128c(rws, __builtin_bit_cast(u32x4, kv), WS_KVP + ((cb * 16 + rb_) * 4 + w) * 1024 + l * 16);
            if (r16 == 0) st128c(rws, __builtin_bit_cast(u32x4, z), WS_ZP + ((cb * 16 + rb_) * 4 + w) * 64 + g * 16);
        }
        FS2(8);
        bar();
        publish_counted(ws, WS_FKV + cb * 256 + rb_ * 8, tag, lds + CNT, 4, l);
        STAMP(2);
        wait_flags(ws, WS_FKV + cb * 256 + r16 * 8, tag);
        STAMP(4);
        u32x4 part[16];
#pragma unroll
        for (int r = 0; r < 16; ++r) part[r] = ld128c(rws, WS_KVP + (cb * 16 * 4 + w) * 1024 + l * 16, r * 4096);
        u32x4 zp = ld128c(rws, WS_ZP + ((cb * 16 + (l >> 2)) * 4 + w) * 64 + (l & 3) * 16);
        float s[4] = {u2f(part[0].x), u2f(part[0].y), u2f(part[0].z), u2f(part[0].w)};
#pragma unroll
        for (int r = 1; r < 16; ++r)
#pragma unroll
            for (int q = 0; q < 4; ++q) s[q] += u2f(part[r][q]);
        float zs[4] = {u2f(zp.x), u2f(zp.y), u2f(zp.z), u2f(zp.w)};
#pragma unroll
        for (int q = 0; q < 4; ++q) {
            float x = zs[q];
            x += dpp_mov<0x124>(x);
            x += dpp_mov<0x128>(x);
            zs[q] = x;
        }
        rows_reduce2<false>(zs[0], zs[1]);
        rows_reduce2<false>(zs[2], zs[3]);
        FS2(9);
        ldsp F = lds + FR + w * 1024;
        lds_w64(F + (r16 + 32 * (g >> 1)) * 16 + 8 * (g & 1), u32x2{pk2(s[0], s[1]), pk2(s[2], s[3])});
        lds_w64(F + (16 + (l >> 2) + 32 * ((l & 3) >> 1)) * 16 + 8 * (l & 1), u32x2{pk2(zs[0], zs[1]), pk2(zs[2], zs[3])});
        bar();
    }
}

DEV void final_stage(const Params& p, ldsp lds, char* ws, u64 tag, int ba, int w, int l STAMP_ARG) {
    const int t = threadIdx.x, g = l >> 4, r16 = l & 15, cw = w & 3, th = w >> 2;
    const int tt64 = 4 * (ba & 7) + ((ba >> 3) & 3), ct = ba >> 5;
    rsrc_t ru = mkrsrc(p.u, L * D * 2), rws = mkrsrc(ws, WS_USED), rout = mkrsrc(p.out, L * D * 2);
    const int tok0 = tt64 * 64, chl = ct * 64 + 16 * cw + 4 * g, tok = tok0 + 32 * th + r16;
    constexpr int YL = 4096;
    u32x2 uu[2];
#pragma unroll
    for (int tt = 0; tt < 2; ++tt) uu[tt] = ld64(ru, (tok + 16 * tt) * 1024 + chl * 2);
    const u32x2 dsk = *(const GAS u32x2*)(p.dsk + chl);
    if (ba == 0 && w >= 4) {
        const int q = t - 256;
        wait_flags(ws, WS_FUT + (q >> 5) * 256 + (q & 31) * 8, tag);
        wait_flags(ws, WS_FKV + (q >> 4) * 256 + (q & 15) * 8, tag);
    }
    WAIT_VM(0);
    bar();
    if (ba == 0 && t == 0) flag_st(ws, WS_E, tag);
    u32x4 wf[16];
    {
        const int row = 16 * (g >> 1) + 4 * (g & 1) + (r16 >> 2);
        ldsp q = lds + 16384 + row * 128 + ((cw ^ ((row >> 1) & 3)) * 32) + 8 * (l & 3);
#pragma unroll
        for (int ks = 0; ks < 16; ++ks) wf[ks] = cat(trread(q + ks * 4096), trread(q + ks * 4096 + 1024));
    }
    if (w == 0) {
        const int tg8 = tt64 * 8 + (l & 7), ga = tg8 >> 7, gc = (tg8 >> 5) & 3, gj = tg8 & 31;
        const int gtb = 16 * ga + (gj >> 1), gcg = 2 * gc + (gj & 1);
        wait_flags(ws, l < 32 ? WS_FY + ct * 256 + l * 8 : WS_FUT + gcg * 256 + gtb * 8, tag);
    }
    bar();
    STAMP(8);
    float gate[2];
    {
        u32x4 y = ld128c(rws, WS_YT + (ct * 64 + (t >> 3)) * 4096 + tok0 * 2 + (t & 7) * 16);
#pragma unroll
        for (int tt = 0; tt < 2; ++tt) gate[tt] = u2f(ld32c(rws, WS_G + (tok + 16 * tt) * 4));
        __builtin_amdgcn_s_setprio(PRIO_F);
        if (w == 0) wait_flags(ws, WS_FO + (tok0 >> 7) * 256 + r16 * 8, tag);
        lds_w128(lds + YL + (t >> 3) * 128 + (t & 7) * 16, y);
    }
    bar();
    STAMP(10);
    u32x4 ov[8];
#pragma unroll
    for (int i = 0; i < 8; ++i) ov[i] = ld128c(rws, WS_OP + (tok0 + 8 * w) * 1024 + l * 16, i * 1024);
    __builtin_amdgcn_sched_barrier(0);
    float ga[2][4], g1[2];
#pragma unroll
    for (int tt = 0; tt < 2; ++tt) {
        u32x2 y = trread(lds + YL + (16 * cw + 4 * g + (r16 >> 2)) * 128 + (32 * th + 16 * tt) * 2 + 8 * (l & 3));
        const float gg = gate[tt];
        g1[tt] = rb(1.f - gg);
        u32 t0 = pk2(blo(uu[tt].x) * blo(dsk.x), bhi(uu[tt].x) * bhi(dsk.x)), t1 = pk2(blo(uu[tt].y) * blo(dsk.y), bhi(uu[tt].y) * bhi(dsk.y));
        u32 a0 = pk2(blo(y.x) + blo(t0), bhi(y.x) + bhi(t0)), a1 = pk2(blo(y.y) + blo(t1), bhi(y.y) + bhi(t1));
        u32 ga0 = pk2(gg * blo(a0), gg * bhi(a0)), ga1 = pk2(gg * blo(a1), gg * bhi(a1));
        ga[tt][0] = blo(ga0); ga[tt][1] = bhi(ga0); ga[tt][2] = blo(ga1); ga[tt][3] = bhi(ga1);
        asm volatile("" : "+v"(ga[tt][0]), "+v"(ga[tt][1]), "+v"(ga[tt][2]), "+v"(ga[tt][3]), "+v"(g1[tt]));
    }
    __builtin_amdgcn_sched_barrier(0);
#pragma unroll
    for (int i = 0; i < 8; ++i) {
        const int tk = 8 * w + i;
        lds_w128(lds + 16384 + tk * 1024 + ((l ^ (tk & 15)) * 16), ov[i]);
    }
    bar();
    STAMP(11);
    f32x4 acc[2] = {f32x4{0, 0, 0, 0}, f32x4{0, 0, 0, 0}};
    {
        u32 ob[4];
#pragma unroll
        for (int k4 = 0; k4 < 4; ++k4) ob[k4] = 16384 + (32 * th + r16) * 1024 + ((((4 * k4) ^ (r16 & 12)) | (g ^ (r16 & 3))) * 16);
#pragma unroll
        for (int ks = 0; ks < 16; ++ks)
#pragma unroll
            for (int tt = 0; tt < 2; ++tt)
                acc[tt] = mfma32(wf[ks], lds_r128(lds + ob[ks & 3] + (ks >> 2) * 256 + tt * 16384), acc[tt]);
    }
#pragma unroll
    for (int tt = 0; tt < 2; ++tt) {
        u32x2 o = {pk2(__builtin_fmaf(g1[tt], acc[tt][0], ga[tt][0]), __builtin_fmaf(g1[tt], acc[tt][1], ga[tt][1])),
                   pk2(__builtin_fmaf(g1[tt], acc[tt][2], ga[tt][2]), __builtin_fmaf(g1[tt], acc[tt][3], ga[tt][3]))};
        __builtin_amdgcn_raw_buffer_store_b64(o, rout, (tok + 16 * tt) * 1024 + chl * 2, 0, OUT_AUX);
    }
    STAMP(12);
}

__global__ __launch_bounds__(512, 4) void s4d_hedgehog_mix(Params p) {
    extern __shared__ __attribute__((aligned(16))) char smem[];
    ldsp lds = (ldsp)smem;
    char* ws = p.ws;
    const int b = blockIdx.x, l = threadIdx.x & 63, w = uni(threadIdx.x >> 6);
    STAMP(0);
    asm volatile("" :: "s"(p.ws), "s"(p.u), "s"(p.wqkv), "s"(p.ldt), "s"(p.gw));
    const u64 e = flag_ld(ws, WS_E);
    if (b >= 256) {
        const u64 tag = role_conv(p, lds, ws, e, b - 256, w, l STAMP_PASS);
        final_stage(p, lds, ws, tag, b - 256, w, l STAMP_PASS);
    } else {
        role_attn(p, lds, ws, e, b, w, l STAMP_PASS);
    }
}

#if defined(__HIP_DEVICE_COMPILE__)
#pragma clang attribute pop
#endif

extern "C" void kernel_launch(void* const* d_in, const int* in_sizes, int n_in, void* d_out, int out_size, void* d_ws, size_t ws_size,
                              hipStream_t stream) {
    Params p;
    p.u = (const u16*)d_in[0]; p.lar = (const u16*)d_in[1]; p.aim = (const u16*)d_in[2]; p.cre = (const u16*)d_in[3];
    p.cim = (const u16*)d_in[4]; p.ldt = (const u16*)d_in[5]; p.dsk = (const u16*)d_in[6]; p.wqkv = (const u16*)d_in[8];
    p.wphi = (const u16*)d_in[9]; p.wo = (const u16*)d_in[10]; p.gw = (const u16*)d_in[11]; p.out = (u16*)d_out; p.ws = (char*)d_ws;
    (void)hipFuncSetAttribute((const void*)s4d_hedgehog_mix, hipFuncAttributeMaxDynamicSharedMemorySize, LDS_BYTES);
    s4d_hedgehog_mix<<<512, 512, LDS_BYTES, stream>>>(p);
}
```

```cpp
#include <hip/hip_runtime.h>
#include <hip/hip_bf16.h>
#include <stdint.h>

#if defined(__HIP_DEVICE_COMPILE__)
#pragma clang attribute push(__attribute__((target("no-packed-fp32-ops"))), apply_to = function)
#endif

typedef unsigned short u16;
typedef unsigned u32;
typedef unsigned long long u64;
typedef float f32x2 __attribute__((ext_vector_type(2)));
typedef float f32x4 __attribute__((ext_vector_type(4)));
typedef u32 u32x2 __attribute__((ext_vector_type(2)));
typedef u32 u32x4 __attribute__((ext_vector_type(4)));
typedef short s16x4 __attribute__((ext_vector_type(4)));
typedef __bf16 bf16x2 __attribute__((ext_vector_type(2)));
typedef __bf16 bf16x8 __attribute__((ext_vector_type(8)));
#define LAS __attribute__((address_space(3)))
#define GAS __attribute__((address_space(1)))
typedef LAS char* ldsp;
typedef __amdgpu_buffer_rsrc_t rsrc_t;
#define DEV __device__ __forceinline__

constexpr int L = 2048, D = 512;
constexpr int LDS_BYTES = 81920;
constexpr u64 TAG_STEP = 0x9E3779B97F4A7C15ull;
constexpr u32 SPIN_CAP = 40000;

constexpr u32 WS_E = 0, WS_TMO = 64;
constexpr u32 WS_FUT = 4096, WS_FRD = WS_FUT + 4096, WS_FY = WS_FRD + 4096, WS_FO = WS_FY + 4096;
constexpr u32 WS_G = 0x28000;
constexpr u32 WS_UT = 0x40000;
constexpr u32 WS_YT = WS_UT + 0x200000;
constexpr u32 WS_OP = WS_YT + 0x200000;
constexpr u32 WS_KVP = WS_OP + 0x200000;
constexpr u32 WS_ZP = WS_KVP + 0x100000;
constexpr u32 WS_DBG = WS_ZP + 0x10000;
constexpr u32 WS_USED = WS_DBG + 0x400000;

constexpr int AUX_SC1 = 16;

struct IdxTab { u16 v[2048]; };
constexpr IdxTab make_idx() {
    IdxTab t{};
    for (int i = 0; i < 2048; ++i) {
        int j = 2047 - i, p = j;
        int sh = j >= 1024 ? 3 : (j >= 512 ? 2 : (j >= 256 ? 1 : 0));
        if (sh) {
            int q = j >> sh, rem = j & ((1 << sh) - 1), half = 1 << (sh - 1);
            if (rem > half || (rem == half && (q & 1))) ++q;
            p = q << sh;
        }
        int row = 0, col = (p >> 3) & 15;
        if (p < 256) row = (p & 7) + 8 * (p >> 7);
        else if (p < 512) row = 16 + ((p - 256) >> 7) * 4 + ((p & 7) >> 1);
        else if (p < 1024) row = 24 + ((p - 512) >> 7) * 2 + ((p & 7) >> 2);
        else row = 32 + ((p - 1024) >> 7);
        t.v[i] = (u16)((row * 16 + col) * 2);
    }
    return t;
}
__device__ const IdxTab g_idx = make_idx();

DEV float u2f(u32 x) { return __builtin_bit_cast(float, x); }
DEV u32 f2u(float x) { return __builtin_bit_cast(u32, x); }
DEV float fexp(float x) { return __builtin_amdgcn_exp2f(x * 1.4426950408889634f); }
template <int CTRL> DEV float dpp_mov(float x) { return u2f(__builtin_amdgcn_update_dpp(0u, f2u(x), CTRL, 0xf, 0xf, true)); }
DEV float row16_sum(float x) {
    x += dpp_mov<0xB1>(x);
    x += dpp_mov<0x4E>(x);
    x += dpp_mov<0x141>(x);
    x += dpp_mov<0x140>(x);
    return x;
}
DEV u32 pk2(float lo, float hi) { f32x2 v = {lo, hi}; return __builtin_bit_cast(u32, __builtin_convertvector(v, bf16x2)); }
DEV float blo(u32 x) { return u2f(x << 16); }
DEV float bhi(u32 x) { return u2f(x & 0xffff0000u); }
DEV float rb(float f) { return blo(pk2(f, 0.f)); }
DEV f32x4 mfma32(u32x4 a, u32x4 b, f32x4 c) {
    return __builtin_amdgcn_mfma_f32_16x16x32_bf16(__builtin_bit_cast(bf16x8, a), __builtin_bit_cast(bf16x8, b), c, 0, 0, 0);
}
DEV f32x4 mfma16(u32x2 a, u32x2 b, f32x4 c) {
    return __builtin_amdgcn_mfma_f32_16x16x16bf16_1k(__builtin_bit_cast(s16x4, a), __builtin_bit_cast(s16x4, b), c, 0, 0, 0);
}
DEV u32x2 trread(ldsp p) { return __builtin_bit_cast(u32x2, __builtin_amdgcn_ds_read_tr16_b64_v4i16((LAS s16x4*)p)); }
DEV u32x4 lds_r128(ldsp p) { return *(LAS u32x4*)p; }
DEV u32x2 lds_r64(ldsp p) { return *(volatile LAS u32x2*)p; }
DEV void lds_w128(ldsp p, u32x4 v) { *(LAS u32x4*)p = v; }
DEV void lds_w64(ldsp p, u32x2 v) { *(LAS u32x2*)p = v; }
DEV void lds_w32(ldsp p, u32 v) { *(LAS u32*)p = v; }
DEV u32x4 cat(u32x2 a, u32x2 b) { u32x4 r = {a.x, a.y, b.x, b.y}; return r; }
DEV rsrc_t mkrsrc(const void* p, u32 bytes) { return __builtin_amdgcn_make_buffer_rsrc((void*)p, 0, bytes, 0x00020000); }
DEV u32x4 mkdesc(const void* p, u32 bytes) {
    u64 a = (u64)p;
    u32x4 d = {(u32)a, (u32)(a >> 32) & 0xffffu, bytes, 0x00020000u};
    return d;
}
DEV void dma16(u32x4 desc, ldsp dst, u32 voff, u32 soff) {
    asm volatile("s_mov_b32 m0, %0\n\ts_nop 0\n\tbuffer_load_dwordx4 %1, %2, %3 offen lds"
                 :: "s"((u32)(uintptr_t)dst), "v"(voff), "s"(desc), "s"(soff) : "memory");
}
DEV void dma16c(u32x4 desc, ldsp dst, u32 voff, u32 soff) {
    asm volatile("s_mov_b32 m0, %0\n\ts_nop 0\n\tbuffer_load_dwordx4 %1, %2, %3 offen sc1 lds"
                 :: "s"((u32)(uintptr_t)dst), "v"(voff), "s"(desc), "s"(soff) : "memory");
}
DEV u32x4 ld128(rsrc_t r, u32 voff, u32 soff = 0) { return __builtin_amdgcn_raw_buffer_load_b128(r, voff, soff, 0); }
DEV u32x2 ld64(rsrc_t r, u32 voff, u32 soff = 0) { return __builtin_amdgcn_raw_buffer_load_b64(r, voff, soff, 0); }
DEV u32 ld16(rsrc_t r, u32 voff, u32 soff = 0) { return __builtin_amdgcn_raw_buffer_load_b16(r, voff, soff, 0); }
DEV u32x4 ld128c(rsrc_t r, u32 voff, u32 soff = 0) { return __builtin_amdgcn_raw_buffer_load_b128(r, voff, soff, AUX_SC1); }
DEV u32x2 ld64c(rsrc_t r, u32 voff, u32 soff = 0) { return __builtin_amdgcn_raw_buffer_load_b64(r, voff, soff, AUX_SC1); }
DEV u32 ld32c(rsrc_t r, u32 voff, u32 soff = 0) { return __builtin_amdgcn_raw_buffer_load_b32(r, voff, soff, AUX_SC1); }
DEV void st128c(rsrc_t r, u32x4 v, u32 voff, u32 soff = 0) { __builtin_amdgcn_raw_buffer_store_b128(v, r, voff, soff, AUX_SC1); }
DEV void st64c(rsrc_t r, u32x2 v, u32 voff, u32 soff = 0) { __builtin_amdgcn_raw_buffer_store_b64(v, r, voff, soff, AUX_SC1); }
#define WAIT_VM(n) asm volatile("s_waitcnt vmcnt(" #n ")" ::: "memory")
#define SCHED_A() __builtin_amdgcn_sched_barrier(0)
#define WAIT_LGKM0() asm volatile("s_waitcnt lgkmcnt(0)" ::: "memory")
DEV void bar() { asm volatile("s_waitcnt lgkmcnt(0)" ::: "memory"); __builtin_amdgcn_s_barrier(); asm volatile("" ::: "memory"); }
#define PRIO_A0 3
#define PRIO_A1 1
#define PRIO_BH2 3
#define OUT_AUX 0
#define PRIO_A2 1
#define PRIO_BQ 2
#define PRIO_B2 2
#define PRIO_F 3
#define PRIO_BC 2
#define PRIO_BH 0
DEV u32 uni(u32 x) { return __builtin_amdgcn_readfirstlane(x); }

DEV float swap16_lo(float a, float b, float& other) {
    auto s = __builtin_amdgcn_permlane16_swap(f2u(a), f2u(b), false, false);
    other = u2f(s[1]);
    return u2f(s[0]);
}
DEV float swap32_lo(float a, float b, float& other) {
    auto s = __builtin_amdgcn_permlane32_swap(f2u(a), f2u(b), false, false);
    other = u2f(s[1]);
    return u2f(s[0]);
}
template <bool MAX> DEV void rows_reduce2(float& a, float& b) {
    float o, t = swap16_lo(a, b, o);
    t = MAX ? __builtin_fmaxf(t, o) : t + o;
    float u = swap32_lo(t, t, o);
    u = MAX ? __builtin_fmaxf(u, o) : u + o;
    a = swap16_lo(u, u, b);
}

typedef GAS u64 gu64;
DEV u64 make_tag(u64 e) {
    u32 lo = (u32)e, hi = (u32)(e >> 32);
    asm volatile("" : "+v"(lo), "+v"(hi));
    return (((u64)uni(hi) << 32) | uni(lo)) + TAG_STEP;
}
DEV u64 flag_ld(char* ws, u32 off) { return __hip_atomic_load((gu64*)(ws + off), __ATOMIC_RELAXED, __HIP_MEMORY_SCOPE_AGENT); }
DEV void flag_st(char* ws, u32 off, u64 v) { __hip_atomic_store((gu64*)(ws + off), v, __ATOMIC_RELAXED, __HIP_MEMORY_SCOPE_AGENT); }
DEV void wait_flags(char* ws, u32 off, u64 tag) {
#pragma unroll 1
    for (u32 spins = 0; spins < SPIN_CAP; ++spins) {
        u64 v = flag_ld(ws, off);
        if (__builtin_amdgcn_ballot_w64(v != tag) == 0) { asm volatile("" ::: "memory"); return; }
        __builtin_amdgcn_s_sleep(1);
    }
    flag_st(ws, WS_TMO, tag);
}
DEV void publish(char* ws, u32 off, u64 tag) {
    WAIT_VM(0);
    bar();
    if (threadIdx.x == 0) flag_st(ws, off, tag);
}

DEV void publish_counted(char* ws, u32 off, u64 tag, ldsp cnt, u32 n, int l) {
    WAIT_VM(0);
    u32 old = 0;
    if (l == 0) old = __hip_atomic_fetch_add((LAS u32*)cnt, 1u, __ATOMIC_RELAXED, __HIP_MEMORY_SCOPE_WORKGROUP);
    if (l == 0 && old == n - 1) flag_st(ws, off, tag);
}

#define STAMP(i) asm volatile("; MARK " #i)
#define FSTAMP(i) do { } while (0)
#define FS1(i) do { } while (0)
#define FS2(i) do { } while (0)
#define FS3(i) do { } while (0)
#define FS4(i) do { } while (0)
#define STAMP_ARG
#define STAMP_PASS

struct Params {
    char* ws; const u16* u; const u16* wqkv; const u16* wphi; const u16* lar; const u16* aim; const u16* cre; const u16* cim;
    const u16* ldt; const u16* gw; const u16* dsk; const u16* wo; u16* out;
};

struct cplx { float r, i; };
DEV cplx cmul(cplx a, cplx b) { cplx c; c.r = a.r * b.r - a.i * b.i; c.i = a.r * b.i + a.i * b.r; return c; }
DEV cplx cexp_p(float zr2, float zrev, float p) {
    float e = __builtin_amdgcn_exp2f(zr2 * p);
    float a = __builtin_amdgcn_fractf(zrev * p);
    cplx c; c.r = e * __builtin_amdgcn_cosf(a); c.i = e * __builtin_amdgcn_sinf(a); return c;
}

template <int NW> DEV void wo_prefetch(const Params& p, ldsp lds, int ct, int w, int l) {
    const u32x4 rwo = mkdesc(p.wo, D * D * 2);
    u32 voff = (l >> 3) * 1024 + ct * 128 + ((((l & 7) >> 1) ^ ((l >> 4) & 3)) * 32) + (l & 1) * 16;
#pragma unroll
    for (int i = 0; i < 64 / NW; ++i) {
        int piece = i * NW + w;
        dma16(rwo, lds + 16384 + piece * 1024, voff, piece * 8192);
    }
}

DEV u64 role_conv(const Params& p, ldsp lds, char* ws, u64 e, int ba, int w, int l STAMP_ARG) {
    const int t = threadIdx.x, xcd = ba & 7, j = ba >> 3, g = l >> 4, r16 = l & 15;
    const int a = xcd >> 2, c = xcd & 3;
    rsrc_t ru = mkrsrc(p.u, L * D * 2), rws = mkrsrc(ws, WS_USED);
    const int ch0 = 2 * (32 * xcd + j);
    __builtin_amdgcn_s_setprio(PRIO_A0);
    const int tb = 16 * a + (j >> 1), cg = 2 * c + (j & 1);
    {
        u32 voff = (tb * 64 + (l >> 3)) * 1024 + cg * 128 + (l & 7) * 16;
        dma16(mkdesc(p.u, L * D * 2), lds + 70656 + w * 1024, voff, w * 8192);
    }
    const int gtok = 1024 * a + 8 * (32 * c + j) + w;
    u32x4 gx = ld128(ru, gtok * 1024 + l * 16);
    u32x4 gwv = *(const GAS u32x4*)(p.gw + l * 8);
    const int kch = (t >> 7) & 1, kn = t & 127;
    const int pidx = (ch0 + kch) * 128 + kn;
    float lar = blo(p.lar[pidx]), aim = blo(p.aim[pidx]), cre = blo(p.cre[pidx]), cim = blo(p.cim[pidx]), ldt = blo(p.ldt[ch0 + kch]);
    u32x4 idx = *(const GAS u32x4*)(g_idx.v + (t & 255) * 8);
    SCHED_A();
    __builtin_amdgcn_s_setprio(PRIO_A1);
    WAIT_VM(0);
    const u64 tag = make_tag(e);
    bar();
    STAMP(1);
    {
        const int cw = w & 3, s = w >> 2;
        ldsp src = lds + 70656 + (32 * s + 8 * g + (r16 >> 2)) * 128 + 32 * cw + 8 * (l & 3);
        u32x4 v = cat(trread(src), trread(src + 4 * 128));
        st128c(rws, v, WS_UT + (cg * 64 + 16 * cw + r16) * 4096 + (tb * 64 + 32 * s + 8 * g) * 2);
    }
    {
        float s0 = 0.f, s1 = 0.f;
#pragma unroll
        for (int q = 0; q < 4; ++q) {
            u32 pr = pk2(blo(gx[q]) * blo(gwv[q]), bhi(gx[q]) * bhi(gwv[q]));
            s0 += blo(pr); s1 += bhi(pr);
        }
        s0 = row16_sum(s0 + s1);
        s1 = s0;
        rows_reduce2<false>(s0, s1);
        float g0 = rb(__builtin_amdgcn_rcpf(1.f + fexp(-rb(s0))));
        if (l == 0) __builtin_amdgcn_raw_buffer_store_b32(f2u(g0), rws, WS_G + gtok * 4, 0, AUX_SC1);
    }
    STAMP(2);
    {
        const float dt = rb(fexp(ldt));
        const float zr2 = -rb(fexp(lar)) * dt * 1.4426950408889634f, zrev = aim * dt * 0.15915494309189535f;
        ldsp T = lds + kch * 33792 + kn * 4;
        auto put = [&](int row, cplx v) { lds_w32(T + row * 528, pk2(v.r, v.i)); };
        const cplx w128 = cexp_p(zr2, zrev, 128.f);
        cplx G; G.r = 2.f * cre * dt; G.i = 2.f * cim * dt;
        if (w < 4) {
            cplx wp[8];
            wp[1] = cexp_p(zr2, zrev, 1.f); wp[2] = cmul(wp[1], wp[1]); wp[3] = cmul(wp[2], wp[1]); wp[4] = cmul(wp[2], wp[2]);
            wp[5] = cmul(wp[4], wp[1]); wp[6] = cmul(wp[4], wp[2]); wp[7] = cmul(wp[4], wp[3]);
#pragma unroll
            for (int aa = 0; aa < 4; ++aa) {
                if (aa == 1) { FS4(7); publish(ws, WS_FUT + cg * 256 + tb * 8, tag); FS4(8); }
                if (aa < 2) {
                    put(8 * aa, G);
#pragma unroll
                    for (int cc = 1; cc < 8; ++cc) put(8 * aa + cc, cmul(G, wp[cc]));
                } else {
                    put(16 + 4 * (aa - 2), G);
#pragma unroll
                    for (int cc = 1; cc < 4; ++cc) put(16 + 4 * (aa - 2) + cc, cmul(G, wp[2 * cc]));
                }
                if (aa < 3) G = cmul(G, w128);
            }
        } else {
            const cplx w4 = cexp_p(zr2, zrev, 4.f);
            G = cmul(G, cexp_p(zr2, zrev, 512.f));
#pragma unroll
            for (int aa = 4; aa <= 16; ++aa) {
                if (aa == 10) { FS4(7); publish(ws, WS_FUT + cg * 256 + tb * 8, tag); FS4(8); }
                if (aa < 8) {
                    put(24 + 2 * (aa - 4), G);
                    put(24 + 2 * (aa - 4) + 1, cmul(G, w4));
                } else put(32 + aa - 8, G);
                if (aa < 16) G = cmul(G, w128);
            }
            cplx B[9];
            B[1] = cmul(w4, w4); B[2] = cmul(B[1], B[1]); B[3] = cmul(B[2], B[1]); B[4] = cmul(B[2], B[2]);
            B[5] = cmul(B[4], B[1]); B[6] = cmul(B[4], B[2]); B[7] = cmul(B[4], B[3]); B[8] = cmul(B[4], B[4]);
            lds_w32(T + 48 * 528, 0x00003f80u);
#pragma unroll
            for (int b = 1; b < 16; ++b) {
                cplx v = b <= 8 ? B[b] : cmul(B[8], B[b - 8]);
                lds_w32(T + (48 + b) * 528, pk2(v.r, -v.i));
            }
        }
    }
    FS4(9);
    bar();
    FS4(10);
    if (w == 7) wait_flags(ws, WS_FUT + xcd * 256 + (l & 31) * 8, tag);
    if (w < 6) {
        const int ch = w >= 3, tile = w - 3 * ch;
        ldsp T = lds + ch * 33792 + r16 * 528 + g * 16;
        f32x4 k0 = {0, 0, 0, 0};
#pragma unroll
        for (int s = 0; s < 8; ++s) k0 = mfma32(lds_r128(T + tile * 16 * 528 + s * 64), lds_r128(T + 48 * 528 + s * 64), k0);
        ldsp KC = lds + 67584 + ch * 1536 + r16 * 2 + (tile * 16 + 4 * g) * 32;
        u32 a0 = pk2(k0[0], k0[1]), a1 = pk2(k0[2], k0[3]);
        *(LAS u16*)(KC) = (u16)a0; *(LAS u16*)(KC + 32) = (u16)(a0 >> 16);
        *(LAS u16*)(KC + 64) = (u16)a1; *(LAS u16*)(KC + 96) = (u16)(a1 >> 16);
    }
    bar();
    STAMP(3);
    const int hch = w >> 2, t8 = t & 255;
    const u32 uoff = WS_UT + (ch0 + hch) * 4096 + t8 * 16;
    u32x4 d0 = ld128c(rws, uoff), d1 = ld128c(rws, uoff + 4), d2 = ld128c(rws, uoff + 8);
    u32x2 f = ld64c(rws, WS_UT + (ch0 + hch) * 4096);
    SCHED_A();
    {
        ldsp KC = lds + 67584 + hch * 1536;
        u32x4 v;
#pragma unroll
        for (int q = 0; q < 4; ++q) {
            u32 lo = *(LAS u16*)(KC + (idx[q] & 0xffffu)), hi = *(LAS u16*)(KC + (idx[q] >> 16));
            v[q] = lo | (hi << 16);
        }
        lds_w128(lds + hch * 4608 + t8 * 16, v);
        if (t8 < 32) { u32x4 z = {0, 0, 0, 0}; lds_w128(lds + hch * 4608 + 4096 + t8 * 16, z); }
    }
    SCHED_A();
    STAMP(4);
    constexpr int UB = 9216, CS = 4160;
    {
        u32x4 v1, v3;
#pragma unroll
        for (int q = 0; q < 4; ++q) { v1[q] = __builtin_amdgcn_alignbit(d1[q], d0[q], 16); v3[q] = __builtin_amdgcn_alignbit(d2[q], d1[q], 16); }
        ldsp U = lds + UB + hch * 4 * CS + 32 + t8 * 16;
        lds_w128(U, d0); lds_w128(U + CS, v1); lds_w128(U + 2 * CS, d1); lds_w128(U + 3 * CS, v3);
        if (t8 < 4) {
            u32x4 z = {0, 0, 0, 0}, y = z;
            if (t8 == 1) y.w = f.x << 16;
            if (t8 == 2) y.w = f.x;
            if (t8 == 3) { y.z = f.x << 16; y.w = __builtin_amdgcn_alignbit(f.y, f.x, 16); }
            ldsp F = lds + UB + (hch * 4 + t8) * CS;
            lds_w128(F, z); lds_w128(F + 16, y);
        }
    }
    bar();
    STAMP(5);
    __builtin_amdgcn_s_setprio(PRIO_A2);
    {
        const int mq = w & 3;
        const int s = (r16 + 1) & 3;
        ldsp A0 = lds + UB + (hch * 4 + s) * CS + 2 * (1 + r16 - s + 8 * g) + mq * 2 * 64;
        ldsp B0 = lds + hch * 4608 + 4064 - 3584 - 32 * r16 + 16 * g + mq * 2 * 64;
        f32x4 acc[8];
#pragma unroll
        for (int i = 0; i < 8; ++i) acc[i] = f32x4{0, 0, 0, 0};
#pragma unroll
        for (int mm = 0; mm < 2; ++mm) {
            u32x4 Af[8], Bf[8];
#pragma unroll
            for (int i = 0; i < 8; ++i) {
                Bf[i] = lds_r128(B0 + mm * 64 + (7 - i) * 512);
                Af[i] = cat(lds_r64(A0 + mm * 64 + i * 512), lds_r64(A0 + mm * 64 + i * 512 + 8));
            }
#pragma unroll
            for (int i = 0; i < 8; ++i)
#pragma unroll
                for (int cc = i; cc < 8; ++cc) acc[cc] = mfma32(Af[i], Bf[cc - i], acc[cc]);
            __builtin_amdgcn_sched_barrier(0);
        }
        FSTAMP(7);
        bar();
        FSTAMP(8);
        ldsp R = lds + hch * 24576 + l * 16;
#pragma unroll
        for (int cc = 0; cc < 8; ++cc)
            if ((cc >> 1) != mq) lds_w128(R + (cc * 3 + ((mq - (cc >> 1) - 1) & 3)) * 1024, __builtin_bit_cast(u32x4, acc[cc]));
        bar();
        FSTAMP(9);
#pragma unroll
        for (int q = 0; q < 4; ++q)
            if (q == mq) {
                u32 pk[2][2];
#pragma unroll
                for (int h = 0; h < 2; ++h) {
                    float v[4];
#pragma unroll
                    for (int r = 0; r < 4; ++r) v[r] = acc[2 * q + h][r];
#pragma unroll
                    for (int sl = 0; sl < 3; ++sl) {
                        u32x4 o = lds_r128(R + ((2 * q + h) * 3 + sl) * 1024);
#pragma unroll
                        for (int r = 0; r < 4; ++r) v[r] += u2f(o[r]);
                    }
                    pk[h][0] = pk2(v[0], v[1]); pk[h][1] = pk2(v[2], v[3]);
                }
                auto sx = __builtin_amdgcn_permlane16_swap(pk[0][0], pk[1][0], false, false);
                auto sy = __builtin_amdgcn_permlane16_swap(pk[0][1], pk[1][1], false, false);
                u32x4 v = {sx[0], sy[0], sx[1], sy[1]};
                st128c(rws, v, WS_YT + (ch0 + hch) * 4096 + (256 * (2 * q + (g & 1)) + 16 * r16 + 4 * (g & 2)) * 2);
            }
    }
    FSTAMP(10);
    bar();
    wo_prefetch<8>(p, lds, ba >> 5, w, l);
    WAIT_VM(8);
    bar();
    if (t == 0) flag_st(ws, WS_FY + xcd * 256 + j * 8, tag);
    STAMP(6);
    return tag;
}

typedef float f32x16 __attribute__((ext_vector_type(16)));
DEV f32x16 mfma32x32(u32x4 a, u32x4 b, f32x16 c) {
    return __builtin_amdgcn_mfma_f32_32x32x16_bf16(__builtin_bit_cast(bf16x8, a), __builtin_bit_cast(bf16x8, b), c, 0, 0, 0);
}
DEV float vmax3(float a, float b, float c) { float r; asm("v_max3_f32 %0, %1, %2, %3" : "=v"(r) : "v"(a), "v"(b), "v"(c)); return r; }
DEV float vmin3(float a, float b, float c) { float r; asm("v_min3_f32 %0, %1, %2, %3" : "=v"(r) : "v"(a), "v"(b), "v"(c)); return r; }

template <bool POS, bool NEG> DEV void feature_map(u32x4 x0, u32x4 x1, u32x4 wown, int l, u32 (&ph)[16]) {
    const bool odd = (l >> 2) & 1;
    u32x4 a0, a1;
#pragma unroll
    for (int q = 0; q < 4; ++q) { a0[q] = odd ? 0u : wown[q]; a1[q] = odd ? wown[q] : 0u; }
    f32x16 pr;
#pragma unroll
    for (int r = 0; r < 16; ++r) pr[r] = 0.f;
    pr = mfma32x32(a0, x0, pr);
    pr = mfma32x32(a1, x1, pr);
    asm volatile("s_nop 11" : "+v"(pr));
    float p[16];
#pragma unroll
    for (int r = 0; r < 16; r += 2) {
        p[r] = pr[r]; p[r + 1] = pr[r + 1];
    }
    constexpr float LG = 1.4426950408889634f;
#pragma unroll
    for (int sg = 0; sg < 2; ++sg) {
        if (sg == 0 ? !POS : !NEG) continue;
        float m = sg ? vmin3(p[0], p[1], p[2]) : vmax3(p[0], p[1], p[2]);
#pragma unroll
        for (int r = 3; r < 15; r += 2) m = sg ? vmin3(m, p[r], p[r + 1]) : vmax3(m, p[r], p[r + 1]);
        m = sg ? vmin3(m, p[15], p[15]) : vmax3(m, p[15], p[15]);
        const float sc = sg ? -LG : LG, c = -m * sc;
        float a[16];
#pragma unroll
        for (int r = 0; r < 16; ++r) a[r] = __builtin_fmaf(p[r], sc, c);
#pragma unroll
        for (int r = 0; r < 16; ++r) a[r] = __builtin_amdgcn_exp2f(a[r]);
        float sa[4];
#pragma unroll
        for (int q = 0; q < 4; ++q) sa[q] = (a[4 * q] + a[4 * q + 1]) + (a[4 * q + 2] + a[4 * q + 3]);
        const float ia = __builtin_amdgcn_rcpf((sa[0] + sa[1]) + (sa[2] + sa[3]));
#pragma unroll
        for (int r = 0; r < 8; ++r) ph[8 * sg + r] = pk2(a[2 * r] * ia, a[2 * r + 1] * ia);
    }
}

DEV void role_attn(const Params& p, ldsp lds, char* ws, u64 e, int bb, int w, int l STAMP_ARG) {
    const int xcd = bb & 7, j = bb >> 3, g = l >> 4, r16 = l & 15, h = l >> 5, w4 = w & 3;
    const int rb_ = 8 * (xcd >> 2) + (j >> 2), cb = 4 * (xcd & 3) + (j & 3);
#define ROT_MODE 1
    const int rot = (ROT_MODE == 0 ? j + xcd : ROT_MODE == 1 ? 2 * (j & 3) + (j >> 2) + xcd : ROT_MODE == 2 ? 2 * (j & 3) + 3 * (j >> 2) + xcd : 0) & 7;
    rsrc_t rws = mkrsrc(ws, WS_USED);
#define SCHED() __builtin_amdgcn_sched_barrier(0)
    constexpr int WPH = 49152, PH = 0, PHS = 32768, VV = 53248, VS = 5120, CNT = 10240, FR = 12288;
    const int tok = 32 * w4 + (l & 31);
    if (w >= 4) {
        __builtin_amdgcn_s_setprio(PRIO_BH);
        const u32x4 ru = mkdesc(p.u, L * D * 2), rw = mkdesc(p.wqkv, D * 3 * D * 2);
        const u32 xv = (rb_ * 128 + w4 * 8 + (l >> 3)) * 1024 + (((l & 7) ^ (((w4 & 1) << 2) | ((l >> 4) & 3))) * 16);
        const u32 wv = (16 * w4 + (l >> 2)) * 3072 + cb * 64 + (l & 3) * 16;
        auto issue_x = [&](int s) {
#pragma unroll
            for (int i = 0; i < 4; ++i) dma16(ru, lds + (s % 3) * 16384 + (4 * i + w4) * 1024, xv, ((s + rot) & 7) * 128 + i * 32768);
        };
        auto issue_w = [&](int s, int sec) {
            dma16(rw, lds + 49152 + ((3 * s + sec) & 7) * 4096 + w4 * 1024, wv, ((s + rot) & 7) * (64 * 3072) + sec * 1024);
        };
        FS3(6);
        issue_w(0, 0); issue_w(0, 1); issue_w(0, 2); issue_x(0);
        FS3(7);
        __builtin_amdgcn_s_barrier();
        FS3(8);
        issue_w(1, 0); issue_w(1, 1); issue_w(1, 2); issue_x(1);
        FS3(9);
#pragma unroll
        for (int s = 0; s < 8; ++s) {
            if (s == 0) WAIT_VM(7); else if (s < 7) WAIT_VM(6); else WAIT_VM(0);
            if (s == 0) FS3(10);
            __builtin_amdgcn_s_barrier();
            if (s >= 1 && s + 1 < 8) issue_w(s + 1, 2);
            if (s + 2 < 8) { issue_x(s + 2); issue_w(s + 2, 0); issue_w(s + 2, 1); }
            if (s == 6 && w == 4) dma16(mkdesc(p.wphi, 32 * 256 * 2), lds + WPH, l * 16, cb * 1024);
        }
        return;
    }
    __builtin_amdgcn_s_setprio(PRIO_BC);
    f32x16 acc[3];
#pragma unroll
    for (int sec = 0; sec < 3; ++sec)
#pragma unroll
        for (int r = 0; r < 16; ++r) acc[sec][r] = 0.f;
    const u32 xa = tok * 128 + ((h ^ ((tok >> 1) & 7)) * 16);
    const u32 wa = 49152 + (8 * h + (r16 >> 2)) * 64 + (g & 1) * 32 + 8 * (l & 3);
    __builtin_amdgcn_s_barrier();
#pragma unroll
    for (int s = 0; s < 8; ++s) {
        __builtin_amdgcn_s_barrier();
        asm volatile("" ::: "memory");
        if (s == 0) FS1(6);
        if (s == 1) FS1(7);
        if (s == 2) FS1(8);
        if (s == 4) FS1(9);
        if (s == 6) FS1(10);
        u32x4 xf[4], wf[12];
        auto read_x = [&](int ks) { xf[ks] = lds_r128(lds + (s % 3) * 16384 + (xa ^ (32 * ks))); };
        auto read_w = [&](int i) {
            ldsp a = lds + wa + ((3 * s + i % 3) & 7) * 4096 + (i / 3) * 1024;
            wf[i] = cat(trread(a), trread(a + 256));
        };
        read_x(0); read_w(0); read_w(1); read_w(2);
        SCHED();
#pragma unroll
        for (int i = 0; i < 12; ++i) {
            if (i % 3 == 0 && i / 3 + 1 < 4) read_x(i / 3 + 1);
            if (i + 3 < 12) read_w(i + 3);
            SCHED();
            acc[i % 3] = mfma32x32(wf[i], xf[i / 3], acc[i % 3]);
            SCHED();
        }
    }
    const u32 wph = WPH + (l & 1) * 512 + (4 * h + (r16 >> 2)) * 32 + (8 * (g & 1) + 4 * ((l & 3) >> 1)) * 2;
    const u32x4 wown = cat(trread(lds + wph), trread(lds + wph + 8 * 32));
    u32x4 fr[3][2];
#pragma unroll
    for (int sec = 0; sec < 3; ++sec)
#pragma unroll
        for (int hh = 0; hh < 2; ++hh)
#pragma unroll
            for (int q = 0; q < 4; ++q) fr[sec][hh][q] = pk2(acc[sec][8 * hh + 2 * q], acc[sec][8 * hh + 2 * q + 1]);
    STAMP(1);
#pragma unroll
    for (int hh = 0; hh < 2; ++hh) {
        ldsp q = lds + VV + hh * VS + tok * 40 + 8 * h;
        lds_w64(q, u32x2{fr[2][hh].x, fr[2][hh].y});
        lds_w64(q + 16, u32x2{fr[2][hh].z, fr[2][hh].w});
    }
    if (w == 0 && l == 0) lds_w32(lds + CNT, 0u);
    {
        u32 ph[16];
        feature_map<true, true>(fr[1][0], fr[1][1], wown, l, ph);
        ldsp q = lds + PH + h * PHS + tok * 80;
#pragma unroll
        for (int i = 0; i < 4; ++i) lds_w128(q + i * 16, u32x4{ph[4 * i], ph[4 * i + 1], ph[4 * i + 2], ph[4 * i + 3]});
    }
    const u64 tag = make_tag(e);
    if (threadIdx.x == 0) flag_st(ws, WS_FRD + bb * 8, tag);
    const u32 tag16 = (u32)tag & 0xffffu;
    FS2(6);
    bar();
    FS2(7);
    {
        const int hh = w >> 1, sg = w & 1;
        f32x4 kv = {0, 0, 0, 0}, z = {0, 0, 0, 0};
        const u32x4 ones = {0x3f803f80u, 0x3f803f80u, 0x3f803f80u, 0x3f803f80u};
        ldsp pa = lds + PH + hh * PHS + (8 * g + (r16 >> 2)) * 80 + sg * 32 + 8 * (l & 3);
        ldsp pb = lds + VV + hh * VS + (8 * g + (r16 >> 2)) * 40 + 8 * (l & 3);
#pragma unroll
        for (int tg = 0; tg < 4; ++tg) {
            u32x4 A = cat(trread(pa + tg * 32 * 80), trread(pa + tg * 32 * 80 + 4 * 80));
            u32x4 B = cat(trread(pb + tg * 32 * 40), trread(pb + tg * 32 * 40 + 4 * 40));
            kv = mfma32(A, B, kv);
            z = mfma32(A, ones, z);
        }
        u32x4 kg = {f2u(kv[0]), f2u(kv[1]), f2u(kv[2]), (pk2(kv[3], 0.f) << 16) | tag16};
        u32x4 zg = {f2u(z[0]), f2u(z[1]), f2u(z[2]), (pk2(z[3], 0.f) << 16) | tag16};
        st128c(rws, kg, WS_KVP + ((cb * 16 + rb_) * 4 + w) * 1024 + l * 16);
        if (r16 == 0) st128c(rws, zg, WS_ZP + ((cb * 16 + rb_) * 4 + w) * 64 + g * 16);
    }
    FS2(8);
    STAMP(2);
    __builtin_amdgcn_s_setprio(PRIO_BQ);
    u32x4 bq[2][2];
    {
        u32 ph[16];
        feature_map<true, true>(fr[0][0], fr[0][1], wown, l, ph);
#pragma unroll
        for (int sg = 0; sg < 2; ++sg)
#pragma unroll
            for (int q = 0; q < 4; ++q) {
                auto sw = __builtin_amdgcn_permlane32_swap(ph[8 * sg + q], ph[8 * sg + q + 4], false, false);
                bq[0][sg][q] = sw[0]; bq[1][sg][q] = sw[1];
            }
    }
    STAMP(3);
    __builtin_amdgcn_s_setprio(PRIO_B2);
    {
        const u32 soff = l & 16 ? WS_ZP + ((cb * 16 + r16) * 4 + w) * 64 + 60 : WS_KVP + ((cb * 16 + r16) * 4 + w) * 1024 + 1020;
        u32x4 part[16], zp;
#pragma unroll 1
        for (u32 spins = 0;; ++spins) {
            bool ok = (ld32c(rws, soff) & 0xffffu) == tag16;
            if (__builtin_amdgcn_ballot_w64(!ok) == 0) {
                asm volatile("" ::: "memory");
                STAMP(4);
#pragma unroll
                for (int r = 0; r < 16; ++r) part[r] = ld128c(rws, WS_KVP + (cb * 16 * 4 + w) * 1024 + l * 16, r * 4096);
                zp = ld128c(rws, WS_ZP + ((cb * 16 + (l >> 2)) * 4 + w) * 64 + (l & 3) * 16);
                u32 bad = (zp.w ^ tag16) & 0xffffu;
#pragma unroll
                for (int r = 0; r < 16; ++r) bad |= (part[r].w ^ tag16) & 0xffffu;
                if (__builtin_amdgcn_ballot_w64(bad != 0) == 0) break;
            }
            if (spins >= SPIN_CAP) { flag_st(ws, WS_TMO, tag); break; }
            __builtin_amdgcn_s_sleep(1);
        }
        zp.w &= 0xffff0000u;
        float s[4] = {u2f(part[0].x), u2f(part[0].y), u2f(part[0].z), u2f(part[0].w & 0xffff0000u)};
#pragma unroll
        for (int r = 1; r < 16; ++r) {
            s[0] += u2f(part[r].x); s[1] += u2f(part[r].y); s[2] += u2f(part[r].z); s[3] += u2f(part[r].w & 0xffff0000u);
        }
        float zs[4] = {u2f(zp.x), u2f(zp.y), u2f(zp.z), u2f(zp.w)};
#pragma unroll
        for (int q = 0; q < 4; ++q) {
            float x = zs[q];
            x += dpp_mov<0x124>(x);
            x += dpp_mov<0x128>(x);
            zs[q] = x;
        }
        rows_reduce2<false>(zs[0], zs[1]);
        rows_reduce2<false>(zs[2], zs[3]);
        FS2(9);
        ldsp F = lds + FR + w * 1024;
        lds_w64(F + (r16 + 32 * (g >> 1)) * 16 + 8 * (g & 1), u32x2{pk2(s[0], s[1]), pk2(s[2], s[3])});
        lds_w64(F + (16 + (l >> 2) + 32 * ((l & 3) >> 1)) * 16 + 8 * (l & 1), u32x2{pk2(zs[0], zs[1]), pk2(zs[2], zs[3])});
    }
    bar();
#pragma unroll
    for (int hh = 0; hh < 2; ++hh) {
        f32x16 nd;
#pragma unroll
        for (int r = 0; r < 16; ++r) nd[r] = 0.f;
        nd = mfma32x32(lds_r128(lds + FR + (2 * hh) * 1024 + l * 16), bq[hh][0], nd);
        nd = mfma32x32(lds_r128(lds + FR + (2 * hh + 1) * 1024 + l * 16), bq[hh][1], nd);
        const float inv = __builtin_amdgcn_rcpf(rb(rb(nd[8]) + 1e-6f));
        u32x4 o;
#pragma unroll
        for (int q = 0; q < 4; ++q) { u32 n = pk2(nd[2 * q], nd[2 * q + 1]); o[q] = pk2(blo(n) * inv, bhi(n) * inv); }
        st128c(rws, o, WS_OP + (rb_ * 128 + tok) * 1024 + cb * 64 + hh * 32 + h * 16);
    }
    FS2(10);
    publish_counted(ws, WS_FO + rb_ * 256 + cb * 8, tag, lds + CNT, 4, l);
    STAMP(5);
}

DEV void final_stage(const Params& p, ldsp lds, char* ws, u64 tag, int ba, int w, int l STAMP_ARG) {
    const int t = threadIdx.x, g = l >> 4, r16 = l & 15, cw = w & 3, th = w >> 2;
    const int tt64 = 4 * (ba & 7) + ((ba >> 3) & 3), ct = ba >> 5;
    rsrc_t ru = mkrsrc(p.u, L * D * 2), rws = mkrsrc(ws, WS_USED), rout = mkrsrc(p.out, L * D * 2);
    const int tok0 = tt64 * 64, chl = ct * 64 + 16 * cw + 4 * g, tok = tok0 + 32 * th + r16;
    constexpr int YL = 4096;
    u32x2 uu[2];
#pragma unroll
    for (int tt = 0; tt < 2; ++tt) uu[tt] = ld64(ru, (tok + 16 * tt) * 1024 + chl * 2);
    const u32x2 dsk = *(const GAS u32x2*)(p.dsk + chl);
    if (ba == 0 && w >= 4) {
        const int q = t - 256;
        wait_flags(ws, WS_FUT + (q >> 5) * 256 + (q & 31) * 8, tag);
        wait_flags(ws, WS_FRD + q * 8, tag);
    }
    WAIT_VM(0);
    bar();
    if (ba == 0 && t == 0) flag_st(ws, WS_E, tag);
    u32x4 wf[16];
    {
        const int row = 16 * (g >> 1) + 4 * (g & 1) + (r16 >> 2);
        ldsp q = lds + 16384 + row * 128 + ((cw ^ ((row >> 1) & 3)) * 32) + 8 * (l & 3);
#pragma unroll
        for (int ks = 0; ks < 16; ++ks) wf[ks] = cat(trread(q + ks * 4096), trread(q + ks * 4096 + 1024));
    }
    if (w == 0) {
        const int tg8 = tt64 * 8 + (l & 7), ga = tg8 >> 7, gc = (tg8 >> 5) & 3, gj = tg8 & 31;
        const int gtb = 16 * ga + (gj >> 1), gcg = 2 * gc + (gj & 1);
        wait_flags(ws, l < 32 ? WS_FY + ct * 256 + l * 8 : WS_FUT + gcg * 256 + gtb * 8, tag);
    }
    bar();
    STAMP(8);
    const u32x4 dws = mkdesc(ws, WS_USED);
    dma16c(dws, lds + YL + w * 1024, WS_YT + (ct * 64 + 8 * w + (l >> 3)) * 4096 + tok0 * 2 + (l & 7) * 16, 0);
    float gate[2];
#pragma unroll
    for (int tt = 0; tt < 2; ++tt) gate[tt] = u2f(ld32c(rws, WS_G + (tok + 16 * tt) * 4));
    __builtin_amdgcn_s_setprio(PRIO_F);
    if (w == 0) wait_flags(ws, WS_FO + (tok0 >> 7) * 256 + r16 * 8, tag);
    asm volatile("" : "+v"(gate[0]), "+v"(gate[1]));
    WAIT_VM(0);
    bar();
    STAMP(10);
#pragma unroll
    for (int i = 0; i < 8; ++i) {
        const int tk = 8 * w + i;
        dma16c(dws, lds + 16384 + tk * 1024, WS_OP + (tok0 + 8 * w) * 1024 + ((l ^ (tk & 15)) * 16), i * 1024);
    }
    __builtin_amdgcn_sched_barrier(0);
    float ga[2][4], g1[2];
#pragma unroll
    for (int tt = 0; tt < 2; ++tt) {
        u32x2 y = trread(lds + YL + (16 * cw + 4 * g + (r16 >> 2)) * 128 + (32 * th + 16 * tt) * 2 + 8 * (l & 3));
        const float gg = gate[tt];
        g1[tt] = rb(1.f - gg);
        u32 t0 = pk2(blo(uu[tt].x) * blo(dsk.x), bhi(uu[tt].x) * bhi(dsk.x)), t1 = pk2(blo(uu[tt].y) * blo(dsk.y), bhi(uu[tt].y) * bhi(dsk.y));
        u32 a0 = pk2(blo(y.x) + blo(t0), bhi(y.x) + bhi(t0)), a1 = pk2(blo(y.y) + blo(t1), bhi(y.y) + bhi(t1));
        u32 ga0 = pk2(gg * blo(a0), gg * bhi(a0)), ga1 = pk2(gg * blo(a1), gg * bhi(a1));
        ga[tt][0] = blo(ga0); ga[tt][1] = bhi(ga0); ga[tt][2] = blo(ga1); ga[tt][3] = bhi(ga1);
        asm volatile("" : "+v"(ga[tt][0]), "+v"(ga[tt][1]), "+v"(ga[tt][2]), "+v"(ga[tt][3]), "+v"(g1[tt]));
    }
    __builtin_amdgcn_sched_barrier(0);
    WAIT_VM(0);
    bar();
    STAMP(11);
    f32x4 acc[2] = {f32x4{0, 0, 0, 0}, f32x4{0, 0, 0, 0}};
    {
        u32 ob[4];
#pragma unroll
        for (int k4 = 0; k4 < 4; ++k4) ob[k4] = 16384 + (32 * th + r16) * 1024 + ((((4 * k4) ^ (r16 & 12)) | (g ^ (r16 & 3))) * 16);
#pragma unroll
        for (int ks = 0; ks < 16; ++ks)
#pragma unroll
            for (int tt = 0; tt < 2; ++tt)
                acc[tt] = mfma32(wf[ks], lds_r128(lds + ob[ks & 3] + (ks >> 2) * 256 + tt * 16384), acc[tt]);
    }
#pragma unroll
    for (int tt = 0; tt < 2; ++tt) {
        u32x2 o = {pk2(__builtin_fmaf(g1[tt], acc[tt][0], ga[tt][0]), __builtin_fmaf(g1[tt], acc[tt][1], ga[tt][1])),
                   pk2(__builtin_fmaf(g1[tt], acc[tt][2], ga[tt][2]), __builtin_fmaf(g1[tt], acc[tt][3], ga[tt][3]))};
        __builtin_amdgcn_raw_buffer_store_b64(o, rout, (tok + 16 * tt) * 1024 + chl * 2, 0, OUT_AUX);
    }
    STAMP(12);
}

__global__ __launch_bounds__(512, 4) void s4d_hedgehog_mix(Params p) {
    extern __shared__ __attribute__((aligned(16))) char smem[];
    ldsp lds = (ldsp)smem;
    char* ws = p.ws;
    const int b = blockIdx.x, l = threadIdx.x & 63, w = uni(threadIdx.x >> 6);
    STAMP(0);
    asm volatile("" :: "s"(p.ws), "s"(p.u), "s"(p.wqkv), "s"(p.ldt), "s"(p.gw));
    const u64 e = flag_ld(ws, WS_E);
    if (b >= 256) {
        const u64 tag = role_conv(p, lds, ws, e, b - 256, w, l STAMP_PASS);
        final_stage(p, lds, ws, tag, b - 256, w, l STAMP_PASS);
    } else {
        role_attn(p, lds, ws, e, b, w, l STAMP_PASS);
    }
}

#if defined(__HIP_DEVICE_COMPILE__)
#pragma clang attribute pop
#endif

extern "C" void kernel_launch(void* const* d_in, const int* in_sizes, int n_in, void* d_out, int out_size, void* d_ws, size_t ws_size,
                              hipStream_t stream) {
    Params p;
    p.u = (const u16*)d_in[0]; p.lar = (const u16*)d_in[1]; p.aim = (const u16*)d_in[2]; p.cre = (const u16*)d_in[3];
    p.cim = (const u16*)d_in[4]; p.ldt = (const u16*)d_in[5]; p.dsk = (const u16*)d_in[6]; p.wqkv = (const u16*)d_in[8];
    p.wphi = (const u16*)d_in[9]; p.wo = (const u16*)d_in[10]; p.gw = (const u16*)d_in[11]; p.out = (u16*)d_out; p.ws = (char*)d_ws;
    (void)hipFuncSetAttribute((const void*)s4d_hedgehog_mix, hipFuncAttributeMaxDynamicSharedMemorySize, LDS_BYTES);
    s4d_hedgehog_mix<<<512, 512, LDS_BYTES, stream>>>(p);
}
```

```cpp
#include <hip/hip_runtime.h>
#include <hip/hip_bf16.h>
#include <stdint.h>

#if defined(__HIP_DEVICE_COMPILE__)
#pragma clang attribute push(__attribute__((target("no-packed-fp32-ops"))), apply_to = function)
#endif

typedef unsigned short u16;
typedef unsigned u32;
typedef unsigned long long u64;
typedef float f32x2 __attribute__((ext_vector_type(2)));
typedef float f32x4 __attribute__((ext_vector_type(4)));
typedef u32 u32x2 __attribute__((ext_vector_type(2)));
typedef u32 u32x4 __attribute__((ext_vector_type(4)));
typedef short s16x4 __attribute__((ext_vector_type(4)));
typedef __bf16 bf16x2 __attribute__((ext_vector_type(2)));
typedef __bf16 bf16x8 __attribute__((ext_vector_type(8)));
#define LAS __attribute__((address_space(3)))
#define GAS __attribute__((address_space(1)))
typedef LAS char* ldsp;
typedef __amdgpu_buffer_rsrc_t rsrc_t;
#define DEV __device__ __forceinline__

constexpr int L = 2048, D = 512;
constexpr int LDS_BYTES = 81920;
constexpr u64 TAG_STEP = 0x9E3779B97F4A7C15ull;
constexpr u32 SPIN_CAP = 40000;

constexpr u32 WS_E = 0, WS_TMO = 64;
constexpr u32 WS_FUT = 4096, WS_FRD = WS_FUT + 4096, WS_FY = WS_FRD + 4096, WS_FO = WS_FY + 4096;
constexpr u32 WS_G = 0x28000;
constexpr u32 WS_UT = 0x40000;
constexpr u32 WS_YT = WS_UT + 0x200000;
constexpr u32 WS_OP = WS_YT + 0x200000;
constexpr u32 WS_KVP = WS_OP + 0x200000;
constexpr u32 WS_ZP = WS_KVP + 0x100000;
constexpr u32 WS_USED = WS_ZP + 0x10000;

constexpr int AUX_SC1 = 16;

struct IdxTab { u16 v[2048]; };
constexpr IdxTab make_idx() {
    IdxTab t{};
    for (int i = 0; i < 2048; ++i) {
        int j = 2047 - i, p = j;
        int sh = j >= 1024 ? 3 : (j >= 512 ? 2 : (j >= 256 ? 1 : 0));
        if (sh) {
            int q = j >> sh, rem = j & ((1 << sh) - 1), half = 1 << (sh - 1);
            if (rem > half || (rem == half && (q & 1))) ++q;
            p = q << sh;
        }
        int row = 0, col = (p >> 3) & 15;
        if (p < 256) row = (p & 7) + 8 * (p >> 7);
        else if (p < 512) row = 16 + ((p - 256) >> 7) * 4 + ((p & 7) >> 1);
        else if (p < 1024) row = 24 + ((p - 512) >> 7) * 2 + ((p & 7) >> 2);
        else row = 32 + ((p - 1024) >> 7);
        t.v[i] = (u16)((row * 16 + col) * 2);
    }
    return t;
}
__device__ const IdxTab g_idx = make_idx();

DEV float u2f(u32 x) { return __builtin_bit_cast(float, x); }
DEV u32 f2u(float x) { return __builtin_bit_cast(u32, x); }
DEV float fexp(float x) { return __builtin_amdgcn_exp2f(x * 1.4426950408889634f); }
template <int CTRL> DEV float dpp_mov(float x) { return u2f(__builtin_amdgcn_update_dpp(0u, f2u(x), CTRL, 0xf, 0xf, true)); }
DEV float row16_sum(float x) {
    x += dpp_mov<0xB1>(x);
    x += dpp_mov<0x4E>(x);
    x += dpp_mov<0x141>(x);
    x += dpp_mov<0x140>(x);
    return x;
}
DEV u32 pk2(float lo, float hi) { f32x2 v = {lo, hi}; return __builtin_bit_cast(u32, __builtin_convertvector(v, bf16x2)); }
DEV float blo(u32 x) { return u2f(x << 16); }
DEV float bhi(u32 x) { return u2f(x & 0xffff0000u); }
DEV float rb(float f) { return blo(pk2(f, 0.f)); }
DEV f32x4 mfma32(u32x4 a, u32x4 b, f32x4 c) {
    return __builtin_amdgcn_mfma_f32_16x16x32_bf16(__builtin_bit_cast(bf16x8, a), __builtin_bit_cast(bf16x8, b), c, 0, 0, 0);
}
DEV u32x2 trread(ldsp p) { return __builtin_bit_cast(u32x2, __builtin_amdgcn_ds_read_tr16_b64_v4i16((LAS s16x4*)p)); }
DEV u32x4 lds_r128(ldsp p) { return *(LAS u32x4*)p; }
DEV u32x2 lds_r64(ldsp p) { return *(volatile LAS u32x2*)p; }
DEV void lds_w128(ldsp p, u32x4 v) { *(LAS u32x4*)p = v; }
DEV void lds_w64(ldsp p, u32x2 v) { *(LAS u32x2*)p = v; }
DEV void lds_w32(ldsp p, u32 v) { *(LAS u32*)p = v; }
DEV u32x4 cat(u32x2 a, u32x2 b) { u32x4 r = {a.x, a.y, b.x, b.y}; return r; }
DEV rsrc_t mkrsrc(const void* p, u32 bytes) { return __builtin_amdgcn_make_buffer_rsrc((void*)p, 0, bytes, 0x00020000); }
DEV u32x4 mkdesc(const void* p, u32 bytes) {
    u64 a = (u64)p;
    u32x4 d = {(u32)a, (u32)(a >> 32) & 0xffffu, bytes, 0x00020000u};
    return d;
}
DEV void dma16(u32x4 desc, ldsp dst, u32 voff, u32 soff) {
    asm volatile("s_mov_b32 m0, %0\n\ts_nop 0\n\tbuffer_load_dwordx4 %1, %2, %3 offen lds"
                 :: "s"((u32)(uintptr_t)dst), "v"(voff), "s"(desc), "s"(soff) : "memory");
}
DEV void dma16c(u32x4 desc, ldsp dst, u32 voff, u32 soff) {
    asm volatile("s_mov_b32 m0, %0\n\ts_nop 0\n\tbuffer_load_dwordx4 %1, %2, %3 offen sc1 lds"
                 :: "s"((u32)(uintptr_t)dst), "v"(voff), "s"(desc), "s"(soff) : "memory");
}
DEV u32x4 ld128(rsrc_t r, u32 voff, u32 soff = 0) { return __builtin_amdgcn_raw_buffer_load_b128(r, voff, soff, 0); }
DEV u32x2 ld64(rsrc_t r, u32 voff, u32 soff = 0) { return __builtin_amdgcn_raw_buffer_load_b64(r, voff, soff, 0); }
DEV u32x4 ld128c(rsrc_t r, u32 voff, u32 soff = 0) { return __builtin_amdgcn_raw_buffer_load_b128(r, voff, soff, AUX_SC1); }
DEV u32x2 ld64c(rsrc_t r, u32 voff, u32 soff = 0) { return __builtin_amdgcn_raw_buffer_load_b64(r, voff, soff, AUX_SC1); }
DEV u32 ld32c(rsrc_t r, u32 voff, u32 soff = 0) { return __builtin_amdgcn_raw_buffer_load_b32(r, voff, soff, AUX_SC1); }
DEV void st128c(rsrc_t r, u32x4 v, u32 voff, u32 soff = 0) { __builtin_amdgcn_raw_buffer_store_b128(v, r, voff, soff, AUX_SC1); }
#define WAIT_VM(n) asm volatile("s_waitcnt vmcnt(" #n ")" ::: "memory")
#define SCHED() __builtin_amdgcn_sched_barrier(0)
DEV void bar() { asm volatile("s_waitcnt lgkmcnt(0)" ::: "memory"); __builtin_amdgcn_s_barrier(); asm volatile("" ::: "memory"); }
DEV u32 uni(u32 x) { return __builtin_amdgcn_readfirstlane(x); }

DEV float swap16_lo(float a, float b, float& other) {
    auto s = __builtin_amdgcn_permlane16_swap(f2u(a), f2u(b), false, false);
    other = u2f(s[1]);
    return u2f(s[0]);
}
DEV float swap32_lo(float a, float b, float& other) {
    auto s = __builtin_amdgcn_permlane32_swap(f2u(a), f2u(b), false, false);
    other = u2f(s[1]);
    return u2f(s[0]);
}
template <bool MAX> DEV void rows_reduce2(float& a, float& b) {
    float o, t = swap16_lo(a, b, o);
    t = MAX ? __builtin_fmaxf(t, o) : t + o;
    float u = swap32_lo(t, t, o);
    u = MAX ? __builtin_fmaxf(u, o) : u + o;
    a = swap16_lo(u, u, b);
}

typedef GAS u64 gu64;
DEV u64 make_tag(u64 e) {
    u32 lo = (u32)e, hi = (u32)(e >> 32);
    asm volatile("" : "+v"(lo), "+v"(hi));
    return (((u64)uni(hi) << 32) | uni(lo)) + TAG_STEP;
}
DEV u64 flag_ld(char* ws, u32 off) { return __hip_atomic_load((gu64*)(ws + off), __ATOMIC_RELAXED, __HIP_MEMORY_SCOPE_AGENT); }
DEV void flag_st(char* ws, u32 off, u64 v) { __hip_atomic_store((gu64*)(ws + off), v, __ATOMIC_RELAXED, __HIP_MEMORY_SCOPE_AGENT); }
DEV void wait_flags(char* ws, u32 off, u64 tag) {
#pragma unroll 1
    for (u32 spins = 0; spins < SPIN_CAP; ++spins) {
        u64 v = flag_ld(ws, off);
        if (__builtin_amdgcn_ballot_w64(v != tag) == 0) { asm volatile("" ::: "memory"); return; }
        __builtin_amdgcn_s_sleep(1);
    }
    flag_st(ws, WS_TMO, tag);
}
DEV void publish(char* ws, u32 off, u64 tag) {
    WAIT_VM(0);
    bar();
    if (threadIdx.x == 0) flag_st(ws, off, tag);
}

DEV void publish_counted(char* ws, u32 off, u64 tag, ldsp cnt, u32 n, int l) {
    WAIT_VM(0);
    u32 old = 0;
    if (l == 0) old = __hip_atomic_fetch_add((LAS u32*)cnt, 1u, __ATOMIC_RELAXED, __HIP_MEMORY_SCOPE_WORKGROUP);
    if (l == 0 && old == n - 1) flag_st(ws, off, tag);
}

struct Params {
    char* ws; const u16* u; const u16* wqkv; const u16* wphi; const u16* lar; const u16* aim; const u16* cre; const u16* cim;
    const u16* ldt; const u16* gw; const u16* dsk; const u16* wo; u16* out;
};

struct cplx { float r, i; };
DEV cplx cmul(cplx a, cplx b) { cplx c; c.r = a.r * b.r - a.i * b.i; c.i = a.r * b.i + a.i * b.r; return c; }
DEV cplx cexp_p(float zr2, float zrev, float p) {
    float e = __builtin_amdgcn_exp2f(zr2 * p);
    float a = __builtin_amdgcn_fractf(zrev * p);
    cplx c; c.r = e * __builtin_amdgcn_cosf(a); c.i = e * __builtin_amdgcn_sinf(a); return c;
}

template <int NW> DEV void wo_prefetch(const Params& p, ldsp lds, int ct, int w, int l) {
    const u32x4 rwo = mkdesc(p.wo, D * D * 2);
    u32 voff = (l >> 3) * 1024 + ct * 128 + ((((l & 7) >> 1) ^ ((l >> 4) & 3)) * 32) + (l & 1) * 16;
#pragma unroll
    for (int i = 0; i < 64 / NW; ++i) {
        int piece = i * NW + w;
        dma16(rwo, lds + 16384 + piece * 1024, voff, piece * 8192);
    }
}

DEV u64 role_conv(const Params& p, ldsp lds, char* ws, u64 e, u64 code0, u32 code_bytes, int ba, int w, int l) {
    const int t = threadIdx.x, xcd = ba & 7, j = ba >> 3, g = l >> 4, r16 = l & 15;
    const int a = xcd >> 2, c = xcd & 3;
    rsrc_t ru = mkrsrc(p.u, L * D * 2), rws = mkrsrc(ws, WS_USED);
    const int ch0 = 2 * (32 * xcd + j);
    __builtin_amdgcn_s_setprio(3);
    const int tb = 16 * a + (j >> 1), cg = 2 * c + (j & 1);
    {
        u32 voff = (tb * 64 + (l >> 3)) * 1024 + cg * 128 + (l & 7) * 16;
        dma16(mkdesc(p.u, L * D * 2), lds + 70656 + w * 1024, voff, w * 8192);
    }
    const int gtok = 1024 * a + 8 * (32 * c + j) + w;
    u32x4 gx = ld128(ru, gtok * 1024 + l * 16);
    u32x4 gwv = *(const GAS u32x4*)(p.gw + l * 8);
    const int kch = (t >> 7) & 1, kn = t & 127;
    const int pidx = (ch0 + kch) * 128 + kn;
    float lar = blo(p.lar[pidx]), aim = blo(p.aim[pidx]), cre = blo(p.cre[pidx]), cim = blo(p.cim[pidx]), ldt = blo(p.ldt[ch0 + kch]);
    u32x4 idx = *(const GAS u32x4*)(g_idx.v + (t & 255) * 8);
    u32 pf = 0;
    if (w == 5) pf = *(const GAS u32*)(p.dsk + (l & 7) * 64);
    else if (w == 4 && l < 6 && (6 * j + l) * 128 < code_bytes) pf = *(const GAS u32*)(code0 + (6 * j + l) * 128);
    SCHED();
    __builtin_amdgcn_s_setprio(1);
    WAIT_VM(0);
    asm volatile("" :: "v"(pf));
    const u64 tag = make_tag(e);
    bar();
    {
        const int cw = w & 3, s = w >> 2;
        ldsp src = lds + 70656 + (32 * s + 8 * g + (r16 >> 2)) * 128 + 32 * cw + 8 * (l & 3);
        u32x4 v = cat(trread(src), trread(src + 4 * 128));
        st128c(rws, v, WS_UT + (cg * 64 + 16 * cw + r16) * 4096 + (tb * 64 + 32 * s + 8 * g) * 2);
    }
    {
        float s0 = 0.f, s1 = 0.f;
#pragma unroll
        for (int q = 0; q < 4; ++q) {
            u32 pr = pk2(blo(gx[q]) * blo(gwv[q]), bhi(gx[q]) * bhi(gwv[q]));
            s0 += blo(pr); s1 += bhi(pr);
        }
        s0 = row16_sum(s0 + s1);
        s1 = s0;
        rows_reduce2<false>(s0, s1);
        float g0 = rb(__builtin_amdgcn_rcpf(1.f + fexp(-rb(s0))));
        if (l == 0) __builtin_amdgcn_raw_buffer_store_b32(f2u(g0), rws, WS_G + gtok * 4, 0, AUX_SC1);
    }
    {
        const float dt = rb(fexp(ldt));
        const float zr2 = -rb(fexp(lar)) * dt * 1.4426950408889634f, zrev = aim * dt * 0.15915494309189535f;
        ldsp T = lds + kch * 33792 + kn * 4;
        auto put = [&](int row, cplx v) { lds_w32(T + row * 528, pk2(v.r, v.i)); };
        const cplx w128 = cexp_p(zr2, zrev, 128.f);
        cplx G; G.r = 2.f * cre * dt; G.i = 2.f * cim * dt;
        if (w < 4) {
            cplx wp[8];
            wp[1] = cexp_p(zr2, zrev, 1.f); wp[2] = cmul(wp[1], wp[1]); wp[3] = cmul(wp[2], wp[1]); wp[4] = cmul(wp[2], wp[2]);
            wp[5] = cmul(wp[4], wp[1]); wp[6] = cmul(wp[4], wp[2]); wp[7] = cmul(wp[4], wp[3]);
#pragma unroll
            for (int aa = 0; aa < 4; ++aa) {
                if (aa == 1) publish(ws, WS_FUT + cg * 256 + tb * 8, tag);
                if (aa < 2) {
                    put(8 * aa, G);
#pragma unroll
                    for (int cc = 1; cc < 8; ++cc) put(8 * aa + cc, cmul(G, wp[cc]));
                } else {
                    put(16 + 4 * (aa - 2), G);
#pragma unroll
                    for (int cc = 1; cc < 4; ++cc) put(16 + 4 * (aa - 2) + cc, cmul(G, wp[2 * cc]));
                }
                if (aa < 3) G = cmul(G, w128);
            }
        } else {
            const cplx w4 = cexp_p(zr2, zrev, 4.f);
            G = cmul(G, cexp_p(zr2, zrev, 512.f));
#pragma unroll
            for (int aa = 4; aa <= 16; ++aa) {
                if (aa == 10) publish(ws, WS_FUT + cg * 256 + tb * 8, tag);
                if (aa < 8) {
                    put(24 + 2 * (aa - 4), G);
                    put(24 + 2 * (aa - 4) + 1, cmul(G, w4));
                } else put(32 + aa - 8, G);
                if (aa < 16) G = cmul(G, w128);
            }
            cplx B[9];
            B[1] = cmul(w4, w4); B[2] = cmul(B[1], B[1]); B[3] = cmul(B[2], B[1]); B[4] = cmul(B[2], B[2]);
            B[5] = cmul(B[4], B[1]); B[6] = cmul(B[4], B[2]); B[7] = cmul(B[4], B[3]); B[8] = cmul(B[4], B[4]);
            lds_w32(T + 48 * 528, 0x00003f80u);
#pragma unroll
            for (int b = 1; b < 16; ++b) {
                cplx v = b <= 8 ? B[b] : cmul(B[8], B[b - 8]);
                lds_w32(T + (48 + b) * 528, pk2(v.r, -v.i));
            }
        }
    }
    bar();
    if (w == 7) wait_flags(ws, WS_FUT + xcd * 256 + (l & 31) * 8, tag);
    if (w < 6) {
        const int ch = w >= 3, tile = w - 3 * ch;
        ldsp T = lds + ch * 33792 + r16 * 528 + g * 16;
        f32x4 k0 = {0, 0, 0, 0};
#pragma unroll
        for (int s = 0; s < 8; ++s) k0 = mfma32(lds_r128(T + tile * 16 * 528 + s * 64), lds_r128(T + 48 * 528 + s * 64), k0);
        ldsp KC = lds + 67584 + ch * 1536 + r16 * 2 + (tile * 16 + 4 * g) * 32;
        u32 a0 = pk2(k0[0], k0[1]), a1 = pk2(k0[2], k0[3]);
        *(LAS u16*)(KC) = (u16)a0; *(LAS u16*)(KC + 32) = (u16)(a0 >> 16);
        *(LAS u16*)(KC + 64) = (u16)a1; *(LAS u16*)(KC + 96) = (u16)(a1 >> 16);
    }
    bar();
    const int hch = w >> 2, t8 = t & 255;
    const u32 uoff = WS_UT + (ch0 + hch) * 4096 + t8 * 16;
    u32x4 d0 = ld128c(rws, uoff), d1 = ld128c(rws, uoff + 4), d2 = ld128c(rws, uoff + 8);
    u32x2 f = ld64c(rws, WS_UT + (ch0 + hch) * 4096);
    u32 pfw = 0;
    if (w >= 6) pfw = *(const GAS u32*)(p.wo + ((j & 3) * 128 + (w - 6) * 64 + l) * 512 + (j >> 2) * 64);
    SCHED();
    {
        ldsp KC = lds + 67584 + hch * 1536;
        u32x4 v;
#pragma unroll
        for (int q = 0; q < 4; ++q) {
            u32 lo = *(LAS u16*)(KC + (idx[q] & 0xffffu)), hi = *(LAS u16*)(KC + (idx[q] >> 16));
            v[q] = lo | (hi << 16);
        }
        lds_w128(lds + hch * 4608 + t8 * 16, v);
        if (t8 < 32) { u32x4 z = {0, 0, 0, 0}; lds_w128(lds + hch * 4608 + 4096 + t8 * 16, z); }
    }
    SCHED();
    constexpr int UB = 9216, CS = 4160;
    {
        u32x4 v1, v3;
#pragma unroll
        for (int q = 0; q < 4; ++q) { v1[q] = __builtin_amdgcn_alignbit(d1[q], d0[q], 16); v3[q] = __builtin_amdgcn_alignbit(d2[q], d1[q], 16); }
        ldsp U = lds + UB + hch * 4 * CS + 32 + t8 * 16;
        lds_w128(U, d0); lds_w128(U + CS, v1); lds_w128(U + 2 * CS, d1); lds_w128(U + 3 * CS, v3);
        if (t8 < 4) {
            u32x4 z = {0, 0, 0, 0}, y = z;
            if (t8 == 1) y.w = f.x << 16;
            if (t8 == 2) y.w = f.x;
            if (t8 == 3) { y.z = f.x << 16; y.w = __builtin_amdgcn_alignbit(f.y, f.x, 16); }
            ldsp F = lds + UB + (hch * 4 + t8) * CS;
            lds_w128(F, z); lds_w128(F + 16, y);
        }
    }
    bar();
    {
        const int mq = w & 3;
        const int s = (r16 + 1) & 3;
        ldsp A0 = lds + UB + (hch * 4 + s) * CS + 2 * (1 + r16 - s + 8 * g) + mq * 2 * 64;
        ldsp B0 = lds + hch * 4608 + 4064 - 3584 - 32 * r16 + 16 * g + mq * 2 * 64;
        f32x4 acc[8];
#pragma unroll
        for (int i = 0; i < 8; ++i) acc[i] = f32x4{0, 0, 0, 0};
#pragma unroll
        for (int mm = 0; mm < 2; ++mm) {
            u32x4 Af[8], Bf[8];
#pragma unroll
            for (int i = 0; i < 8; ++i) {
                Bf[i] = lds_r128(B0 + mm * 64 + (7 - i) * 512);
                Af[i] = cat(lds_r64(A0 + mm * 64 + i * 512), lds_r64(A0 + mm * 64 + i * 512 + 8));
            }
#pragma unroll
            for (int i = 0; i < 8; ++i)
#pragma unroll
                for (int cc = i; cc < 8; ++cc) acc[cc] = mfma32(Af[i], Bf[cc - i], acc[cc]);
            __builtin_amdgcn_sched_barrier(0);
        }
        bar();
        ldsp R = lds + hch * 24576 + l * 16;
#pragma unroll
        for (int cc = 0; cc < 8; ++cc)
            if ((cc >> 1) != mq) lds_w128(R + (cc * 3 + ((mq - (cc >> 1) - 1) & 3)) * 1024, __builtin_bit_cast(u32x4, acc[cc]));
        bar();
#pragma unroll
        for (int q = 0; q < 4; ++q)
            if (q == mq) {
                u32 pk[2][2];
#pragma unroll
                for (int h = 0; h < 2; ++h) {
                    float v[4];
#pragma unroll
                    for (int r = 0; r < 4; ++r) v[r] = acc[2 * q + h][r];
#pragma unroll
                    for (int sl = 0; sl < 3; ++sl) {
                        u32x4 o = lds_r128(R + ((2 * q + h) * 3 + sl) * 1024);
#pragma unroll
                        for (int r = 0; r < 4; ++r) v[r] += u2f(o[r]);
                    }
                    pk[h][0] = pk2(v[0], v[1]); pk[h][1] = pk2(v[2], v[3]);
                }
                auto sx = __builtin_amdgcn_permlane16_swap(pk[0][0], pk[1][0], false, false);
                auto sy = __builtin_amdgcn_permlane16_swap(pk[0][1], pk[1][1], false, false);
                u32x4 v = {sx[0], sy[0], sx[1], sy[1]};
                st128c(rws, v, WS_YT + (ch0 + hch) * 4096 + (256 * (2 * q + (g & 1)) + 16 * r16 + 4 * (g & 2)) * 2);
            }
    }
    asm volatile("" :: "v"(pfw));
    bar();
    wo_prefetch<8>(p, lds, ba >> 5, w, l);
    WAIT_VM(8);
    bar();
    if (t == 0) flag_st(ws, WS_FY + xcd * 256 + j * 8, tag);
    return tag;
}

typedef float f32x16 __attribute__((ext_vector_type(16)));
DEV f32x16 mfma32x32(u32x4 a, u32x4 b, f32x16 c) {
    return __builtin_amdgcn_mfma_f32_32x32x16_bf16(__builtin_bit_cast(bf16x8, a), __builtin_bit_cast(bf16x8, b), c, 0, 0, 0);
}
DEV float vmax3(float a, float b, float c) { float r; asm("v_max3_f32 %0, %1, %2, %3" : "=v"(r) : "v"(a), "v"(b), "v"(c)); return r; }
DEV float vmin3(float a, float b, float c) { float r; asm("v_min3_f32 %0, %1, %2, %3" : "=v"(r) : "v"(a), "v"(b), "v"(c)); return r; }

DEV void feature_map(u32x4 x0, u32x4 x1, u32x4 wown, int l, u32 (&ph)[16]) {
    const bool odd = (l >> 2) & 1;
    u32x4 a0, a1;
#pragma unroll
    for (int q = 0; q < 4; ++q) { a0[q] = odd ? 0u : wown[q]; a1[q] = odd ? wown[q] : 0u; }
    f32x16 pr;
#pragma unroll
    for (int r = 0; r < 16; ++r) pr[r] = 0.f;
    pr = mfma32x32(a0, x0, pr);
    pr = mfma32x32(a1, x1, pr);
    asm volatile("s_nop 11" : "+v"(pr));
    float p[16];
#pragma unroll
    for (int r = 0; r < 16; ++r) p[r] = pr[r];
    float mx[5], mn[5];
#pragma unroll
    for (int q = 0; q < 5; ++q) { mx[q] = vmax3(p[3 * q], p[3 * q + 1], p[3 * q + 2]); mn[q] = vmin3(p[3 * q], p[3 * q + 1], p[3 * q + 2]); }
    const float mxa = vmax3(mx[0], mx[1], mx[2]), mna = vmin3(mn[0], mn[1], mn[2]);
    const float mxb = vmax3(mx[3], mx[4], p[15]), mnb = vmin3(mn[3], mn[4], p[15]);
    const float rmax = vmax3(mxa, mxb, mxb), rmin = vmin3(mna, mnb, mnb);
    constexpr float LG = 1.4426950408889634f;
#pragma unroll
    for (int sg = 0; sg < 2; ++sg) {
        const float m = sg ? rmin : rmax;
        const float sc = sg ? -LG : LG, c = -m * sc;
        float a[16];
#pragma unroll
        for (int r = 0; r < 16; ++r) a[r] = __builtin_fmaf(p[r], sc, c);
#pragma unroll
        for (int r = 0; r < 16; ++r) a[r] = __builtin_amdgcn_exp2f(a[r]);
        float sa[4];
#pragma unroll
        for (int q = 0; q < 4; ++q) sa[q] = (a[4 * q] + a[4 * q + 1]) + (a[4 * q + 2] + a[4 * q + 3]);
        const float ia = __builtin_amdgcn_rcpf((sa[0] + sa[1]) + (sa[2] + sa[3]));
#pragma unroll
        for (int r = 0; r < 8; ++r) ph[8 * sg + r] = pk2(a[2 * r] * ia, a[2 * r + 1] * ia);
    }
}

DEV void role_attn(const Params& p, ldsp lds, char* ws, u64 e, int bb, int w, int l) {
    const int xcd = bb & 7, j = bb >> 3, g = l >> 4, r16 = l & 15, h = l >> 5, w4 = w & 3;
    const int rb_ = 8 * (xcd >> 2) + (j >> 2), cb = 4 * (xcd & 3) + (j & 3);
    const int rot = (2 * (j & 3) + (j >> 2) + xcd) & 7;
    rsrc_t rws = mkrsrc(ws, WS_USED);
    constexpr int WPH = 49152, PH = 0, PHS = 32768, VV = 53248, VS = 5120, CNT = 10240, FR = 12288;
    const int tok = 32 * w4 + (l & 31);
    if (w >= 4) {
        __builtin_amdgcn_s_setprio(0);
        const u32x4 ru = mkdesc(p.u, L * D * 2), rw = mkdesc(p.wqkv, D * 3 * D * 2);
        const u32 xv = (rb_ * 128 + w4 * 8 + (l >> 3)) * 1024 + (((l & 7) ^ (((w4 & 1) << 2) | ((l >> 4) & 3))) * 16);
        const u32 wv = (16 * w4 + (l >> 2)) * 3072 + cb * 64 + (l & 3) * 16;
        auto issue_x = [&](int s) {
#pragma unroll
            for (int i = 0; i < 4; ++i) dma16(ru, lds + (s % 3) * 16384 + (4 * i + w4) * 1024, xv, ((s + rot) & 7) * 128 + i * 32768);
        };
        auto issue_w = [&](int s, int sec) {
            dma16(rw, lds + 49152 + ((3 * s + sec) & 7) * 4096 + w4 * 1024, wv, ((s + rot) & 7) * (64 * 3072) + sec * 1024);
        };
        issue_w(0, 0); issue_w(0, 1); issue_w(0, 2); issue_x(0);
        __builtin_amdgcn_s_barrier();
        issue_w(1, 0); issue_w(1, 1); issue_w(1, 2); issue_x(1);
#pragma unroll
        for (int s = 0; s < 8; ++s) {
            if (s == 0) WAIT_VM(7); else if (s < 7) WAIT_VM(6); else WAIT_VM(0);
            __builtin_amdgcn_s_barrier();
            if (s >= 1 && s + 1 < 8) issue_w(s + 1, 2);
            if (s + 2 < 8) { issue_x(s + 2); issue_w(s + 2, 0); issue_w(s + 2, 1); }
            if (s == 6 && w == 4) dma16(mkdesc(p.wphi, 32 * 256 * 2), lds + WPH, l * 16, cb * 1024);
        }
        return;
    }
    __builtin_amdgcn_s_setprio(2);
    f32x16 acc[3];
#pragma unroll
    for (int sec = 0; sec < 3; ++sec)
#pragma unroll
        for (int r = 0; r < 16; ++r) acc[sec][r] = 0.f;
    const u32 xa = tok * 128 + ((h ^ ((tok >> 1) & 7)) * 16);
    const u32 wa = 49152 + (8 * h + (r16 >> 2)) * 64 + (g & 1) * 32 + 8 * (l & 3);
    u32 pf = *(const GAS u32*)(p.wphi + cb * 512 + (l & 7) * 64);
    __builtin_amdgcn_s_barrier();
#pragma unroll
    for (int s = 0; s < 8; ++s) {
        __builtin_amdgcn_s_barrier();
        asm volatile("" ::: "memory");
        if (s == 3) asm volatile("" :: "v"(pf));
        if (s == 4 && w == 1) pf = ld32c(rws, l < 32 ? WS_KVP + (cb * 16 + rb_) * 4096 + l * 128 : WS_ZP + (cb * 16 + rb_) * 256 + (l & 1) * 128);
        if (s == 7) asm volatile("" :: "v"(pf));
        u32x4 xf[4], wf[12];
        auto read_x = [&](int ks) { xf[ks] = lds_r128(lds + (s % 3) * 16384 + (xa ^ (32 * ks))); };
        auto read_w = [&](int i) {
            ldsp a = lds + wa + ((3 * s + i % 3) & 7) * 4096 + (i / 3) * 1024;
            wf[i] = cat(trread(a), trread(a + 256));
        };
        read_x(0); read_w(0); read_w(1); read_w(2);
        SCHED();
#pragma unroll
        for (int i = 0; i < 12; ++i) {
            if (i % 3 == 0 && i / 3 + 1 < 4) read_x(i / 3 + 1);
            if (i + 3 < 12) read_w(i + 3);
            SCHED();
            acc[i % 3] = mfma32x32(wf[i], xf[i / 3], acc[i % 3]);
            SCHED();
        }
    }
    const u32 wph = WPH + (l & 1) * 512 + (4 * h + (r16 >> 2)) * 32 + (8 * (g & 1) + 4 * ((l & 3) >> 1)) * 2;
    const u32x4 wown = cat(trread(lds + wph), trread(lds + wph + 8 * 32));
    u32x4 fr[3][2];
#pragma unroll
    for (int sec = 0; sec < 3; ++sec)
#pragma unroll
        for (int hh = 0; hh < 2; ++hh)
#pragma unroll
            for (int q = 0; q < 4; ++q) fr[sec][hh][q] = pk2(acc[sec][8 * hh + 2 * q], acc[sec][8 * hh + 2 * q + 1]);
#pragma unroll
    for (int hh = 0; hh < 2; ++hh) {
        ldsp q = lds + VV + hh * VS + tok * 40 + 8 * h;
        lds_w64(q, u32x2{fr[2][hh].x, fr[2][hh].y});
        lds_w64(q + 16, u32x2{fr[2][hh].z, fr[2][hh].w});
    }
    if (w == 0 && l == 0) lds_w32(lds + CNT, 0u);
    {
        u32 ph[16];
        feature_map(fr[1][0], fr[1][1], wown, l, ph);
        ldsp q = lds + PH + h * PHS + tok * 80;
#pragma unroll
        for (int i = 0; i < 4; ++i) lds_w128(q + i * 16, u32x4{ph[4 * i], ph[4 * i + 1], ph[4 * i + 2], ph[4 * i + 3]});
    }
    const u64 tag = make_tag(e);
    if (threadIdx.x == 0) flag_st(ws, WS_FRD + bb * 8, tag);
    const u32 tag16 = (u32)tag & 0xffffu;
    bar();
    {
        const int hh = w >> 1, sg = w & 1;
        f32x4 kv = {0, 0, 0, 0}, z = {0, 0, 0, 0};
        const u32x4 ones = {0x3f803f80u, 0x3f803f80u, 0x3f803f80u, 0x3f803f80u};
        ldsp pa = lds + PH + hh * PHS + (8 * g + (r16 >> 2)) * 80 + sg * 32 + 8 * (l & 3);
        ldsp pb = lds + VV + hh * VS + (8 * g + (r16 >> 2)) * 40 + 8 * (l & 3);
#pragma unroll
        for (int tg = 0; tg < 4; ++tg) {
            u32x4 A = cat(trread(pa + tg * 32 * 80), trread(pa + tg * 32 * 80 + 4 * 80));
            u32x4 B = cat(trread(pb + tg * 32 * 40), trread(pb + tg * 32 * 40 + 4 * 40));
            kv = mfma32(A, B, kv);
            z = mfma32(A, ones, z);
        }
        u32x4 kg = {f2u(kv[0]), f2u(kv[1]), f2u(kv[2]), (pk2(kv[3], 0.f) << 16) | tag16};
        u32x4 zg = {f2u(z[0]), f2u(z[1]), f2u(z[2]), (pk2(z[3], 0.f) << 16) | tag16};
        st128c(rws, kg, WS_KVP + ((cb * 16 + rb_) * 4 + w) * 1024 + l * 16);
        if (r16 == 0) st128c(rws, zg, WS_ZP + ((cb * 16 + rb_) * 4 + w) * 64 + g * 16);
    }
    u32x4 bq[2][2];
    {
        u32 ph[16];
        feature_map(fr[0][0], fr[0][1], wown, l, ph);
#pragma unroll
        for (int sg = 0; sg < 2; ++sg)
#pragma unroll
            for (int q = 0; q < 4; ++q) {
                auto sw = __builtin_amdgcn_permlane32_swap(ph[8 * sg + q], ph[8 * sg + q + 4], false, false);
                bq[0][sg][q] = sw[0]; bq[1][sg][q] = sw[1];
            }
    }
    {
        const u32 soff = l & 16 ? WS_ZP + ((cb * 16 + r16) * 4 + w) * 64 + 60 : WS_KVP + ((cb * 16 + r16) * 4 + w) * 1024 + 1020;
        u32x4 part[16], zp;
#pragma unroll 1
        for (u32 spins = 0;; ++spins) {
            bool ok = true;
            if (spins > 0) ok = (ld32c(rws, soff) & 0xffffu) == tag16;
            if (__builtin_amdgcn_ballot_w64(!ok) == 0) {
                asm volatile("" ::: "memory");
#pragma unroll
                for (int r = 0; r < 16; ++r) part[r] = ld128c(rws, WS_KVP + (cb * 16 * 4 + w) * 1024 + l * 16, r * 4096);
                zp = ld128c(rws, WS_ZP + ((cb * 16 + (l >> 2)) * 4 + w) * 64 + (l & 3) * 16);
                u32 bad = (zp.w ^ tag16) & 0xffffu;
#pragma unroll
                for (int r = 0; r < 16; ++r) bad |= (part[r].w ^ tag16) & 0xffffu;
                if (__builtin_amdgcn_ballot_w64(bad != 0) == 0) break;
            }
            if (spins >= SPIN_CAP) { flag_st(ws, WS_TMO, tag); break; }
            __builtin_amdgcn_s_sleep(1);
        }
        zp.w &= 0xffff0000u;
        float s[4] = {u2f(part[0].x), u2f(part[0].y), u2f(part[0].z), u2f(part[0].w & 0xffff0000u)};
#pragma unroll
        for (int r = 1; r < 16; ++r) {
            s[0] += u2f(part[r].x); s[1] += u2f(part[r].y); s[2] += u2f(part[r].z); s[3] += u2f(part[r].w & 0xffff0000u);
        }
        float zs[4] = {u2f(zp.x), u2f(zp.y), u2f(zp.z), u2f(zp.w)};
#pragma unroll
        for (int q = 0; q < 4; ++q) {
            float x = zs[q];
            x += dpp_mov<0x124>(x);
            x += dpp_mov<0x128>(x);
            zs[q] = x;
        }
        rows_reduce2<false>(zs[0], zs[1]);
        rows_reduce2<false>(zs[2], zs[3]);
        ldsp F = lds + FR + w * 1024;
        lds_w64(F + (r16 + 32 * (g >> 1)) * 16 + 8 * (g & 1), u32x2{pk2(s[0], s[1]), pk2(s[2], s[3])});
        lds_w64(F + (16 + (l >> 2) + 32 * ((l & 3) >> 1)) * 16 + 8 * (l & 1), u32x2{pk2(zs[0], zs[1]), pk2(zs[2], zs[3])});
    }
    bar();
    u32x4 o[2];
#pragma unroll
    for (int hh = 0; hh < 2; ++hh) {
        f32x16 nd;
#pragma unroll
        for (int r = 0; r < 16; ++r) nd[r] = 0.f;
        nd = mfma32x32(lds_r128(lds + FR + (2 * hh) * 1024 + l * 16), bq[hh][0], nd);
        nd = mfma32x32(lds_r128(lds + FR + (2 * hh + 1) * 1024 + l * 16), bq[hh][1], nd);
        const float inv = __builtin_amdgcn_rcpf(rb(rb(nd[8]) + 1e-6f));
#pragma unroll
        for (int q = 0; q < 4; ++q) { u32 n = pk2(nd[2 * q], nd[2 * q + 1]); o[hh][q] = pk2(blo(n) * inv, bhi(n) * inv); }
    }
    {
        u32x4 s0, s1;
#pragma unroll
        for (int q = 0; q < 4; ++q) {
            auto sw = __builtin_amdgcn_permlane16_swap(o[0][q], o[1][q], false, false);
            s0[q] = sw[0]; s1[q] = sw[1];
        }
        const u32 off = WS_OP + (rb_ * 64 + 16 * w4 + (r16 >> 1)) * 2048 + cb * 128 + (r16 & 1) * 64 + (g & 1) * 32 + (g >> 1) * 16;
        st128c(rws, s0, off);
        st128c(rws, s1, off + 8 * 2048);
    }
    publish_counted(ws, WS_FO + rb_ * 256 + cb * 8, tag, lds + CNT, 4, l);
}

DEV void final_stage(const Params& p, ldsp lds, char* ws, u64 tag, int ba, int w, int l) {
    const int t = threadIdx.x, g = l >> 4, r16 = l & 15, cw = w & 3, th = w >> 2;
    const int tt64 = 4 * (ba & 7) + ((ba >> 3) & 3), ct = ba >> 5;
    rsrc_t ru = mkrsrc(p.u, L * D * 2), rws = mkrsrc(ws, WS_USED), rout = mkrsrc(p.out, L * D * 2);
    const int tok0 = tt64 * 64, chl = ct * 64 + 16 * cw + 4 * g, tok = tok0 + 32 * th + r16;
    constexpr int YL = 4096;
    u32x2 uu[2];
#pragma unroll
    for (int tt = 0; tt < 2; ++tt) uu[tt] = ld64(ru, (tok + 16 * tt) * 1024 + chl * 2);
    const u32x2 dsk = *(const GAS u32x2*)(p.dsk + chl);
    if (ba == 0 && w >= 4) {
        const int q = t - 256;
        wait_flags(ws, WS_FUT + (q >> 5) * 256 + (q & 31) * 8, tag);
        wait_flags(ws, WS_FRD + q * 8, tag);
    }
    WAIT_VM(0);
    bar();
    if (ba == 0 && t == 0) flag_st(ws, WS_E, tag);
    u32x4 wf[16];
    {
        const int row = 16 * (g >> 1) + 4 * (g & 1) + (r16 >> 2);
        ldsp q = lds + 16384 + row * 128 + ((cw ^ ((row >> 1) & 3)) * 32) + 8 * (l & 3);
#pragma unroll
        for (int ks = 0; ks < 16; ++ks) wf[ks] = cat(trread(q + ks * 4096), trread(q + ks * 4096 + 1024));
    }
    __builtin_amdgcn_s_setprio(3);
    if (w == 0) {
        const int tg8 = tt64 * 8 + (l & 7), ga = tg8 >> 7, gc = (tg8 >> 5) & 3, gj = tg8 & 31;
        const int gtb = 16 * ga + (gj >> 1), gcg = 2 * gc + (gj & 1);
        wait_flags(ws, l < 32 ? WS_FY + ct * 256 + l * 8 : (l < 48 ? WS_FO + (tok0 >> 7) * 256 + r16 * 8 : WS_FUT + gcg * 256 + gtb * 8), tag);
    }
    bar();
    const u32x4 dws = mkdesc(ws, WS_USED);
    dma16c(dws, lds + YL + w * 1024, WS_YT + (ct * 64 + 8 * w + (l >> 3)) * 4096 + tok0 * 2 + (l & 7) * 16, 0);
    float gate[2];
#pragma unroll
    for (int tt = 0; tt < 2; ++tt) gate[tt] = u2f(ld32c(rws, WS_G + (tok + 16 * tt) * 4));
#pragma unroll
    for (int i = 0; i < 8; ++i) {
        const int pair = 4 * w + (i >> 1), hf = i & 1;
        const int ps = 32 * hf + ((l & 31) ^ ((2 * pair + (l >> 5)) & 15));
        dma16c(dws, lds + 16384 + pair * 2048 + hf * 1024, WS_OP + (tok0 >> 1) * 2048 + (ps >> 2) * 128 + (l >> 5) * 64 + (ps & 3) * 16, pair * 2048);
    }
    u32 ud[2][2];
#pragma unroll
    for (int tt = 0; tt < 2; ++tt) {
        ud[tt][0] = pk2(blo(uu[tt].x) * blo(dsk.x), bhi(uu[tt].x) * bhi(dsk.x));
        ud[tt][1] = pk2(blo(uu[tt].y) * blo(dsk.y), bhi(uu[tt].y) * bhi(dsk.y));
    }
    asm volatile("" : "+v"(gate[0]), "+v"(gate[1]), "+v"(ud[0][0]), "+v"(ud[0][1]), "+v"(ud[1][0]), "+v"(ud[1][1]));
    WAIT_VM(0);
    bar();
    float ga[2][4], g1[2];
#pragma unroll
    for (int tt = 0; tt < 2; ++tt) {
        u32x2 y = trread(lds + YL + (16 * cw + 4 * g + (r16 >> 2)) * 128 + (32 * th + 16 * tt) * 2 + 8 * (l & 3));
        const float gg = gate[tt];
        g1[tt] = rb(1.f - gg);
        u32 a0 = pk2(blo(y.x) + blo(ud[tt][0]), bhi(y.x) + bhi(ud[tt][0])), a1 = pk2(blo(y.y) + blo(ud[tt][1]), bhi(y.y) + bhi(ud[tt][1]));
        ga[tt][0] = gg * blo(a0); ga[tt][1] = gg * bhi(a0); ga[tt][2] = gg * blo(a1); ga[tt][3] = gg * bhi(a1);
    }
    f32x4 acc[2] = {f32x4{0, 0, 0, 0}, f32x4{0, 0, 0, 0}};
    {
        u32 ob[4];
#pragma unroll
        for (int k4 = 0; k4 < 4; ++k4) ob[k4] = 16384 + (16 * th + (r16 >> 1)) * 2048 + (r16 & 1) * 512 + ((((4 * k4) ^ (r16 & 12)) | (g ^ (r16 & 3))) * 16);
#pragma unroll
        for (int ks = 0; ks < 16; ++ks)
#pragma unroll
            for (int tt = 0; tt < 2; ++tt)
                acc[tt] = mfma32(wf[ks], lds_r128(lds + ob[ks & 3] + ((ks >> 2) & 1) * 256 + (ks >> 3) * 1024 + tt * 16384), acc[tt]);
    }
#pragma unroll
    for (int tt = 0; tt < 2; ++tt) {
        u32x2 o = {pk2(__builtin_fmaf(g1[tt], acc[tt][0], ga[tt][0]), __builtin_fmaf(g1[tt], acc[tt][1], ga[tt][1])),
                   pk2(__builtin_fmaf(g1[tt], acc[tt][2], ga[tt][2]), __builtin_fmaf(g1[tt], acc[tt][3], ga[tt][3]))};
        __builtin_amdgcn_raw_buffer_store_b64(o, rout, (tok + 16 * tt) * 1024 + chl * 2, 0, 0);
    }
    asm volatile("s4d_code_end:");
}

__global__ __launch_bounds__(512, 4) void s4d_hedgehog_mix(Params p) {
    extern __shared__ __attribute__((aligned(16))) char smem[];
    ldsp lds = (ldsp)smem;
    char* ws = p.ws;
    const int b = blockIdx.x, l = threadIdx.x & 63, w = uni(threadIdx.x >> 6);
    asm volatile("" :: "s"(p.ws), "s"(p.u), "s"(p.wqkv), "s"(p.ldt), "s"(p.gw));
    const u64 e = flag_ld(ws, WS_E);
    u64 code0;
    u32 code_bytes;
    asm volatile("s_getpc_b64 %0\ns4d_code_begin:\n\ts_mov_b32 %1, s4d_code_end - s4d_code_begin" : "=s"(code0), "=s"(code_bytes));
    if (b >= 256) {
        const u64 tag = role_conv(p, lds, ws, e, code0, code_bytes, b - 256, w, l);
        final_stage(p, lds, ws, tag, b - 256, w, l);
    } else {
        role_attn(p, lds, ws, e, b, w, l);
    }
}

#if defined(__HIP_DEVICE_COMPILE__)
#pragma clang attribute pop
#endif

extern "C" void kernel_launch(void* const* d_in, const int* in_sizes, int n_in, void* d_out, int out_size, void* d_ws, size_t ws_size,
                              hipStream_t stream) {
    Params p;
    p.u = (const u16*)d_in[0]; p.lar = (const u16*)d_in[1]; p.aim = (const u16*)d_in[2]; p.cre = (const u16*)d_in[3];
    p.cim = (const u16*)d_in[4]; p.ldt = (const u16*)d_in[5]; p.dsk = (const u16*)d_in[6]; p.wqkv = (const u16*)d_in[8];
    p.wphi = (const u16*)d_in[9]; p.wo = (const u16*)d_in[10]; p.gw = (const u16*)d_in[11]; p.out = (u16*)d_out; p.ws = (char*)d_ws;
    (void)hipFuncSetAttribute((const void*)s4d_hedgehog_mix, hipFuncAttributeMaxDynamicSharedMemorySize, LDS_BYTES);
    s4d_hedgehog_mix<<<512, 512, LDS_BYTES, stream>>>(p);
}
```
